```python
import torch

def _softmax(x, axis=-1):
    return torch.softmax(x, dim=axis)
def _gelu(x):
    return torch.nn.functional.gelu(x, approximate="tanh")
def _silu(x):
    return torch.nn.functional.silu(x)
def _sigmoid(x):
    return torch.sigmoid(x)
def _layernorm(x, axis=-1, eps=1e-5):
    m = x.mean(dim=axis, keepdim=True)
    v = x.var(dim=axis, keepdim=True, unbiased=False)
    return (x - m) / torch.sqrt(v + eps)
def _scatter_add(tgt, idx, src, axis=0):
    return tgt.clone().index_add_(axis, idx, src)
def _segment_sum(src, seg, n):
    out = torch.zeros((n,) + src.shape[1:], dtype=src.dtype)
    return out.index_add_(0, seg, src)
def _pad(x, pw):
    flat = tuple(v for pair in reversed(pw) for v in pair)
    return torch.nn.functional.pad(x, flat)

kernel_name = 'recurrent_scan__mha_block_cached__mixer_token_mlp__prompt'

def setup_inputs():

    _w = (lambda a: (lambda t: t.to(torch.bfloat16) if t.is_floating_point() else t)(torch.as_tensor(a)))
    def _A_setup():
        B, Sq, D = 1, 2048, 640
        H, Sk, Dh = 32, 2048, 128
        return {
            "x": _w((lambda *s: torch.randn(*s, dtype=torch.bfloat16) * 0.1)(B, Sq, D)),
            "k_cache": _w((lambda *s: torch.randn(*s, dtype=torch.bfloat16) * 0.8)(B, H, Sk, Dh)),
            "v_cache": _w((lambda *s: torch.randn(*s, dtype=torch.bfloat16) * 3.2)(B, H, Sk, Dh)),
            "wq": _w((lambda *s: torch.randn(*s, dtype=torch.bfloat16) * 0.8)(D, H * Dh) * (D ** -0.5)),
            "wo": _w((lambda *s: torch.randn(*s, dtype=torch.bfloat16) * 3.2)(H * Dh, D) * ((H * Dh) ** -0.5)),
        }
    _A_in = _A_setup()

    def _B_setup():
        B, S, D, F = 1, 2048, 640, 1728
        return {
            "x": _w((lambda *s: torch.randn(*s, dtype=torch.bfloat16) * 0.1)(B, S, D)),
            "w1": _w((lambda *s: torch.randn(*s, dtype=torch.bfloat16) * 3.2)(S, F) * (S ** -0.5)),
            "b1": _w((lambda *s: torch.randn(*s, dtype=torch.bfloat16) * 3.2)(F) * 0.02),
            "w2": _w((lambda *s: torch.randn(*s, dtype=torch.bfloat16) * 3.2)(F, S) * (F ** -0.5)),
            "b2": _w((lambda *s: torch.randn(*s, dtype=torch.bfloat16) * 3.2)(S) * 0.02),
        }
    _B_in = _B_setup()

    return {
        "A_x": _A_in["x"],
        "A_k_cache": _A_in["k_cache"],
        "A_v_cache": _A_in["v_cache"],
        "A_wq": _A_in["wq"],
        "A_wo": _A_in["wo"],
        "B_x": _B_in["x"],
        "B_w1": _B_in["w1"],
        "B_b1": _B_in["b1"],
        "B_w2": _B_in["w2"],
        "B_b2": _B_in["b2"],
    }

def reference(A_x, A_k_cache, A_v_cache, A_wq, A_wo, B_x, B_w1, B_b1, B_w2, B_b2):
    def _A_ref(x, k_cache, v_cache, wq, wo):
        B, Sq, D = x.shape
        H, Dh = 32, 128
        q = (lambda x, *d: x.permute(*d))((x @ wq).reshape(B, Sq, H, Dh), 0, 2, 1, 3)
        scores = torch.einsum("bhqd,bhkd->bhqk", q, k_cache) * (Dh ** -0.5)
        p = _softmax(scores, axis=-1)
        o = torch.einsum("bhqk,bhkd->bhqd", p, v_cache)
        return (lambda x, *d: x.permute(*d))(o, 0, 2, 1, 3).reshape(B, Sq, H * Dh) @ wo

    def _B_ref(x, w1, b1, w2, b2):
        B, S, D = x.shape
        h = _layernorm(x, axis=-1)
        u = torch.einsum("bsd,sf->bdf", h, w1) + b1
        u = _gelu(u)
        y = torch.einsum("bdf,fs->bds", u, w2) + b2
        return (lambda x, *d: x.permute(*d))(y, 0, 2, 1)

    x = A_x
    xn = _layernorm(x, axis=-1)
    s = _A_ref(xn, A_k_cache, A_v_cache, A_wq, A_wo)
    g = _silu(_B_ref(xn, B_w1, B_b1, B_w2, B_b2))
    return x + s * g
```

```cpp
#include <hip/hip_runtime.h>

struct Args {
  const void *x, *k, *v, *wq, *wo, *w1, *b1, *w2, *b2;
  void *out, *ws;
};

__global__ __launch_bounds__(256, 1) void fused_block_kernel(Args a) {
  __shared__ __attribute__((aligned(16))) char lds[163840];
  const unsigned w = __builtin_amdgcn_readfirstlane(threadIdx.x >> 6);
  asm volatile(
"s_load_dwordx16 s[8:23], %0, 0x0\n"
"s_load_dwordx4 s[24:27], %0, 0x40\n"
"s_load_dwordx2 s[28:29], %0, 0x50\n"
"s_mov_b32 s30, %2\n"
"s_mov_b32 s31, %1\n"
"v_mbcnt_lo_u32_b32 v0, -1, 0\n"
"v_mbcnt_hi_u32_b32 v0, -1, v0\n"
"s_and_b32 s33, s31, 7\n"
"s_lshr_b32 s5, s31, 3\n"
"v_lshlrev_b32 v175, 4, v0\n"
"s_and_b32 s70, s33, 7\n"
"s_lshl_b32 s70, s70, 2\n"
"s_and_b32 s71, s5, 3\n"
"s_add_u32 s70, s70, s71\n"
"s_lshr_b32 s71, s33, 3\n"
"s_lshl_b32 s71, s71, 5\n"
"s_add_u32 s71, s71, s5\n"
"s_lshr_b32 s71, s71, 2\n"
"s_lshl_b32 s95, s71, 5\n"
"s_add_u32 s95, s95, s70\n"
"v_lshrrev_b32 v222, 5, v0\n"
"s_lshl_b32 s69, s30, 1\n"
"v_add_u32 v222, s69, v222\n"
"v_and_b32 v223, 31, v0\n"
"v_lshlrev_b32 v223, 3, v223\n"
"v_mul_u32_u24 v1, 1280, v222\n"
"v_add_u32 v1, v1, v223\n"
"s_mul_i32 s69, s95, 10240\n"
"v_mov_b32 v2, 0\n"
"s_waitcnt lgkmcnt(0)\n"
"s_add_u32 s6, s28, 3145728\n"
"s_addc_u32 s7, s29, 0\n"
"s_add_u32 s40, s8, s69\n"
"s_addc_u32 s41, s9, 0\n"
"s_mov_b32 s42, 10240\n"
"s_mov_b32 s43, 0x20000\n"
"global_load_dwordx2 v[220:221], v2, s[6:7] sc1\n"
"buffer_load_dwordx2 v[176:177], v1, s[40:43], 0 offen offset:0\n"
"buffer_load_dwordx2 v[178:179], v1, s[40:43], 0 offen offset:256\n"
"buffer_load_dwordx2 v[180:181], v1, s[40:43], 0 offen offset:512\n"
"buffer_load_dwordx2 v[182:183], v1, s[40:43], 0 offen offset:768\n"
"buffer_load_dwordx2 v[184:185], v1, s[40:43], 0 offen offset:1024\n"
"s_lshl_b32 s69, s70, 8\n"
"s_sub_u32 s61, 5242880, s69\n"
"s_sub_u32 s93, s71, 5\n"
"s_cselect_b32 s93, s71, s93\n"
"s_lshl_b32 s93, s93, 1\n"
"s_lshl_b32 s71, s71, 2\n"
"s_add_u32 s71, s71, s30\n"
"s_mul_i32 s71, s71, 81920\n"
"s_add_u32 s52, s8, s71\n"
"s_addc_u32 s53, s9, 0\n"
"s_mov_b32 s54, 81920\n"
"s_mov_b32 s55, 0x20000\n"
"s_add_u32 s44, s14, s69\n"
"s_addc_u32 s45, s15, 0\n"
"s_mov_b32 s46, s61\n"
"s_mov_b32 s47, 0x20000\n"
"s_lshl_b32 s59, s30, 13\n"
"s_lshl_b32 s63, s93, 7\n"
"s_add_u32 s58, s63, 40960\n"
"v_lshrrev_b32 v1, 3, v0\n"
"v_mul_u32_u24 v1, 1280, v1\n"
"v_and_b32 v3, 7, v0\n"
"v_lshrrev_b32 v2, 4, v0\n"
"v_or_b32 v5, 0, v2\n"
"v_xor_b32 v5, v5, v3\n"
"v_lshl_add_u32 v5, v5, 4, v1\n"
"v_add_u32 v8, 0, v5\n"
"v_or_b32 v5, 4, v2\n"
"v_xor_b32 v5, v5, v3\n"
"v_lshl_add_u32 v5, v5, 4, v1\n"
"v_add_u32 v9, 9216, v5\n"
"v_or_b32 v5, 0, v2\n"
"v_xor_b32 v5, v5, v3\n"
"v_lshl_add_u32 v5, v5, 4, v1\n"
"v_add_u32 v10, 18432, v5\n"
"v_or_b32 v5, 4, v2\n"
"v_xor_b32 v5, v5, v3\n"
"v_lshl_add_u32 v5, v5, 4, v1\n"
"v_add_u32 v11, 27648, v5\n"
"s_lshl_b32 s68, s30, 12\n"
"s_add_u32 s64, s68, 0\n"
"s_add_u32 s65, s68, 65536\n"
"s_lshl_b32 s69, s30, 17\n"
"s_lshl_b32 s62, s93, 19\n"
"s_add_u32 s93, s63, 0\n"
"s_sub_u32 s70, s93, 1280\n"
"s_cselect_b32 s93, s93, s70\n"
"v_and_b32 v1, 15, v0\n"
"v_lshlrev_b32 v3, 1, v2\n"
"v_lshlrev_b32 v6, 13, v2\n"
"v_add_u32 v6, s69, v6\n"
"v_or_b32 v5, 0, v3\n"
"v_xor_b32 v5, v5, v1\n"
"v_lshl_add_u32 v5, v5, 4, v6\n"
"v_add_u32 v171, 0, v5\n"
"v_or_b32 v5, 0, v3\n"
"v_xor_b32 v5, v5, v1\n"
"v_lshl_add_u32 v5, v5, 4, v6\n"
"v_add_u32 v172, 31744, v5\n"
"v_or_b32 v5, 8, v3\n"
"v_xor_b32 v5, v5, v1\n"
"v_lshl_add_u32 v5, v5, 4, v6\n"
"v_add_u32 v173, 63488, v5\n"
"v_or_b32 v5, 8, v3\n"
"v_xor_b32 v5, v5, v1\n"
"v_lshl_add_u32 v5, v5, 4, v6\n"
"v_add_u32 v174, 95232, v5\n"
"s_add_u32 m0, s59, 0\n"
"s_nop 0\n"
"buffer_load_dwordx4 v8, s[52:55], s63 offen offset:0 lds\n"
"buffer_load_dwordx4 v9, s[52:55], s63 offen offset:1024 lds\n"
"buffer_load_dwordx4 v10, s[52:55], s63 offen offset:2048 lds\n"
"buffer_load_dwordx4 v11, s[52:55], s63 offen offset:3072 lds\n"
"s_add_u32 m0, s59, 4096\n"
"s_nop 0\n"
"buffer_load_dwordx4 v8, s[52:55], s58 offen offset:0 lds\n"
"buffer_load_dwordx4 v9, s[52:55], s58 offen offset:1024 lds\n"
"buffer_load_dwordx4 v10, s[52:55], s58 offen offset:2048 lds\n"
"buffer_load_dwordx4 v11, s[52:55], s58 offen offset:3072 lds\n"
"s_add_u32 s63, s63, 128\n"
"s_cmp_eq_u32 s63, 1280\n"
"s_cselect_b32 s63, 0, s63\n"
"s_add_u32 s58, s63, 40960\n"
"s_add_u32 m0, s65, 32768\n"
"s_nop 0\n"
"buffer_load_dwordx4 v171, s[44:47], s62 offen offset:0 lds\n"
"buffer_load_dwordx4 v172, s[44:47], s62 offen offset:1024 lds\n"
"buffer_load_dwordx4 v173, s[44:47], s62 offen offset:2048 lds\n"
"buffer_load_dwordx4 v174, s[44:47], s62 offen offset:3072 lds\n"
"s_and_b32 s70, s33, 7\n"
"s_lshl_b32 s70, s70, 2\n"
"s_and_b32 s71, s5, 3\n"
"s_add_u32 s70, s70, s71\n"
"s_lshr_b32 s71, s33, 3\n"
"s_lshl_b32 s71, s71, 5\n"
"s_add_u32 s71, s71, s5\n"
"s_lshr_b32 s71, s71, 2\n"
"s_lshl_b32 s70, s70, 19\n"
"s_add_u32 s36, s10, s70\n"
"s_addc_u32 s37, s11, 0\n"
"s_mov_b32 s38, 524288\n"
"s_mov_b32 s39, 0x20000\n"
"s_add_u32 s40, s12, s70\n"
"s_addc_u32 s41, s13, 0\n"
"s_mov_b32 s42, 524288\n"
"s_mov_b32 s43, 0x20000\n"
"s_mov_b32 s56, 0\n"
"s_mov_b32 s57, 0\n"
"s_mov_b32 s66, 0\n"
"v_and_b32 v1, 15, v0\n"
"v_lshrrev_b32 v2, 4, v0\n"
"v_lshlrev_b32 v6, 7, v1\n"
"v_add_u32 v6, s59, v6\n"
"v_lshrrev_b32 v5, 1, v1\n"
"v_or_b32 v7, 0, v2\n"
"v_xor_b32 v7, v7, v5\n"
"v_lshl_add_u32 v12, v7, 4, v6\n"
"v_or_b32 v7, 4, v2\n"
"v_xor_b32 v7, v7, v5\n"
"v_lshl_add_u32 v13, v7, 4, v6\n"
"v_bfe_u32 v4, v0, 2, 2\n"
"v_and_b32 v3, 3, v0\n"
"v_lshl_add_u32 v6, v2, 3, v4\n"
"v_lshlrev_b32 v6, 8, v6\n"
"v_add_u32 v6, 65536, v6\n"
"v_and_b32 v5, 1, v2\n"
"v_lshl_or_b32 v5, v5, 2, v4\n"
"v_lshlrev_b32 v5, 1, v5\n"
"v_or_b32 v7, 0, v3\n"
"v_xor_b32 v7, v7, v5\n"
"v_lshl_add_u32 v167, v7, 4, v6\n"
"v_or_b32 v7, 4, v3\n"
"v_xor_b32 v7, v7, v5\n"
"v_lshl_add_u32 v168, v7, 4, v6\n"
"v_or_b32 v7, 8, v3\n"
"v_xor_b32 v7, v7, v5\n"
"v_lshl_add_u32 v169, v7, 4, v6\n"
"v_or_b32 v7, 12, v3\n"
"v_xor_b32 v7, v7, v5\n"
"v_lshl_add_u32 v170, v7, 4, v6\n"
"v_mov_b32 v144, 0x3f803f80\n"
"v_mov_b32 v145, 0x3f803f80\n"
"v_mov_b32 v146, 0x3f803f80\n"
"v_mov_b32 v147, 0x3f803f80\n"
"v_and_b32 v7, 1, v0\n"
"v_mul_u32_u24 v7, 0xaaaa, v7\n"
"v_add_u32 v7, 0x4444, v7\n"
"v_lshl_or_b32 v148, v7, 16, v7\n"
"v_accvgpr_write_b32 a64, 0\n"
"v_accvgpr_write_b32 a65, 0\n"
"v_accvgpr_write_b32 a66, 0\n"
"v_accvgpr_write_b32 a67, 0\n"
"v_accvgpr_write_b32 a68, 0\n"
"v_accvgpr_write_b32 a69, 0\n"
"v_accvgpr_write_b32 a70, 0\n"
"v_accvgpr_write_b32 a71, 0\n"
"v_accvgpr_write_b32 a72, 0\n"
"v_accvgpr_write_b32 a73, 0\n"
"v_accvgpr_write_b32 a74, 0\n"
"v_accvgpr_write_b32 a75, 0\n"
"v_accvgpr_write_b32 a76, 0\n"
"v_accvgpr_write_b32 a77, 0\n"
"v_accvgpr_write_b32 a78, 0\n"
"v_accvgpr_write_b32 a79, 0\n"
"v_accvgpr_write_b32 a80, 0\n"
"v_accvgpr_write_b32 a81, 0\n"
"v_accvgpr_write_b32 a82, 0\n"
"v_accvgpr_write_b32 a83, 0\n"
"v_accvgpr_write_b32 a84, 0\n"
"v_accvgpr_write_b32 a85, 0\n"
"v_accvgpr_write_b32 a86, 0\n"
"v_accvgpr_write_b32 a87, 0\n"
"v_accvgpr_write_b32 a88, 0\n"
"v_accvgpr_write_b32 a89, 0\n"
"v_accvgpr_write_b32 a90, 0\n"
"v_accvgpr_write_b32 a91, 0\n"
"v_accvgpr_write_b32 a92, 0\n"
"v_accvgpr_write_b32 a93, 0\n"
"v_accvgpr_write_b32 a94, 0\n"
"v_accvgpr_write_b32 a95, 0\n"
"s_waitcnt vmcnt(12)\n"
"v_readfirstlane_b32 s34, v220\n"
"v_readfirstlane_b32 s35, v221\n"
"v_lshlrev_b32 v186, 16, v176\n"
"v_and_b32 v187, 0xffff0000, v176\n"
"v_lshlrev_b32 v188, 16, v177\n"
"v_and_b32 v189, 0xffff0000, v177\n"
"v_lshlrev_b32 v190, 16, v178\n"
"v_and_b32 v191, 0xffff0000, v178\n"
"v_lshlrev_b32 v192, 16, v179\n"
"v_and_b32 v193, 0xffff0000, v179\n"
"v_lshlrev_b32 v194, 16, v180\n"
"v_and_b32 v195, 0xffff0000, v180\n"
"v_lshlrev_b32 v196, 16, v181\n"
"v_and_b32 v197, 0xffff0000, v181\n"
"v_lshlrev_b32 v198, 16, v182\n"
"v_and_b32 v199, 0xffff0000, v182\n"
"v_lshlrev_b32 v200, 16, v183\n"
"v_and_b32 v201, 0xffff0000, v183\n"
"v_lshlrev_b32 v202, 16, v184\n"
"v_and_b32 v203, 0xffff0000, v184\n"
"v_lshlrev_b32 v204, 16, v185\n"
"v_and_b32 v205, 0xffff0000, v185\n"
"s_add_u32 s34, s34, 1\n"
"s_addc_u32 s35, s35, 0\n"
"v_pk_add_f32 v[206:207], v[186:187], v[188:189]\n"
"v_pk_add_f32 v[208:209], v[190:191], v[192:193]\n"
"v_pk_add_f32 v[206:207], v[206:207], v[194:195]\n"
"v_pk_add_f32 v[208:209], v[208:209], v[196:197]\n"
"v_pk_add_f32 v[206:207], v[206:207], v[198:199]\n"
"v_pk_add_f32 v[208:209], v[208:209], v[200:201]\n"
"v_pk_add_f32 v[206:207], v[206:207], v[202:203]\n"
"v_pk_add_f32 v[208:209], v[208:209], v[204:205]\n"
"v_pk_add_f32 v[206:207], v[206:207], v[208:209]\n"
"v_add_f32 v206, v206, v207\n"
"s_nop 1\n"
"v_add_f32_dpp v206, v206, v206 row_ror:8 row_mask:0xf bank_mask:0xf\n"
"s_nop 1\n"
"v_add_f32_dpp v206, v206, v206 row_ror:4 row_mask:0xf bank_mask:0xf\n"
"s_nop 1\n"
"v_add_f32_dpp v206, v206, v206 row_ror:2 row_mask:0xf bank_mask:0xf\n"
"s_nop 1\n"
"v_add_f32_dpp v206, v206, v206 row_ror:1 row_mask:0xf bank_mask:0xf\n"
"v_mov_b32 v207, v206\n"
"s_nop 1\n"
"v_permlane16_swap_b32 v206, v207\n"
"v_add_f32 v206, v206, v207\n"
"v_mul_f32 v210, 0xbacccccd, v206\n"
"v_mul_f32 v211, 0xbacccccd, v206\n"
"v_pk_add_f32 v[186:187], v[186:187], v[210:211]\n"
"v_pk_add_f32 v[188:189], v[188:189], v[210:211]\n"
"v_pk_add_f32 v[190:191], v[190:191], v[210:211]\n"
"v_pk_add_f32 v[192:193], v[192:193], v[210:211]\n"
"v_pk_add_f32 v[194:195], v[194:195], v[210:211]\n"
"v_pk_add_f32 v[196:197], v[196:197], v[210:211]\n"
"v_pk_add_f32 v[198:199], v[198:199], v[210:211]\n"
"v_pk_add_f32 v[200:201], v[200:201], v[210:211]\n"
"v_pk_add_f32 v[202:203], v[202:203], v[210:211]\n"
"v_pk_add_f32 v[204:205], v[204:205], v[210:211]\n"
"v_pk_mul_f32 v[206:207], v[186:187], v[186:187]\n"
"v_pk_mul_f32 v[208:209], v[188:189], v[188:189]\n"
"v_pk_fma_f32 v[206:207], v[190:191], v[190:191], v[206:207]\n"
"v_pk_fma_f32 v[208:209], v[192:193], v[192:193], v[208:209]\n"
"v_pk_fma_f32 v[206:207], v[194:195], v[194:195], v[206:207]\n"
"v_pk_fma_f32 v[208:209], v[196:197], v[196:197], v[208:209]\n"
"v_pk_fma_f32 v[206:207], v[198:199], v[198:199], v[206:207]\n"
"v_pk_fma_f32 v[208:209], v[200:201], v[200:201], v[208:209]\n"
"v_pk_fma_f32 v[206:207], v[202:203], v[202:203], v[206:207]\n"
"v_pk_fma_f32 v[208:209], v[204:205], v[204:205], v[208:209]\n"
"v_pk_add_f32 v[206:207], v[206:207], v[208:209]\n"
"v_add_f32 v206, v206, v207\n"
"s_nop 1\n"
"v_add_f32_dpp v206, v206, v206 row_ror:8 row_mask:0xf bank_mask:0xf\n"
"s_nop 1\n"
"v_add_f32_dpp v206, v206, v206 row_ror:4 row_mask:0xf bank_mask:0xf\n"
"s_nop 1\n"
"v_add_f32_dpp v206, v206, v206 row_ror:2 row_mask:0xf bank_mask:0xf\n"
"s_nop 1\n"
"v_add_f32_dpp v206, v206, v206 row_ror:1 row_mask:0xf bank_mask:0xf\n"
"v_mov_b32 v207, v206\n"
"s_nop 1\n"
"v_permlane16_swap_b32 v206, v207\n"
"v_add_f32 v206, v206, v207\n"
"v_mov_b32 v207, 0x3727c5ac\n"
"v_fmac_f32 v207, 0x3acccccd, v206\n"
"v_rsq_f32 v212, v207\n"
"s_nop 0\n"
"v_sub_f32 v216, 0, v210\n"
"v_mov_b32 v217, s34\n"
"v_mul_f32 v218, 0x3e0293ee, v212\n"
"v_mov_b32 v219, s34\n"
"s_lshl_b32 s69, s95, 7\n"
"v_lshlrev_b32 v1, 4, v222\n"
"v_add_u32 v1, s69, v1\n"
"s_mov_b32 exec_lo, 1\n"
"s_mov_b32 exec_hi, 1\n"
"global_store_dwordx4 v1, v[216:219], s[28:29] sc1\n"
"s_mov_b64 exec, -1\n"
"s_add_u32 m0, s59, 32768\n"
"s_nop 0\n"
"buffer_load_dwordx4 v8, s[52:55], s63 offen offset:0 lds\n"
"buffer_load_dwordx4 v9, s[52:55], s63 offen offset:1024 lds\n"
"buffer_load_dwordx4 v10, s[52:55], s63 offen offset:2048 lds\n"
"buffer_load_dwordx4 v11, s[52:55], s63 offen offset:3072 lds\n"
"s_add_u32 m0, s59, 36864\n"
"s_nop 0\n"
"buffer_load_dwordx4 v8, s[52:55], s58 offen offset:0 lds\n"
"buffer_load_dwordx4 v9, s[52:55], s58 offen offset:1024 lds\n"
"buffer_load_dwordx4 v10, s[52:55], s58 offen offset:2048 lds\n"
"buffer_load_dwordx4 v11, s[52:55], s58 offen offset:3072 lds\n"
"s_add_u32 s63, s63, 128\n"
"s_cmp_eq_u32 s63, 1280\n"
"s_cselect_b32 s63, 0, s63\n"
"s_add_u32 s58, s63, 40960\n"
"s_add_u32 s62, s62, 524288\n"
"s_cmp_eq_u32 s62, 5242880\n"
"s_cselect_b32 s62, 0, s62\n"
"s_add_u32 m0, s65, 49152\n"
"s_nop 0\n"
"buffer_load_dwordx4 v171, s[44:47], s62 offen offset:0 lds\n"
"buffer_load_dwordx4 v172, s[44:47], s62 offen offset:1024 lds\n"
"buffer_load_dwordx4 v173, s[44:47], s62 offen offset:2048 lds\n"
"buffer_load_dwordx4 v174, s[44:47], s62 offen offset:3072 lds\n"
"s_waitcnt vmcnt(13)\n"
"ds_read_b128 a[0:3], v12 offset:0\n"
"ds_read_b128 a[4:7], v13 offset:0\n"
"ds_read_b128 a[8:11], v12 offset:2048\n"
"ds_read_b128 a[12:15], v13 offset:2048\n"
"ds_read_b128 a[16:19], v12 offset:4096\n"
"ds_read_b128 a[20:23], v13 offset:4096\n"
"ds_read_b128 a[24:27], v12 offset:6144\n"
"ds_read_b128 a[28:31], v13 offset:6144\n"
"s_barrier\n"
"ds_read_b64_tr_b16 a[128:129], v167 offset:32768\n"
"ds_read_b64_tr_b16 a[130:131], v167 offset:33792\n"
"ds_read_b64_tr_b16 a[132:133], v167 offset:40960\n"
"ds_read_b64_tr_b16 a[134:135], v167 offset:41984\n"
"s_waitcnt lgkmcnt(8)\n"
"ds_read_b64_tr_b16 a[136:137], v167 offset:32776\n"
"ds_read_b64_tr_b16 a[138:139], v167 offset:33800\n"
"ds_read_b64_tr_b16 a[140:141], v167 offset:40968\n"
"ds_read_b64_tr_b16 a[142:143], v167 offset:41992\n"
"s_waitcnt lgkmcnt(4)\n"
"v_smfmac_f32_16x16x64_bf16 a[64:67], v[144:147], a[128:135], v148\n"
"v_mfma_f32_16x16x32_bf16 v[16:19], a[128:131], a[0:3], 0\n"
"ds_read_b64_tr_b16 a[144:145], v168 offset:32768\n"
"v_mfma_f32_16x16x32_bf16 v[16:19], a[132:135], a[4:7], v[16:19]\n"
"ds_read_b64_tr_b16 a[146:147], v168 offset:33792\n"
"v_mfma_f32_16x16x32_bf16 v[20:23], a[128:131], a[8:11], 0\n"
"s_and_b32 s69, s95, 31\n"
"ds_read_b64_tr_b16 a[148:149], v168 offset:40960\n"
"v_mfma_f32_16x16x32_bf16 v[20:23], a[132:135], a[12:15], v[20:23]\n"
"s_add_u32 s62, s62, 524288\n"
"s_cmp_eq_u32 s62, 5242880\n"
"s_cselect_b32 s62, 0, s62\n"
"ds_read_b64_tr_b16 a[150:151], v168 offset:41984\n"
"v_mfma_f32_16x16x32_bf16 v[24:27], a[128:131], a[16:19], 0\n"
"s_add_u32 m0, s65, 0\n"
"v_mfma_f32_16x16x32_bf16 v[24:27], a[132:135], a[20:23], v[24:27]\n"
"s_mul_i32 s69, s69, 5120\n"
"buffer_load_dwordx4 v171, s[44:47], s62 offen offset:0 lds\n"
"v_mfma_f32_16x16x32_bf16 v[28:31], a[128:131], a[24:27], 0\n"
"s_add_u32 s69, s69, 1050624\n"
"v_add_u32 v221, s69, v175\n"
"v_mfma_f32_16x16x32_bf16 v[28:31], a[132:135], a[28:31], v[28:31]\n"
"s_waitcnt lgkmcnt(4)\n"
"buffer_load_dwordx4 v172, s[44:47], s62 offen offset:1024 lds\n"
"v_smfmac_f32_16x16x64_bf16 a[68:71], v[144:147], a[136:143], v148\n"
"s_lshr_b32 s70, s95, 5\n"
"v_mfma_f32_16x16x32_bf16 v[32:35], a[136:139], a[0:3], 0\n"
"s_lshl_b32 s70, s70, 10\n"
"ds_read_b64_tr_b16 a[152:153], v168 offset:32776\n"
"v_mfma_f32_16x16x32_bf16 v[32:35], a[140:143], a[4:7], v[32:35]\n"
"s_sub_u32 s70, s70, 2048\n"
"ds_read_b64_tr_b16 a[154:155], v168 offset:33800\n"
"v_add_u32 v220, s70, v221\n"
"v_mfma_f32_16x16x32_bf16 v[36:39], a[136:139], a[8:11], 0\n"
"ds_read_b64_tr_b16 a[156:157], v168 offset:40968\n"
"v_mul_f32 v186, v186, v212\n"
"v_mfma_f32_16x16x32_bf16 v[36:39], a[140:143], a[12:15], v[36:39]\n"
"ds_read_b64_tr_b16 a[158:159], v168 offset:41992\n"
"v_mul_f32 v187, v187, v212\n"
"v_mfma_f32_16x16x32_bf16 v[40:43], a[136:139], a[16:19], 0\n"
"v_mul_f32 v188, v188, v212\n"
"v_mfma_f32_16x16x32_bf16 v[40:43], a[140:143], a[20:23], v[40:43]\n"
"buffer_load_dwordx4 v173, s[44:47], s62 offen offset:2048 lds\n"
"v_mul_f32 v189, v189, v212\n"
"v_mfma_f32_16x16x32_bf16 v[44:47], a[136:139], a[24:27], 0\n"
"v_mul_f32 v190, v190, v212\n"
"v_mfma_f32_16x16x32_bf16 v[44:47], a[140:143], a[28:31], v[44:47]\n"
"s_waitcnt lgkmcnt(4)\n"
"buffer_load_dwordx4 v174, s[44:47], s62 offen offset:3072 lds\n"
"v_mul_f32 v191, v191, v212\n"
"v_smfmac_f32_16x16x64_bf16 a[72:75], v[144:147], a[144:151], v148\n"
"v_mul_f32 v192, v192, v212\n"
"v_mfma_f32_16x16x32_bf16 v[48:51], a[144:147], a[0:3], 0\n"
"ds_read_b64_tr_b16 a[128:129], v169 offset:32768\n"
"v_mul_f32 v193, v193, v212\n"
"v_mfma_f32_16x16x32_bf16 v[48:51], a[148:151], a[4:7], v[48:51]\n"
"ds_read_b64_tr_b16 a[130:131], v169 offset:33792\n"
"v_mul_f32 v194, v194, v212\n"
"v_mfma_f32_16x16x32_bf16 v[52:55], a[144:147], a[8:11], 0\n"
"ds_read_b64_tr_b16 a[132:133], v169 offset:40960\n"
"v_mul_f32 v195, v195, v212\n"
"v_mfma_f32_16x16x32_bf16 v[52:55], a[148:151], a[12:15], v[52:55]\n"
"ds_read_b64_tr_b16 a[134:135], v169 offset:41984\n"
"v_mul_f32 v196, v196, v212\n"
"v_mfma_f32_16x16x32_bf16 v[56:59], a[144:147], a[16:19], 0\n"
"s_add_u32 m0, s59, 0\n"
"v_mul_f32 v197, v197, v212\n"
"v_mfma_f32_16x16x32_bf16 v[56:59], a[148:151], a[20:23], v[56:59]\n"
"buffer_load_dwordx4 v8, s[52:55], s63 offen offset:0 lds\n"
"v_mul_f32 v198, v198, v212\n"
"v_mfma_f32_16x16x32_bf16 v[60:63], a[144:147], a[24:27], 0\n"
"v_mul_f32 v199, v199, v212\n"
"v_mfma_f32_16x16x32_bf16 v[60:63], a[148:151], a[28:31], v[60:63]\n"
"s_waitcnt lgkmcnt(4)\n"
"buffer_load_dwordx4 v9, s[52:55], s63 offen offset:1024 lds\n"
"v_mul_f32 v200, v200, v212\n"
"v_smfmac_f32_16x16x64_bf16 a[76:79], v[144:147], a[152:159], v148\n"
"v_mul_f32 v201, v201, v212\n"
"v_mfma_f32_16x16x32_bf16 v[64:67], a[152:155], a[0:3], 0\n"
"ds_read_b64_tr_b16 a[136:137], v169 offset:32776\n"
"v_mul_f32 v202, v202, v212\n"
"v_mfma_f32_16x16x32_bf16 v[64:67], a[156:159], a[4:7], v[64:67]\n"
"ds_read_b64_tr_b16 a[138:139], v169 offset:33800\n"
"v_mul_f32 v203, v203, v212\n"
"v_mfma_f32_16x16x32_bf16 v[68:71], a[152:155], a[8:11], 0\n"
"ds_read_b64_tr_b16 a[140:141], v169 offset:40968\n"
"v_mul_f32 v204, v204, v212\n"
"v_mfma_f32_16x16x32_bf16 v[68:71], a[156:159], a[12:15], v[68:71]\n"
"s_waitcnt vmcnt(10)\n"
"ds_read_b64_tr_b16 a[142:143], v169 offset:41992\n"
"v_mul_f32 v205, v205, v212\n"
"v_mfma_f32_16x16x32_bf16 v[72:75], a[152:155], a[16:19], 0\n"
"ds_read_b128 a[32:35], v12 offset:32768\n"
"v_cvt_pk_bf16_f32 v176, v186, v187\n"
"v_mfma_f32_16x16x32_bf16 v[72:75], a[156:159], a[20:23], v[72:75]\n"
"buffer_load_dwordx4 v10, s[52:55], s63 offen offset:2048 lds\n"
"v_cvt_pk_bf16_f32 v177, v188, v189\n"
"v_mfma_f32_16x16x32_bf16 v[76:79], a[152:155], a[24:27], 0\n"
"ds_read_b128 a[36:39], v13 offset:32768\n"
"v_cvt_pk_bf16_f32 v178, v190, v191\n"
"v_mfma_f32_16x16x32_bf16 v[76:79], a[156:159], a[28:31], v[76:79]\n"
"s_waitcnt lgkmcnt(6)\n"
"buffer_load_dwordx4 v11, s[52:55], s63 offen offset:3072 lds\n"
"v_cvt_pk_bf16_f32 v179, v192, v193\n"
"v_smfmac_f32_16x16x64_bf16 a[80:83], v[144:147], a[128:135], v148\n"
"v_cvt_pk_bf16_f32 v180, v194, v195\n"
"v_mfma_f32_16x16x32_bf16 v[80:83], a[128:131], a[0:3], 0\n"
"ds_read_b64_tr_b16 a[144:145], v170 offset:32768\n"
"v_cvt_pk_bf16_f32 v181, v196, v197\n"
"v_mfma_f32_16x16x32_bf16 v[80:83], a[132:135], a[4:7], v[80:83]\n"
"ds_read_b64_tr_b16 a[146:147], v170 offset:33792\n"
"v_cvt_pk_bf16_f32 v182, v198, v199\n"
"v_mfma_f32_16x16x32_bf16 v[84:87], a[128:131], a[8:11], 0\n"
"s_and_b32 s92, s5, 28\n"
"s_add_u32 s92, s92, s30\n"
"s_mul_i32 s92, s92, 163840\n"
"v_lshrrev_b32 v228, 1, v0\n"
"v_min_u32 v228, 19, v228\n"
"v_lshlrev_b32 v228, 13, v228\n"
"v_and_b32 v229, 1, v0\n"
"v_lshl_add_u32 v228, v229, 7, v228\n"
"buffer_load_dword v226, v228, s[44:47], s92 offen\n"
"s_getpc_b64 s[96:97]\n"
"L_pc2_%=:\n"
"s_mov_b32 s98, L_last_%=-L_pc2_%=\n"
"s_mov_b32 s99, 0x20000\n"
"s_lshl_b32 s92, s5, 2\n"
"s_add_u32 s92, s92, s30\n"
"s_lshl_b32 s92, s92, 13\n"
"v_lshlrev_b32 v228, 7, v0\n"
"buffer_load_dword v227, v228, s[96:99], s92 offen\n"
"ds_read_b64_tr_b16 a[148:149], v170 offset:40960\n"
"v_cvt_pk_bf16_f32 v183, v200, v201\n"
"v_mfma_f32_16x16x32_bf16 v[84:87], a[132:135], a[12:15], v[84:87]\n"
"ds_read_b64_tr_b16 a[150:151], v170 offset:41984\n"
"v_cvt_pk_bf16_f32 v184, v202, v203\n"
"v_mfma_f32_16x16x32_bf16 v[88:91], a[128:131], a[16:19], 0\n"
"s_add_u32 m0, s59, 4096\n"
"ds_read_b128 a[40:43], v12 offset:34816\n"
"v_cvt_pk_bf16_f32 v185, v204, v205\n"
"v_mfma_f32_16x16x32_bf16 v[88:91], a[132:135], a[20:23], v[88:91]\n"
"buffer_load_dwordx4 v8, s[52:55], s58 offen offset:0 lds\n"
"v_lshlrev_b32 v186, 16, v176\n"
"v_mfma_f32_16x16x32_bf16 v[92:95], a[128:131], a[24:27], 0\n"
"ds_read_b128 a[44:47], v13 offset:34816\n"
"v_and_b32 v187, 0xffff0000, v176\n"
"v_mfma_f32_16x16x32_bf16 v[92:95], a[132:135], a[28:31], v[92:95]\n"
"s_waitcnt lgkmcnt(8)\n"
"buffer_load_dwordx4 v9, s[52:55], s58 offen offset:1024 lds\n"
"v_lshlrev_b32 v188, 16, v177\n"
"v_smfmac_f32_16x16x64_bf16 a[84:87], v[144:147], a[136:143], v148\n"
"v_and_b32 v189, 0xffff0000, v177\n"
"v_mfma_f32_16x16x32_bf16 v[96:99], a[136:139], a[0:3], 0\n"
"ds_read_b64_tr_b16 a[152:153], v170 offset:32776\n"
"v_lshlrev_b32 v190, 16, v178\n"
"v_mfma_f32_16x16x32_bf16 v[96:99], a[140:143], a[4:7], v[96:99]\n"
"ds_read_b64_tr_b16 a[154:155], v170 offset:33800\n"
"v_and_b32 v191, 0xffff0000, v178\n"
"v_mfma_f32_16x16x32_bf16 v[100:103], a[136:139], a[8:11], 0\n"
"ds_read_b64_tr_b16 a[156:157], v170 offset:40968\n"
"v_lshlrev_b32 v192, 16, v179\n"
"v_mfma_f32_16x16x32_bf16 v[100:103], a[140:143], a[12:15], v[100:103]\n"
"ds_read_b64_tr_b16 a[158:159], v170 offset:41992\n"
"v_and_b32 v193, 0xffff0000, v179\n"
"v_mfma_f32_16x16x32_bf16 v[104:107], a[136:139], a[16:19], 0\n"
"ds_read_b128 a[48:51], v12 offset:36864\n"
"v_lshlrev_b32 v194, 16, v180\n"
"v_mfma_f32_16x16x32_bf16 v[104:107], a[140:143], a[20:23], v[104:107]\n"
"buffer_load_dwordx4 v10, s[52:55], s58 offen offset:2048 lds\n"
"v_and_b32 v195, 0xffff0000, v180\n"
"v_mfma_f32_16x16x32_bf16 v[108:111], a[136:139], a[24:27], 0\n"
"ds_read_b128 a[52:55], v13 offset:36864\n"
"v_lshlrev_b32 v196, 16, v181\n"
"v_mfma_f32_16x16x32_bf16 v[108:111], a[140:143], a[28:31], v[108:111]\n"
"s_waitcnt vmcnt(13) lgkmcnt(8)\n"
"buffer_load_dwordx4 v11, s[52:55], s58 offen offset:3072 lds\n"
"v_and_b32 v197, 0xffff0000, v181\n"
"v_smfmac_f32_16x16x64_bf16 a[88:91], v[144:147], a[144:151], v148\n"
"v_lshlrev_b32 v198, 16, v182\n"
"v_mfma_f32_16x16x32_bf16 v[112:115], a[144:147], a[0:3], 0\n"
"s_barrier\n"
"ds_read_b64_tr_b16 a[128:129], v167 offset:49152\n"
"v_and_b32 v199, 0xffff0000, v182\n"
"v_mfma_f32_16x16x32_bf16 v[112:115], a[148:151], a[4:7], v[112:115]\n"
"s_add_u32 s63, s63, 128\n"
"s_cmp_eq_u32 s63, 1280\n"
"s_cselect_b32 s63, 0, s63\n"
"s_add_u32 s58, s63, 40960\n"
"ds_read_b64_tr_b16 a[130:131], v167 offset:50176\n"
"v_lshlrev_b32 v200, 16, v183\n"
"v_mfma_f32_16x16x32_bf16 v[116:119], a[144:147], a[8:11], 0\n"
"ds_read_b64_tr_b16 a[132:133], v167 offset:57344\n"
"v_and_b32 v201, 0xffff0000, v183\n"
"v_mfma_f32_16x16x32_bf16 v[116:119], a[148:151], a[12:15], v[116:119]\n"
"s_add_u32 m0, s65, 16384\n"
"ds_read_b64_tr_b16 a[134:135], v167 offset:58368\n"
"v_lshlrev_b32 v202, 16, v184\n"
"v_mfma_f32_16x16x32_bf16 v[120:123], a[144:147], a[16:19], 0\n"
"s_add_u32 s62, s62, 524288\n"
"s_cmp_eq_u32 s62, 5242880\n"
"s_cselect_b32 s62, 0, s62\n"
"ds_read_b128 a[56:59], v12 offset:38912\n"
"v_and_b32 v203, 0xffff0000, v184\n"
"v_mfma_f32_16x16x32_bf16 v[120:123], a[148:151], a[20:23], v[120:123]\n"
"buffer_load_dwordx4 v171, s[44:47], s62 offen offset:0 lds\n"
"v_lshlrev_b32 v204, 16, v185\n"
"v_mfma_f32_16x16x32_bf16 v[124:127], a[144:147], a[24:27], 0\n"
"ds_read_b128 a[60:63], v13 offset:38912\n"
"v_and_b32 v205, 0xffff0000, v185\n"
"v_mfma_f32_16x16x32_bf16 v[124:127], a[148:151], a[28:31], v[124:127]\n"
"s_waitcnt lgkmcnt(8)\n"
"buffer_load_dwordx4 v172, s[44:47], s62 offen offset:1024 lds\n"
"v_add_f32 v206, v186, v188\n"
"v_smfmac_f32_16x16x64_bf16 a[92:95], v[144:147], a[152:159], v148\n"
"v_add_f32 v207, v187, v189\n"
"v_mfma_f32_16x16x32_bf16 v[128:131], a[152:155], a[0:3], 0\n"
"ds_read_b64_tr_b16 a[136:137], v167 offset:49160\n"
"v_add_f32 v206, v206, v190\n"
"v_mfma_f32_16x16x32_bf16 v[128:131], a[156:159], a[4:7], v[128:131]\n"
"ds_read_b64_tr_b16 a[138:139], v167 offset:50184\n"
"v_add_f32 v207, v207, v191\n"
"v_mfma_f32_16x16x32_bf16 v[132:135], a[152:155], a[8:11], 0\n"
"ds_read_b64_tr_b16 a[140:141], v167 offset:57352\n"
"v_add_f32 v206, v206, v192\n"
"v_mfma_f32_16x16x32_bf16 v[132:135], a[156:159], a[12:15], v[132:135]\n"
"ds_read_b64_tr_b16 a[142:143], v167 offset:58376\n"
"v_add_f32 v207, v207, v193\n"
"v_mfma_f32_16x16x32_bf16 v[136:139], a[152:155], a[16:19], 0\n"
"v_add_f32 v206, v206, v194\n"
"v_mfma_f32_16x16x32_bf16 v[136:139], a[156:159], a[20:23], v[136:139]\n"
"buffer_load_dwordx4 v173, s[44:47], s62 offen offset:2048 lds\n"
"v_add_f32 v207, v207, v195\n"
"v_mfma_f32_16x16x32_bf16 v[140:143], a[152:155], a[24:27], 0\n"
"v_add_f32 v206, v206, v196\n"
"v_mfma_f32_16x16x32_bf16 v[140:143], a[156:159], a[28:31], v[140:143]\n"
"s_waitcnt lgkmcnt(4)\n"
"buffer_load_dwordx4 v174, s[44:47], s62 offen offset:3072 lds\n"
"v_add_f32 v207, v207, v197\n"
"v_smfmac_f32_16x16x64_bf16 a[64:67], v[144:147], a[128:135], v148\n"
"v_add_f32 v206, v206, v198\n"
"v_mfma_f32_16x16x32_bf16 v[16:19], a[128:131], a[32:35], v[16:19]\n"
"ds_read_b64_tr_b16 a[144:145], v168 offset:49152\n"
"v_add_f32 v207, v207, v199\n"
"v_mfma_f32_16x16x32_bf16 v[16:19], a[132:135], a[36:39], v[16:19]\n"
"ds_read_b64_tr_b16 a[146:147], v168 offset:50176\n"
"v_add_f32 v206, v206, v200\n"
"v_mfma_f32_16x16x32_bf16 v[20:23], a[128:131], a[40:43], v[20:23]\n"
"ds_read_b64_tr_b16 a[148:149], v168 offset:57344\n"
"v_add_f32 v207, v207, v201\n"
"v_mfma_f32_16x16x32_bf16 v[20:23], a[132:135], a[44:47], v[20:23]\n"
"ds_read_b64_tr_b16 a[150:151], v168 offset:58368\n"
"v_add_f32 v206, v206, v202\n"
"v_mfma_f32_16x16x32_bf16 v[24:27], a[128:131], a[48:51], v[24:27]\n"
"s_add_u32 m0, s59, 32768\n"
"v_add_f32 v207, v207, v203\n"
"v_mfma_f32_16x16x32_bf16 v[24:27], a[132:135], a[52:55], v[24:27]\n"
"buffer_load_dwordx4 v8, s[52:55], s63 offen offset:0 lds\n"
"v_add_f32 v206, v206, v204\n"
"v_mfma_f32_16x16x32_bf16 v[28:31], a[128:131], a[56:59], v[28:31]\n"
"v_add_f32 v207, v207, v205\n"
"v_mfma_f32_16x16x32_bf16 v[28:31], a[132:135], a[60:63], v[28:31]\n"
"s_waitcnt lgkmcnt(4)\n"
"buffer_load_dwordx4 v9, s[52:55], s63 offen offset:1024 lds\n"
"v_add_f32 v206, v206, v207\n"
"v_smfmac_f32_16x16x64_bf16 a[68:71], v[144:147], a[136:143], v148\n"
"s_nop 1\n"
"v_add_f32_dpp v206, v206, v206 row_ror:8 row_mask:0xf bank_mask:0xf\n"
"v_mfma_f32_16x16x32_bf16 v[32:35], a[136:139], a[32:35], v[32:35]\n"
"s_nop 1\n"
"ds_read_b64_tr_b16 a[152:153], v168 offset:49160\n"
"v_add_f32_dpp v206, v206, v206 row_ror:4 row_mask:0xf bank_mask:0xf\n"
"v_mfma_f32_16x16x32_bf16 v[32:35], a[140:143], a[36:39], v[32:35]\n"
"s_nop 1\n"
"ds_read_b64_tr_b16 a[154:155], v168 offset:50184\n"
"v_add_f32_dpp v206, v206, v206 row_ror:2 row_mask:0xf bank_mask:0xf\n"
"v_mfma_f32_16x16x32_bf16 v[36:39], a[136:139], a[40:43], v[36:39]\n"
"s_nop 1\n"
"ds_read_b64_tr_b16 a[156:157], v168 offset:57352\n"
"v_add_f32_dpp v206, v206, v206 row_ror:1 row_mask:0xf bank_mask:0xf\n"
"v_mfma_f32_16x16x32_bf16 v[36:39], a[140:143], a[44:47], v[36:39]\n"
"ds_read_b64_tr_b16 a[158:159], v168 offset:58376\n"
"v_mov_b32 v207, v206\n"
"v_mfma_f32_16x16x32_bf16 v[40:43], a[136:139], a[48:51], v[40:43]\n"
"s_nop 1\n"
"v_permlane16_swap_b32 v206, v207\n"
"v_mfma_f32_16x16x32_bf16 v[40:43], a[140:143], a[52:55], v[40:43]\n"
"buffer_load_dwordx4 v10, s[52:55], s63 offen offset:2048 lds\n"
"v_add_f32 v206, v206, v207\n"
"v_mfma_f32_16x16x32_bf16 v[44:47], a[136:139], a[56:59], v[44:47]\n"
"v_mul_f32 v210, 0xbacccccd, v206\n"
"v_mfma_f32_16x16x32_bf16 v[44:47], a[140:143], a[60:63], v[44:47]\n"
"s_waitcnt lgkmcnt(4)\n"
"buffer_load_dwordx4 v11, s[52:55], s63 offen offset:3072 lds\n"
"v_add_f32 v186, v186, v210\n"
"v_smfmac_f32_16x16x64_bf16 a[72:75], v[144:147], a[144:151], v148\n"
"v_add_f32 v187, v187, v210\n"
"v_mfma_f32_16x16x32_bf16 v[48:51], a[144:147], a[32:35], v[48:51]\n"
"ds_read_b64_tr_b16 a[128:129], v169 offset:49152\n"
"v_add_f32 v188, v188, v210\n"
"v_mfma_f32_16x16x32_bf16 v[48:51], a[148:151], a[36:39], v[48:51]\n"
"ds_read_b64_tr_b16 a[130:131], v169 offset:50176\n"
"v_add_f32 v189, v189, v210\n"
"v_mfma_f32_16x16x32_bf16 v[52:55], a[144:147], a[40:43], v[52:55]\n"
"ds_read_b64_tr_b16 a[132:133], v169 offset:57344\n"
"v_add_f32 v190, v190, v210\n"
"v_mfma_f32_16x16x32_bf16 v[52:55], a[148:151], a[44:47], v[52:55]\n"
"ds_read_b64_tr_b16 a[134:135], v169 offset:58368\n"
"v_add_f32 v191, v191, v210\n"
"v_mfma_f32_16x16x32_bf16 v[56:59], a[144:147], a[48:51], v[56:59]\n"
"s_add_u32 m0, s59, 36864\n"
"v_add_f32 v192, v192, v210\n"
"v_mfma_f32_16x16x32_bf16 v[56:59], a[148:151], a[52:55], v[56:59]\n"
"buffer_load_dwordx4 v8, s[52:55], s58 offen offset:0 lds\n"
"v_add_f32 v193, v193, v210\n"
"v_mfma_f32_16x16x32_bf16 v[60:63], a[144:147], a[56:59], v[60:63]\n"
"v_add_f32 v194, v194, v210\n"
"v_mfma_f32_16x16x32_bf16 v[60:63], a[148:151], a[60:63], v[60:63]\n"
"s_waitcnt lgkmcnt(4)\n"
"buffer_load_dwordx4 v9, s[52:55], s58 offen offset:1024 lds\n"
"v_add_f32 v195, v195, v210\n"
"v_smfmac_f32_16x16x64_bf16 a[76:79], v[144:147], a[152:159], v148\n"
"v_add_f32 v196, v196, v210\n"
"v_mfma_f32_16x16x32_bf16 v[64:67], a[152:155], a[32:35], v[64:67]\n"
"ds_read_b64_tr_b16 a[136:137], v169 offset:49160\n"
"v_add_f32 v197, v197, v210\n"
"v_mfma_f32_16x16x32_bf16 v[64:67], a[156:159], a[36:39], v[64:67]\n"
"ds_read_b64_tr_b16 a[138:139], v169 offset:50184\n"
"v_add_f32 v198, v198, v210\n"
"v_mfma_f32_16x16x32_bf16 v[68:71], a[152:155], a[40:43], v[68:71]\n"
"ds_read_b64_tr_b16 a[140:141], v169 offset:57352\n"
"v_add_f32 v199, v199, v210\n"
"v_mfma_f32_16x16x32_bf16 v[68:71], a[156:159], a[44:47], v[68:71]\n"
"s_waitcnt vmcnt(10)\n"
"ds_read_b64_tr_b16 a[142:143], v169 offset:58376\n"
"v_add_f32 v200, v200, v210\n"
"v_mfma_f32_16x16x32_bf16 v[72:75], a[152:155], a[48:51], v[72:75]\n"
"ds_read_b128 a[0:3], v12 offset:0\n"
"v_add_f32 v201, v201, v210\n"
"v_mfma_f32_16x16x32_bf16 v[72:75], a[156:159], a[52:55], v[72:75]\n"
"buffer_load_dwordx4 v10, s[52:55], s58 offen offset:2048 lds\n"
"v_add_f32 v202, v202, v210\n"
"v_mfma_f32_16x16x32_bf16 v[76:79], a[152:155], a[56:59], v[76:79]\n"
"ds_read_b128 a[4:7], v13 offset:0\n"
"v_add_f32 v203, v203, v210\n"
"v_mfma_f32_16x16x32_bf16 v[76:79], a[156:159], a[60:63], v[76:79]\n"
"s_waitcnt lgkmcnt(6)\n"
"buffer_load_dwordx4 v11, s[52:55], s58 offen offset:3072 lds\n"
"v_add_f32 v204, v204, v210\n"
"v_smfmac_f32_16x16x64_bf16 a[80:83], v[144:147], a[128:135], v148\n"
"v_add_f32 v205, v205, v210\n"
"v_mfma_f32_16x16x32_bf16 v[80:83], a[128:131], a[32:35], v[80:83]\n"
"ds_read_b64_tr_b16 a[144:145], v170 offset:49152\n"
"v_mul_f32 v206, v186, v186\n"
"v_mfma_f32_16x16x32_bf16 v[80:83], a[132:135], a[36:39], v[80:83]\n"
"ds_read_b64_tr_b16 a[146:147], v170 offset:50176\n"
"v_mul_f32 v207, v187, v187\n"
"v_mfma_f32_16x16x32_bf16 v[84:87], a[128:131], a[40:43], v[84:87]\n"
"ds_read_b64_tr_b16 a[148:149], v170 offset:57344\n"
"v_fmac_f32 v206, v188, v188\n"
"v_mfma_f32_16x16x32_bf16 v[84:87], a[132:135], a[44:47], v[84:87]\n"
"ds_read_b64_tr_b16 a[150:151], v170 offset:58368\n"
"v_fmac_f32 v207, v189, v189\n"
"v_mfma_f32_16x16x32_bf16 v[88:91], a[128:131], a[48:51], v[88:91]\n"
"ds_read_b128 a[8:11], v12 offset:2048\n"
"v_fmac_f32 v206, v190, v190\n"
"v_mfma_f32_16x16x32_bf16 v[88:91], a[132:135], a[52:55], v[88:91]\n"
"v_fmac_f32 v207, v191, v191\n"
"v_mfma_f32_16x16x32_bf16 v[92:95], a[128:131], a[56:59], v[92:95]\n"
"ds_read_b128 a[12:15], v13 offset:2048\n"
"v_fmac_f32 v206, v192, v192\n"
"v_mfma_f32_16x16x32_bf16 v[92:95], a[132:135], a[60:63], v[92:95]\n"
"s_waitcnt lgkmcnt(8)\n"
"v_fmac_f32 v207, v193, v193\n"
"v_smfmac_f32_16x16x64_bf16 a[84:87], v[144:147], a[136:143], v148\n"
"v_fmac_f32 v206, v194, v194\n"
"v_mfma_f32_16x16x32_bf16 v[96:99], a[136:139], a[32:35], v[96:99]\n"
"ds_read_b64_tr_b16 a[152:153], v170 offset:49160\n"
"v_fmac_f32 v207, v195, v195\n"
"v_mfma_f32_16x16x32_bf16 v[96:99], a[140:143], a[36:39], v[96:99]\n"
"ds_read_b64_tr_b16 a[154:155], v170 offset:50184\n"
"v_fmac_f32 v206, v196, v196\n"
"v_mfma_f32_16x16x32_bf16 v[100:103], a[136:139], a[40:43], v[100:103]\n"
"ds_read_b64_tr_b16 a[156:157], v170 offset:57352\n"
"v_fmac_f32 v207, v197, v197\n"
"v_mfma_f32_16x16x32_bf16 v[100:103], a[140:143], a[44:47], v[100:103]\n"
"ds_read_b64_tr_b16 a[158:159], v170 offset:58376\n"
"v_fmac_f32 v206, v198, v198\n"
"v_mfma_f32_16x16x32_bf16 v[104:107], a[136:139], a[48:51], v[104:107]\n"
"ds_read_b128 a[16:19], v12 offset:4096\n"
"v_fmac_f32 v207, v199, v199\n"
"v_mfma_f32_16x16x32_bf16 v[104:107], a[140:143], a[52:55], v[104:107]\n"
"v_fmac_f32 v206, v200, v200\n"
"v_mfma_f32_16x16x32_bf16 v[108:111], a[136:139], a[56:59], v[108:111]\n"
"ds_read_b128 a[20:23], v13 offset:4096\n"
"v_fmac_f32 v207, v201, v201\n"
"v_mfma_f32_16x16x32_bf16 v[108:111], a[140:143], a[60:63], v[108:111]\n"
"s_waitcnt vmcnt(22) lgkmcnt(8)\n"
"v_fmac_f32 v206, v202, v202\n"
"v_smfmac_f32_16x16x64_bf16 a[88:91], v[144:147], a[144:151], v148\n"
"v_fmac_f32 v207, v203, v203\n"
"v_mfma_f32_16x16x32_bf16 v[112:115], a[144:147], a[32:35], v[112:115]\n"
"s_barrier\n"
"ds_read_b64_tr_b16 a[128:129], v167 offset:0\n"
"v_fmac_f32 v206, v204, v204\n"
"v_mfma_f32_16x16x32_bf16 v[112:115], a[148:151], a[36:39], v[112:115]\n"
"s_add_u32 s63, s63, 128\n"
"s_cmp_eq_u32 s63, 1280\n"
"s_cselect_b32 s63, 0, s63\n"
"s_add_u32 s58, s63, 40960\n"
"ds_read_b64_tr_b16 a[130:131], v167 offset:1024\n"
"v_fmac_f32 v207, v205, v205\n"
"v_mfma_f32_16x16x32_bf16 v[116:119], a[144:147], a[40:43], v[116:119]\n"
"ds_read_b64_tr_b16 a[132:133], v167 offset:8192\n"
"v_add_f32 v206, v206, v207\n"
"v_mfma_f32_16x16x32_bf16 v[116:119], a[148:151], a[44:47], v[116:119]\n"
"s_add_u32 m0, s65, 32768\n"
"ds_read_b64_tr_b16 a[134:135], v167 offset:9216\n"
"v_mfma_f32_16x16x32_bf16 v[120:123], a[144:147], a[48:51], v[120:123]\n"
"s_add_u32 s62, s62, 524288\n"
"s_cmp_eq_u32 s62, 5242880\n"
"s_cselect_b32 s62, 0, s62\n"
"ds_read_b128 a[24:27], v12 offset:6144\n"
"v_mfma_f32_16x16x32_bf16 v[120:123], a[148:151], a[52:55], v[120:123]\n"
"s_nop 1\n"
"buffer_load_dwordx4 v171, s[44:47], s62 offen offset:0 lds\n"
"v_add_f32_dpp v206, v206, v206 row_ror:8 row_mask:0xf bank_mask:0xf\n"
"v_mfma_f32_16x16x32_bf16 v[124:127], a[144:147], a[56:59], v[124:127]\n"
"s_nop 1\n"
"ds_read_b128 a[28:31], v13 offset:6144\n"
"v_add_f32_dpp v206, v206, v206 row_ror:4 row_mask:0xf bank_mask:0xf\n"
"v_mfma_f32_16x16x32_bf16 v[124:127], a[148:151], a[60:63], v[124:127]\n"
"s_waitcnt lgkmcnt(8)\n"
"buffer_load_dwordx4 v172, s[44:47], s62 offen offset:1024 lds\n"
"v_smfmac_f32_16x16x64_bf16 a[92:95], v[144:147], a[152:159], v148\n"
"s_nop 1\n"
"v_add_f32_dpp v206, v206, v206 row_ror:2 row_mask:0xf bank_mask:0xf\n"
"v_mfma_f32_16x16x32_bf16 v[128:131], a[152:155], a[32:35], v[128:131]\n"
"s_nop 1\n"
"ds_read_b64_tr_b16 a[136:137], v167 offset:8\n"
"v_add_f32_dpp v206, v206, v206 row_ror:1 row_mask:0xf bank_mask:0xf\n"
"v_mfma_f32_16x16x32_bf16 v[128:131], a[156:159], a[36:39], v[128:131]\n"
"ds_read_b64_tr_b16 a[138:139], v167 offset:1032\n"
"v_mov_b32 v207, v206\n"
"v_mfma_f32_16x16x32_bf16 v[132:135], a[152:155], a[40:43], v[132:135]\n"
"s_nop 1\n"
"ds_read_b64_tr_b16 a[140:141], v167 offset:8200\n"
"v_permlane16_swap_b32 v206, v207\n"
"v_mfma_f32_16x16x32_bf16 v[132:135], a[156:159], a[44:47], v[132:135]\n"
"ds_read_b64_tr_b16 a[142:143], v167 offset:9224\n"
"v_add_f32 v206, v206, v207\n"
"v_mfma_f32_16x16x32_bf16 v[136:139], a[152:155], a[48:51], v[136:139]\n"
"v_mov_b32 v207, 0x3727c5ac\n"
"v_mfma_f32_16x16x32_bf16 v[136:139], a[156:159], a[52:55], v[136:139]\n"
"buffer_load_dwordx4 v173, s[44:47], s62 offen offset:2048 lds\n"
"v_fmac_f32 v207, 0x3acccccd, v206\n"
"v_mfma_f32_16x16x32_bf16 v[140:143], a[152:155], a[56:59], v[140:143]\n"
"v_rsq_f32 v212, v207\n"
"v_mfma_f32_16x16x32_bf16 v[140:143], a[156:159], a[60:63], v[140:143]\n"
"s_waitcnt lgkmcnt(4)\n"
"buffer_load_dwordx4 v174, s[44:47], s62 offen offset:3072 lds\n"
"v_mfma_f32_16x16x32_bf16 v[16:19], a[128:131], a[0:3], v[16:19]\n"
"s_nop 0\n"
"ds_read_b64_tr_b16 a[144:145], v168 offset:0\n"
"v_mul_f32 v186, v186, v212\n"
"v_mfma_f32_16x16x32_bf16 v[16:19], a[132:135], a[4:7], v[16:19]\n"
"ds_read_b64_tr_b16 a[146:147], v168 offset:1024\n"
"v_mul_f32 v187, v187, v212\n"
"v_mfma_f32_16x16x32_bf16 v[20:23], a[128:131], a[8:11], v[20:23]\n"
"ds_read_b64_tr_b16 a[148:149], v168 offset:8192\n"
"v_mul_f32 v188, v188, v212\n"
"v_mfma_f32_16x16x32_bf16 v[20:23], a[132:135], a[12:15], v[20:23]\n"
"ds_read_b64_tr_b16 a[150:151], v168 offset:9216\n"
"v_mul_f32 v189, v189, v212\n"
"v_mfma_f32_16x16x32_bf16 v[24:27], a[128:131], a[16:19], v[24:27]\n"
"s_add_u32 m0, s59, 0\n"
"v_mul_f32 v190, v190, v212\n"
"v_mfma_f32_16x16x32_bf16 v[24:27], a[132:135], a[20:23], v[24:27]\n"
"buffer_load_dwordx4 v8, s[52:55], s63 offen offset:0 lds\n"
"v_mul_f32 v191, v191, v212\n"
"v_mfma_f32_16x16x32_bf16 v[28:31], a[128:131], a[24:27], v[28:31]\n"
"v_mul_f32 v192, v192, v212\n"
"v_mfma_f32_16x16x32_bf16 v[28:31], a[132:135], a[28:31], v[28:31]\n"
"s_waitcnt lgkmcnt(4)\n"
"buffer_load_dwordx4 v9, s[52:55], s63 offen offset:1024 lds\n"
"v_mul_f32 v193, v193, v212\n"
"v_mfma_f32_16x16x32_bf16 v[32:35], a[136:139], a[0:3], v[32:35]\n"
"ds_read_b64_tr_b16 a[152:153], v168 offset:8\n"
"v_mul_f32 v194, v194, v212\n"
"v_mfma_f32_16x16x32_bf16 v[32:35], a[140:143], a[4:7], v[32:35]\n"
"ds_read_b64_tr_b16 a[154:155], v168 offset:1032\n"
"v_mul_f32 v195, v195, v212\n"
"v_mfma_f32_16x16x32_bf16 v[36:39], a[136:139], a[8:11], v[36:39]\n"
"ds_read_b64_tr_b16 a[156:157], v168 offset:8200\n"
"v_mul_f32 v196, v196, v212\n"
"v_mfma_f32_16x16x32_bf16 v[36:39], a[140:143], a[12:15], v[36:39]\n"
"ds_read_b64_tr_b16 a[158:159], v168 offset:9224\n"
"v_mul_f32 v197, v197, v212\n"
"v_mfma_f32_16x16x32_bf16 v[40:43], a[136:139], a[16:19], v[40:43]\n"
"v_mul_f32 v198, v198, v212\n"
"v_mfma_f32_16x16x32_bf16 v[40:43], a[140:143], a[20:23], v[40:43]\n"
"buffer_load_dwordx4 v10, s[52:55], s63 offen offset:2048 lds\n"
"v_mul_f32 v199, v199, v212\n"
"v_mfma_f32_16x16x32_bf16 v[44:47], a[136:139], a[24:27], v[44:47]\n"
"v_mul_f32 v200, v200, v212\n"
"v_mfma_f32_16x16x32_bf16 v[44:47], a[140:143], a[28:31], v[44:47]\n"
"s_waitcnt lgkmcnt(4)\n"
"buffer_load_dwordx4 v11, s[52:55], s63 offen offset:3072 lds\n"
"v_mul_f32 v201, v201, v212\n"
"v_mfma_f32_16x16x32_bf16 v[48:51], a[144:147], a[0:3], v[48:51]\n"
"ds_read_b64_tr_b16 a[128:129], v169 offset:0\n"
"v_mul_f32 v202, v202, v212\n"
"v_mfma_f32_16x16x32_bf16 v[48:51], a[148:151], a[4:7], v[48:51]\n"
"ds_read_b64_tr_b16 a[130:131], v169 offset:1024\n"
"v_mul_f32 v203, v203, v212\n"
"v_mfma_f32_16x16x32_bf16 v[52:55], a[144:147], a[8:11], v[52:55]\n"
"ds_read_b64_tr_b16 a[132:133], v169 offset:8192\n"
"v_mul_f32 v204, v204, v212\n"
"v_mfma_f32_16x16x32_bf16 v[52:55], a[148:151], a[12:15], v[52:55]\n"
"ds_read_b64_tr_b16 a[134:135], v169 offset:9216\n"
"v_mul_f32 v205, v205, v212\n"
"v_mfma_f32_16x16x32_bf16 v[56:59], a[144:147], a[16:19], v[56:59]\n"
"s_add_u32 m0, s59, 4096\n"
"v_cvt_pk_bf16_f32 v176, v186, v187\n"
"v_mfma_f32_16x16x32_bf16 v[56:59], a[148:151], a[20:23], v[56:59]\n"
"buffer_load_dwordx4 v8, s[52:55], s58 offen offset:0 lds\n"
"v_cvt_pk_bf16_f32 v177, v188, v189\n"
"v_mfma_f32_16x16x32_bf16 v[60:63], a[144:147], a[24:27], v[60:63]\n"
"v_cvt_pk_bf16_f32 v178, v190, v191\n"
"v_mfma_f32_16x16x32_bf16 v[60:63], a[148:151], a[28:31], v[60:63]\n"
"s_waitcnt lgkmcnt(4)\n"
"buffer_load_dwordx4 v9, s[52:55], s58 offen offset:1024 lds\n"
"v_cvt_pk_bf16_f32 v179, v192, v193\n"
"v_mfma_f32_16x16x32_bf16 v[64:67], a[152:155], a[0:3], v[64:67]\n"
"ds_read_b64_tr_b16 a[136:137], v169 offset:8\n"
"v_cvt_pk_bf16_f32 v180, v194, v195\n"
"v_mfma_f32_16x16x32_bf16 v[64:67], a[156:159], a[4:7], v[64:67]\n"
"ds_read_b64_tr_b16 a[138:139], v169 offset:1032\n"
"v_cvt_pk_bf16_f32 v181, v196, v197\n"
"v_mfma_f32_16x16x32_bf16 v[68:71], a[152:155], a[8:11], v[68:71]\n"
"ds_read_b64_tr_b16 a[140:141], v169 offset:8200\n"
"v_cvt_pk_bf16_f32 v182, v198, v199\n"
"v_mfma_f32_16x16x32_bf16 v[68:71], a[156:159], a[12:15], v[68:71]\n"
"s_waitcnt vmcnt(10)\n"
"ds_read_b64_tr_b16 a[142:143], v169 offset:9224\n"
"v_cvt_pk_bf16_f32 v183, v200, v201\n"
"v_mfma_f32_16x16x32_bf16 v[72:75], a[152:155], a[16:19], v[72:75]\n"
"ds_read_b128 a[32:35], v12 offset:32768\n"
"v_cvt_pk_bf16_f32 v184, v202, v203\n"
"v_mfma_f32_16x16x32_bf16 v[72:75], a[156:159], a[20:23], v[72:75]\n"
"buffer_load_dwordx4 v10, s[52:55], s58 offen offset:2048 lds\n"
"v_cvt_pk_bf16_f32 v185, v204, v205\n"
"v_mfma_f32_16x16x32_bf16 v[76:79], a[152:155], a[24:27], v[76:79]\n"
"ds_read_b128 a[36:39], v13 offset:32768\n"
"v_mul_u32_u24 v1, 1296, v222\n"
"v_mfma_f32_16x16x32_bf16 v[76:79], a[156:159], a[28:31], v[76:79]\n"
"s_waitcnt lgkmcnt(6)\n"
"buffer_load_dwordx4 v11, s[52:55], s58 offen offset:3072 lds\n"
"v_add_u32 v1, v1, v223\n"
"v_mfma_f32_16x16x32_bf16 v[80:83], a[128:131], a[0:3], v[80:83]\n"
"ds_read_b64_tr_b16 a[144:145], v170 offset:0\n"
"v_add_u32 v1, 131136, v1\n"
"v_mfma_f32_16x16x32_bf16 v[80:83], a[132:135], a[4:7], v[80:83]\n"
"ds_read_b64_tr_b16 a[146:147], v170 offset:1024\n"
"v_mov_b32 v223, v1\n"
"v_mfma_f32_16x16x32_bf16 v[84:87], a[128:131], a[8:11], v[84:87]\n"
"ds_read_b64_tr_b16 a[148:149], v170 offset:8192\n"
"v_bfe_u32 v1, v0, 2, 2\n"
"v_mfma_f32_16x16x32_bf16 v[84:87], a[132:135], a[12:15], v[84:87]\n"
"ds_read_b64_tr_b16 a[150:151], v170 offset:9216\n"
"v_mul_u32_u24 v1, 1296, v1\n"
"v_mfma_f32_16x16x32_bf16 v[88:91], a[128:131], a[16:19], v[88:91]\n"
"ds_read_b128 a[40:43], v12 offset:34816\n"
"v_lshrrev_b32 v2, 4, v0\n"
"v_mfma_f32_16x16x32_bf16 v[88:91], a[132:135], a[20:23], v[88:91]\n"
"v_lshl_add_u32 v1, v2, 5, v1\n"
"v_mfma_f32_16x16x32_bf16 v[92:95], a[128:131], a[24:27], v[92:95]\n"
"ds_read_b128 a[44:47], v13 offset:34816\n"
"v_and_b32 v3, 3, v0\n"
"v_mfma_f32_16x16x32_bf16 v[92:95], a[132:135], a[28:31], v[92:95]\n"
"s_waitcnt lgkmcnt(8)\n"
"v_lshl_add_u32 v1, v3, 3, v1\n"
"v_mfma_f32_16x16x32_bf16 v[96:99], a[136:139], a[0:3], v[96:99]\n"
"s_lshl_b32 s69, s30, 7\n"
"ds_read_b64_tr_b16 a[152:153], v170 offset:8\n"
"v_mfma_f32_16x16x32_bf16 v[96:99], a[140:143], a[4:7], v[96:99]\n"
"s_add_u32 s69, s69, 131136\n"
"ds_read_b64_tr_b16 a[154:155], v170 offset:1032\n"
"v_add_u32 v240, s69, v1\n"
"v_mfma_f32_16x16x32_bf16 v[100:103], a[136:139], a[8:11], v[100:103]\n"
"s_lshl_b32 s69, s30, 2\n"
"ds_read_b64_tr_b16 a[156:157], v170 offset:8200\n"
"v_add_u32 v2, s69, v2\n"
"v_mfma_f32_16x16x32_bf16 v[100:103], a[140:143], a[12:15], v[100:103]\n"
"ds_read_b64_tr_b16 a[158:159], v170 offset:9224\n"
"v_and_b32 v3, 15, v0\n"
"v_mfma_f32_16x16x32_bf16 v[104:107], a[136:139], a[16:19], v[104:107]\n"
"ds_read_b128 a[48:51], v12 offset:36864\n"
"v_lshlrev_b32 v3, 4, v3\n"
"v_mfma_f32_16x16x32_bf16 v[104:107], a[140:143], a[20:23], v[104:107]\n"
"s_mov_b32 s70, 322560\n"
"v_add_u32 v4, 0, v2\n"
"v_mfma_f32_16x16x32_bf16 v[108:111], a[136:139], a[24:27], v[108:111]\n"
"ds_read_b128 a[52:55], v13 offset:36864\n"
"v_mul_u32_u24 v5, 52, v4\n"
"v_mfma_f32_16x16x32_bf16 v[108:111], a[140:143], a[28:31], v[108:111]\n"
"s_waitcnt vmcnt(20) lgkmcnt(8)\n"
"v_lshrrev_b32 v5, 8, v5\n"
"v_mfma_f32_16x16x32_bf16 v[112:115], a[144:147], a[0:3], v[112:115]\n"
"s_barrier\n"
"ds_read_b64_tr_b16 a[128:129], v167 offset:16384\n"
"v_mul_u32_u24 v5, s70, v5\n"
"v_mfma_f32_16x16x32_bf16 v[112:115], a[148:151], a[4:7], v[112:115]\n"
"s_add_u32 s63, s63, 128\n"
"s_cmp_eq_u32 s63, 1280\n"
"s_cselect_b32 s63, 0, s63\n"
"s_add_u32 s58, s63, 40960\n"
"ds_read_b64_tr_b16 a[130:131], v167 offset:17408\n"
"v_lshl_add_u32 v5, v4, 10, v5\n"
"v_mfma_f32_16x16x32_bf16 v[116:119], a[144:147], a[8:11], v[116:119]\n"
"ds_read_b64_tr_b16 a[132:133], v167 offset:24576\n"
"v_add_u32 v241, v5, v3\n"
"v_mfma_f32_16x16x32_bf16 v[116:119], a[148:151], a[12:15], v[116:119]\n"
"s_add_u32 m0, s65, 49152\n"
"ds_read_b64_tr_b16 a[134:135], v167 offset:25600\n"
"v_add_u32 v4, 16, v2\n"
"v_mfma_f32_16x16x32_bf16 v[120:123], a[144:147], a[16:19], v[120:123]\n"
"s_add_u32 s62, s62, 524288\n"
"s_cmp_eq_u32 s62, 5242880\n"
"s_cselect_b32 s62, 0, s62\n"
"ds_read_b128 a[56:59], v12 offset:38912\n"
"v_mul_u32_u24 v5, 52, v4\n"
"v_mfma_f32_16x16x32_bf16 v[120:123], a[148:151], a[20:23], v[120:123]\n"
"buffer_load_dwordx4 v171, s[44:47], s62 offen offset:0 lds\n"
"v_lshrrev_b32 v5, 8, v5\n"
"v_mfma_f32_16x16x32_bf16 v[124:127], a[144:147], a[24:27], v[124:127]\n"
"ds_read_b128 a[60:63], v13 offset:38912\n"
"v_mul_u32_u24 v5, s70, v5\n"
"v_mfma_f32_16x16x32_bf16 v[124:127], a[148:151], a[28:31], v[124:127]\n"
"s_waitcnt lgkmcnt(8)\n"
"buffer_load_dwordx4 v172, s[44:47], s62 offen offset:1024 lds\n"
"v_lshl_add_u32 v5, v4, 10, v5\n"
"v_mfma_f32_16x16x32_bf16 v[128:131], a[152:155], a[0:3], v[128:131]\n"
"ds_read_b64_tr_b16 a[136:137], v167 offset:16392\n"
"v_add_u32 v242, v5, v3\n"
"v_mfma_f32_16x16x32_bf16 v[128:131], a[156:159], a[4:7], v[128:131]\n"
"ds_read_b64_tr_b16 a[138:139], v167 offset:17416\n"
"v_add_u32 v4, 32, v2\n"
"v_mfma_f32_16x16x32_bf16 v[132:135], a[152:155], a[8:11], v[132:135]\n"
"ds_read_b64_tr_b16 a[140:141], v167 offset:24584\n"
"v_mul_u32_u24 v5, 52, v4\n"
"v_mfma_f32_16x16x32_bf16 v[132:135], a[156:159], a[12:15], v[132:135]\n"
"ds_read_b64_tr_b16 a[142:143], v167 offset:25608\n"
"v_lshrrev_b32 v5, 8, v5\n"
"v_mfma_f32_16x16x32_bf16 v[136:139], a[152:155], a[16:19], v[136:139]\n"
"ds_write_b64 v223, v[176:177] offset:0\n"
"v_mul_u32_u24 v5, s70, v5\n"
"v_mfma_f32_16x16x32_bf16 v[136:139], a[156:159], a[20:23], v[136:139]\n"
"buffer_load_dwordx4 v173, s[44:47], s62 offen offset:2048 lds\n"
"v_lshl_add_u32 v5, v4, 10, v5\n"
"v_mfma_f32_16x16x32_bf16 v[140:143], a[152:155], a[24:27], v[140:143]\n"
"ds_write_b64 v223, v[178:179] offset:256\n"
"v_add_u32 v243, v5, v3\n"
"v_mfma_f32_16x16x32_bf16 v[140:143], a[156:159], a[28:31], v[140:143]\n"
"s_waitcnt lgkmcnt(6)\n"
"buffer_load_dwordx4 v174, s[44:47], s62 offen offset:3072 lds\n"
"v_mfma_f32_16x16x32_bf16 v[16:19], a[128:131], a[32:35], v[16:19]\n"
"s_lshr_b32 s69, s95, 2\n"
"ds_read_b64_tr_b16 a[144:145], v168 offset:16384\n"
"v_mfma_f32_16x16x32_bf16 v[16:19], a[132:135], a[36:39], v[16:19]\n"
"s_mul_i32 s69, s69, 5120\n"
"ds_read_b64_tr_b16 a[146:147], v168 offset:17408\n"
"v_mfma_f32_16x16x32_bf16 v[20:23], a[128:131], a[40:43], v[20:23]\n"
"s_and_b32 s70, s95, 3\n"
"ds_read_b64_tr_b16 a[148:149], v168 offset:24576\n"
"v_mfma_f32_16x16x32_bf16 v[20:23], a[132:135], a[44:47], v[20:23]\n"
"s_lshl_b32 s70, s70, 8\n"
"ds_read_b64_tr_b16 a[150:151], v168 offset:25600\n"
"v_mfma_f32_16x16x32_bf16 v[24:27], a[128:131], a[48:51], v[24:27]\n"
"s_add_u32 m0, s59, 32768\n"
"v_mfma_f32_16x16x32_bf16 v[24:27], a[132:135], a[52:55], v[24:27]\n"
"s_add_u32 s69, s69, s70\n"
"buffer_load_dwordx4 v8, s[52:55], s63 offen offset:0 lds\n"
"v_mfma_f32_16x16x32_bf16 v[28:31], a[128:131], a[56:59], v[28:31]\n"
"s_sub_u32 s70, 2621440, s69\n"
"ds_write_b64 v223, v[180:181] offset:512\n"
"v_mfma_f32_16x16x32_bf16 v[28:31], a[132:135], a[60:63], v[28:31]\n"
"s_waitcnt lgkmcnt(7)\n"
"buffer_load_dwordx4 v9, s[52:55], s63 offen offset:1024 lds\n"
"v_mfma_f32_16x16x32_bf16 v[32:35], a[136:139], a[32:35], v[32:35]\n"
"s_add_u32 s69, s69, 4194304\n"
"ds_read_b64_tr_b16 a[152:153], v168 offset:16392\n"
"v_mfma_f32_16x16x32_bf16 v[32:35], a[140:143], a[36:39], v[32:35]\n"
"s_add_u32 s96, s28, s69\n"
"s_addc_u32 s97, s29, 0\n"
"ds_read_b64_tr_b16 a[154:155], v168 offset:17416\n"
"v_mfma_f32_16x16x32_bf16 v[36:39], a[136:139], a[40:43], v[36:39]\n"
"s_mov_b32 s98, s70\n"
"ds_read_b64_tr_b16 a[156:157], v168 offset:24584\n"
"v_mfma_f32_16x16x32_bf16 v[36:39], a[140:143], a[44:47], v[36:39]\n"
"s_mov_b32 s99, 0x20000\n"
"ds_read_b64_tr_b16 a[158:159], v168 offset:25608\n"
"v_mfma_f32_16x16x32_bf16 v[40:43], a[136:139], a[48:51], v[40:43]\n"
"s_lshr_b32 s69, s95, 5\n"
"ds_write_b64 v223, v[182:183] offset:768\n"
"v_mfma_f32_16x16x32_bf16 v[40:43], a[140:143], a[52:55], v[40:43]\n"
"s_lshl_b32 s69, s69, 2\n"
"buffer_load_dwordx4 v10, s[52:55], s63 offen offset:2048 lds\n"
"v_mfma_f32_16x16x32_bf16 v[44:47], a[136:139], a[56:59], v[44:47]\n"
"s_add_u32 s69, s69, s30\n"
"ds_write_b64 v223, v[184:185] offset:1024\n"
"v_mfma_f32_16x16x32_bf16 v[44:47], a[140:143], a[60:63], v[44:47]\n"
"s_waitcnt lgkmcnt(7)\n"
"buffer_load_dwordx4 v11, s[52:55], s63 offen offset:3072 lds\n"
"v_mfma_f32_16x16x32_bf16 v[48:51], a[144:147], a[32:35], v[48:51]\n"
"s_mul_i32 s70, s69, 1536\n"
"ds_read_b64_tr_b16 a[128:129], v169 offset:16384\n"
"v_mfma_f32_16x16x32_bf16 v[48:51], a[148:151], a[36:39], v[48:51]\n"
"s_lshl_b32 s69, s69, 10\n"
"ds_read_b64_tr_b16 a[130:131], v169 offset:17408\n"
"v_add_u32 v214, s69, v3\n"
"v_mfma_f32_16x16x32_bf16 v[52:55], a[144:147], a[40:43], v[52:55]\n"
"ds_read_b64_tr_b16 a[132:133], v169 offset:24576\n"
"v_min_u32 v215, 11, v0\n"
"v_mfma_f32_16x16x32_bf16 v[52:55], a[148:151], a[44:47], v[52:55]\n"
"ds_read_b64_tr_b16 a[134:135], v169 offset:25600\n"
"v_lshlrev_b32 v215, 7, v215\n"
"v_mfma_f32_16x16x32_bf16 v[56:59], a[144:147], a[48:51], v[56:59]\n"
"s_add_u32 m0, s59, 36864\n"
"buffer_load_dword v215, v215, s[36:39], s70 offen\n"
"v_mfma_f32_16x16x32_bf16 v[56:59], a[148:151], a[52:55], v[56:59]\n"
"buffer_load_dwordx4 v8, s[52:55], s58 offen offset:0 lds\n"
"v_accvgpr_read_b32 v196, a64\n"
"v_mfma_f32_16x16x32_bf16 v[60:63], a[144:147], a[56:59], v[60:63]\n"
"v_accvgpr_read_b32 v197, a65\n"
"v_mfma_f32_16x16x32_bf16 v[60:63], a[148:151], a[60:63], v[60:63]\n"
"s_waitcnt lgkmcnt(6)\n"
"buffer_load_dwordx4 v9, s[52:55], s58 offen offset:1024 lds\n"
"v_add_f32 v196, v196, v197\n"
"v_mfma_f32_16x16x32_bf16 v[64:67], a[152:155], a[32:35], v[64:67]\n"
"ds_read_b64_tr_b16 a[136:137], v169 offset:16392\n"
"v_mov_b32 v197, s34\n"
"v_mfma_f32_16x16x32_bf16 v[64:67], a[156:159], a[36:39], v[64:67]\n"
"ds_read_b64_tr_b16 a[138:139], v169 offset:17416\n"
"v_accvgpr_read_b32 v198, a68\n"
"v_mfma_f32_16x16x32_bf16 v[68:71], a[152:155], a[40:43], v[68:71]\n"
"ds_read_b64_tr_b16 a[140:141], v169 offset:24584\n"
"v_accvgpr_read_b32 v199, a69\n"
"v_mfma_f32_16x16x32_bf16 v[68:71], a[156:159], a[44:47], v[68:71]\n"
"s_waitcnt vmcnt(11)\n"
"ds_read_b64_tr_b16 a[142:143], v169 offset:25608\n"
"v_add_f32 v198, v198, v199\n"
"v_mfma_f32_16x16x32_bf16 v[72:75], a[152:155], a[48:51], v[72:75]\n"
"ds_read_b128 a[0:3], v12 offset:0\n"
"v_mov_b32 v199, s34\n"
"v_mfma_f32_16x16x32_bf16 v[72:75], a[156:159], a[52:55], v[72:75]\n"
"buffer_load_dwordx4 v10, s[52:55], s58 offen offset:2048 lds\n"
"v_accvgpr_read_b32 v200, a72\n"
"v_mfma_f32_16x16x32_bf16 v[76:79], a[152:155], a[56:59], v[76:79]\n"
"ds_read_b128 a[4:7], v13 offset:0\n"
"v_accvgpr_read_b32 v201, a73\n"
"v_mfma_f32_16x16x32_bf16 v[76:79], a[156:159], a[60:63], v[76:79]\n"
"s_waitcnt lgkmcnt(6)\n"
"buffer_load_dwordx4 v11, s[52:55], s58 offen offset:3072 lds\n"
"v_add_f32 v200, v200, v201\n"
"v_mfma_f32_16x16x32_bf16 v[80:83], a[128:131], a[32:35], v[80:83]\n"
"ds_read_b64_tr_b16 a[144:145], v170 offset:16384\n"
"v_mov_b32 v201, s34\n"
"v_mfma_f32_16x16x32_bf16 v[80:83], a[132:135], a[36:39], v[80:83]\n"
"ds_read_b64_tr_b16 a[146:147], v170 offset:17408\n"
"v_accvgpr_read_b32 v202, a76\n"
"v_mfma_f32_16x16x32_bf16 v[84:87], a[128:131], a[40:43], v[84:87]\n"
"ds_read_b64_tr_b16 a[148:149], v170 offset:24576\n"
"v_accvgpr_read_b32 v203, a77\n"
"v_mfma_f32_16x16x32_bf16 v[84:87], a[132:135], a[44:47], v[84:87]\n"
"ds_read_b64_tr_b16 a[150:151], v170 offset:25600\n"
"v_add_f32 v202, v202, v203\n"
"v_mfma_f32_16x16x32_bf16 v[88:91], a[128:131], a[48:51], v[88:91]\n"
"ds_read_b128 a[8:11], v12 offset:2048\n"
"v_mov_b32 v203, s34\n"
"v_mfma_f32_16x16x32_bf16 v[88:91], a[132:135], a[52:55], v[88:91]\n"
"global_load_dwordx4 v[224:227], v214, s[28:29] offset:0 sc1\n"
"v_accvgpr_read_b32 v204, a80\n"
"v_mfma_f32_16x16x32_bf16 v[92:95], a[128:131], a[56:59], v[92:95]\n"
"ds_read_b128 a[12:15], v13 offset:2048\n"
"v_accvgpr_read_b32 v205, a81\n"
"v_mfma_f32_16x16x32_bf16 v[92:95], a[132:135], a[60:63], v[92:95]\n"
"s_waitcnt lgkmcnt(8)\n"
"global_load_dwordx4 v[228:231], v214, s[28:29] offset:256 sc1\n"
"v_add_f32 v204, v204, v205\n"
"v_mfma_f32_16x16x32_bf16 v[96:99], a[136:139], a[32:35], v[96:99]\n"
"ds_read_b64_tr_b16 a[152:153], v170 offset:16392\n"
"v_mov_b32 v205, s34\n"
"v_mfma_f32_16x16x32_bf16 v[96:99], a[140:143], a[36:39], v[96:99]\n"
"ds_read_b64_tr_b16 a[154:155], v170 offset:17416\n"
"v_accvgpr_read_b32 v206, a84\n"
"v_mfma_f32_16x16x32_bf16 v[100:103], a[136:139], a[40:43], v[100:103]\n"
"ds_read_b64_tr_b16 a[156:157], v170 offset:24584\n"
"v_accvgpr_read_b32 v207, a85\n"
"v_mfma_f32_16x16x32_bf16 v[100:103], a[140:143], a[44:47], v[100:103]\n"
"ds_read_b64_tr_b16 a[158:159], v170 offset:25608\n"
"v_add_f32 v206, v206, v207\n"
"v_mfma_f32_16x16x32_bf16 v[104:107], a[136:139], a[48:51], v[104:107]\n"
"ds_read_b128 a[16:19], v12 offset:4096\n"
"v_mov_b32 v207, s34\n"
"v_mfma_f32_16x16x32_bf16 v[104:107], a[140:143], a[52:55], v[104:107]\n"
"global_load_dwordx4 v[232:235], v214, s[28:29] offset:512 sc1\n"
"v_accvgpr_read_b32 v208, a88\n"
"v_mfma_f32_16x16x32_bf16 v[108:111], a[136:139], a[56:59], v[108:111]\n"
"ds_read_b128 a[20:23], v13 offset:4096\n"
"v_accvgpr_read_b32 v209, a89\n"
"v_mfma_f32_16x16x32_bf16 v[108:111], a[140:143], a[60:63], v[108:111]\n"
"s_waitcnt vmcnt(24) lgkmcnt(8)\n"
"global_load_dwordx4 v[236:239], v214, s[28:29] offset:768 sc1\n"
"v_add_f32 v208, v208, v209\n"
"v_mfma_f32_16x16x32_bf16 v[112:115], a[144:147], a[32:35], v[112:115]\n"
"s_barrier\n"
"ds_read_b64_tr_b16 a[128:129], v167 offset:32768\n"
"v_mov_b32 v209, s34\n"
"v_mfma_f32_16x16x32_bf16 v[112:115], a[148:151], a[36:39], v[112:115]\n"
"s_add_u32 s63, s63, 128\n"
"s_cmp_eq_u32 s63, 1280\n"
"s_cselect_b32 s63, 0, s63\n"
"s_add_u32 s58, s63, 40960\n"
"ds_read_b64_tr_b16 a[130:131], v167 offset:33792\n"
"v_accvgpr_read_b32 v210, a92\n"
"v_mfma_f32_16x16x32_bf16 v[116:119], a[144:147], a[40:43], v[116:119]\n"
"ds_read_b64_tr_b16 a[132:133], v167 offset:40960\n"
"v_accvgpr_read_b32 v211, a93\n"
"v_mfma_f32_16x16x32_bf16 v[116:119], a[148:151], a[44:47], v[116:119]\n"
"s_add_u32 m0, s65, 0\n"
"ds_read_b64_tr_b16 a[134:135], v167 offset:41984\n"
"v_add_f32 v210, v210, v211\n"
"v_mfma_f32_16x16x32_bf16 v[120:123], a[144:147], a[48:51], v[120:123]\n"
"s_add_u32 s62, s62, 524288\n"
"s_cmp_eq_u32 s62, 5242880\n"
"s_cselect_b32 s62, 0, s62\n"
"ds_read_b128 a[24:27], v12 offset:6144\n"
"v_mov_b32 v211, s34\n"
"v_mfma_f32_16x16x32_bf16 v[120:123], a[148:151], a[52:55], v[120:123]\n"
"buffer_load_dwordx4 v171, s[44:47], s62 offen offset:0 lds\n"
"v_and_b32 v1, 15, v0\n"
"v_mfma_f32_16x16x32_bf16 v[124:127], a[144:147], a[56:59], v[124:127]\n"
"ds_read_b128 a[28:31], v13 offset:6144\n"
"v_lshrrev_b32 v2, 4, v0\n"
"v_mfma_f32_16x16x32_bf16 v[124:127], a[148:151], a[60:63], v[124:127]\n"
"s_waitcnt lgkmcnt(8)\n"
"buffer_load_dwordx4 v172, s[44:47], s62 offen offset:1024 lds\n"
"v_lshlrev_b32 v6, 8, v2\n"
"v_mfma_f32_16x16x32_bf16 v[128:131], a[152:155], a[32:35], v[128:131]\n"
"ds_read_b64_tr_b16 a[136:137], v167 offset:32776\n"
"v_add_u32 v6, s64, v6\n"
"v_mfma_f32_16x16x32_bf16 v[128:131], a[156:159], a[36:39], v[128:131]\n"
"ds_read_b64_tr_b16 a[138:139], v167 offset:33800\n"
"v_lshlrev_b32 v5, 1, v2\n"
"v_mfma_f32_16x16x32_bf16 v[132:135], a[152:155], a[40:43], v[132:135]\n"
"ds_read_b64_tr_b16 a[140:141], v167 offset:40968\n"
"v_or_b32 v3, 0, v5\n"
"v_mfma_f32_16x16x32_bf16 v[132:135], a[156:159], a[44:47], v[132:135]\n"
"ds_read_b64_tr_b16 a[142:143], v167 offset:41992\n"
"v_xor_b32 v3, v3, v1\n"
"v_mfma_f32_16x16x32_bf16 v[136:139], a[152:155], a[48:51], v[136:139]\n"
"ds_read_b64_tr_b16 v[244:245], v240 offset:0\n"
"v_lshl_add_u32 v165, v3, 4, v6\n"
"v_mfma_f32_16x16x32_bf16 v[136:139], a[156:159], a[52:55], v[136:139]\n"
"buffer_load_dwordx4 v173, s[44:47], s62 offen offset:2048 lds\n"
"v_or_b32 v3, 8, v5\n"
"v_mfma_f32_16x16x32_bf16 v[140:143], a[152:155], a[56:59], v[140:143]\n"
"ds_read_b64_tr_b16 v[246:247], v240 offset:5184\n"
"v_xor_b32 v3, v3, v1\n"
"v_mfma_f32_16x16x32_bf16 v[140:143], a[156:159], a[60:63], v[140:143]\n"
"s_waitcnt lgkmcnt(6)\n"
"buffer_load_dwordx4 v174, s[44:47], s62 offen offset:3072 lds\n"
"v_lshl_add_u32 v166, v3, 4, v6\n"
"v_mfma_f32_16x16x32_bf16 v[16:19], a[128:131], a[0:3], v[16:19]\n"
"ds_read_b64_tr_b16 a[144:145], v168 offset:32768\n"
"v_or_b32 v3, 0, v2\n"
"v_mfma_f32_16x16x32_bf16 v[16:19], a[132:135], a[4:7], v[16:19]\n"
"ds_read_b64_tr_b16 a[146:147], v168 offset:33792\n"
"v_xor_b32 v3, v3, v1\n"
"v_mfma_f32_16x16x32_bf16 v[20:23], a[128:131], a[8:11], v[20:23]\n"
"ds_read_b64_tr_b16 a[148:149], v168 offset:40960\n"
"v_lshl_add_u32 v161, v3, 4, v6\n"
"v_mfma_f32_16x16x32_bf16 v[20:23], a[132:135], a[12:15], v[20:23]\n"
"ds_read_b64_tr_b16 a[150:151], v168 offset:41984\n"
"v_or_b32 v3, 4, v2\n"
"v_mfma_f32_16x16x32_bf16 v[24:27], a[128:131], a[16:19], v[24:27]\n"
"s_add_u32 m0, s59, 0\n"
"ds_read_b64_tr_b16 v[248:249], v240 offset:512\n"
"v_xor_b32 v3, v3, v1\n"
"v_mfma_f32_16x16x32_bf16 v[24:27], a[132:135], a[20:23], v[24:27]\n"
"buffer_load_dwordx4 v8, s[52:55], s63 offen offset:0 lds\n"
"v_lshl_add_u32 v162, v3, 4, v6\n"
"v_mfma_f32_16x16x32_bf16 v[28:31], a[128:131], a[24:27], v[28:31]\n"
"ds_read_b64_tr_b16 v[250:251], v240 offset:5696\n"
"v_or_b32 v3, 8, v2\n"
"v_mfma_f32_16x16x32_bf16 v[28:31], a[132:135], a[28:31], v[28:31]\n"
"s_waitcnt lgkmcnt(8)\n"
"buffer_load_dwordx4 v9, s[52:55], s63 offen offset:1024 lds\n"
"v_xor_b32 v3, v3, v1\n"
"v_mfma_f32_16x16x32_bf16 v[32:35], a[136:139], a[0:3], v[32:35]\n"
"ds_read_b64_tr_b16 a[152:153], v168 offset:32776\n"
"v_lshl_add_u32 v163, v3, 4, v6\n"
"v_mfma_f32_16x16x32_bf16 v[32:35], a[140:143], a[4:7], v[32:35]\n"
"ds_read_b64_tr_b16 a[154:155], v168 offset:33800\n"
"v_or_b32 v3, 12, v2\n"
"v_mfma_f32_16x16x32_bf16 v[36:39], a[136:139], a[8:11], v[36:39]\n"
"s_cmp_lg_u32 s30, 0\n"
"s_cbranch_scc1 L_nocs_%=\n"
"s_cmp_ge_u32 s95, 160\n"
"s_cbranch_scc1 L_nocs_%=\n"
"s_mov_b64 exec, 0xffff\n"
"global_store_dwordx4 v220, v[196:199], s[28:29] offset:0 sc1\n"
"global_store_dwordx4 v220, v[200:203], s[28:29] offset:256 sc1\n"
"global_store_dwordx4 v220, v[204:207], s[28:29] offset:512 sc1\n"
"global_store_dwordx4 v220, v[208:211], s[28:29] offset:768 sc1\n"
"s_mov_b64 exec, -1\n"
"L_nocs_%=:\n"
"ds_read_b64_tr_b16 a[156:157], v168 offset:40968\n"
"v_xor_b32 v3, v3, v1\n"
"v_mfma_f32_16x16x32_bf16 v[36:39], a[140:143], a[12:15], v[36:39]\n"
"ds_read_b64_tr_b16 a[158:159], v168 offset:41992\n"
"v_lshl_add_u32 v164, v3, 4, v6\n"
"v_mfma_f32_16x16x32_bf16 v[40:43], a[136:139], a[16:19], v[40:43]\n"
"ds_read_b64_tr_b16 v[252:253], v240 offset:1024\n"
"v_lshlrev_b32 v6, 8, v1\n"
"v_mfma_f32_16x16x32_bf16 v[40:43], a[140:143], a[20:23], v[40:43]\n"
"buffer_load_dwordx4 v10, s[52:55], s63 offen offset:2048 lds\n"
"v_or_b32 v3, 0, v2\n"
"v_mfma_f32_16x16x32_bf16 v[44:47], a[136:139], a[24:27], v[44:47]\n"
"ds_read_b64_tr_b16 v[254:255], v240 offset:6208\n"
"v_xor_b32 v3, v3, v1\n"
"v_mfma_f32_16x16x32_bf16 v[44:47], a[140:143], a[28:31], v[44:47]\n"
"s_waitcnt lgkmcnt(8)\n"
"buffer_load_dwordx4 v11, s[52:55], s63 offen offset:3072 lds\n"
"v_lshl_add_u32 v149, v3, 4, v6\n"
"v_mfma_f32_16x16x32_bf16 v[48:51], a[144:147], a[0:3], v[48:51]\n"
"ds_read_b64_tr_b16 a[128:129], v169 offset:32768\n"
"v_or_b32 v3, 4, v2\n"
"v_mfma_f32_16x16x32_bf16 v[48:51], a[148:151], a[4:7], v[48:51]\n"
"ds_read_b64_tr_b16 a[130:131], v169 offset:33792\n"
"v_xor_b32 v3, v3, v1\n"
"v_mfma_f32_16x16x32_bf16 v[52:55], a[144:147], a[8:11], v[52:55]\n"
"ds_read_b64_tr_b16 a[132:133], v169 offset:40960\n"
"v_lshl_add_u32 v150, v3, 4, v6\n"
"v_mfma_f32_16x16x32_bf16 v[52:55], a[148:151], a[12:15], v[52:55]\n"
"ds_read_b64_tr_b16 a[134:135], v169 offset:41984\n"
"v_or_b32 v3, 8, v2\n"
"v_mfma_f32_16x16x32_bf16 v[56:59], a[144:147], a[16:19], v[56:59]\n"
"s_add_u32 m0, s59, 4096\n"
"v_xor_b32 v3, v3, v1\n"
"v_mfma_f32_16x16x32_bf16 v[56:59], a[148:151], a[20:23], v[56:59]\n"
"buffer_load_dwordx4 v8, s[52:55], s58 offen offset:0 lds\n"
"v_lshl_add_u32 v151, v3, 4, v6\n"
"v_mfma_f32_16x16x32_bf16 v[60:63], a[144:147], a[24:27], v[60:63]\n"
"v_or_b32 v3, 12, v2\n"
"v_mfma_f32_16x16x32_bf16 v[60:63], a[148:151], a[28:31], v[60:63]\n"
"s_waitcnt lgkmcnt(6)\n"
"buffer_load_dwordx4 v9, s[52:55], s58 offen offset:1024 lds\n"
"v_xor_b32 v3, v3, v1\n"
"v_mfma_f32_16x16x32_bf16 v[64:67], a[152:155], a[0:3], v[64:67]\n"
"ds_read_b64_tr_b16 a[136:137], v169 offset:32776\n"
"v_lshl_add_u32 v152, v3, 4, v6\n"
"v_mfma_f32_16x16x32_bf16 v[64:67], a[156:159], a[4:7], v[64:67]\n"
"ds_read_b64_tr_b16 a[138:139], v169 offset:33800\n"
"v_lshrrev_b32 v2, 2, v0\n"
"v_mfma_f32_16x16x32_bf16 v[68:71], a[152:155], a[8:11], v[68:71]\n"
"ds_read_b64_tr_b16 a[140:141], v169 offset:40968\n"
"v_and_b32 v3, 3, v0\n"
"v_mfma_f32_16x16x32_bf16 v[68:71], a[156:159], a[12:15], v[68:71]\n"
"s_waitcnt vmcnt(14)\n"
"ds_read_b64_tr_b16 a[142:143], v169 offset:41992\n"
"v_and_b32 v5, 7, v2\n"
"v_mfma_f32_16x16x32_bf16 v[72:75], a[152:155], a[16:19], v[72:75]\n"
"ds_read_b128 a[32:35], v12 offset:32768\n"
"v_lshlrev_b32 v6, 8, v2\n"
"v_mfma_f32_16x16x32_bf16 v[72:75], a[156:159], a[20:23], v[72:75]\n"
"buffer_load_dwordx4 v10, s[52:55], s58 offen offset:2048 lds\n"
"v_add_u32 v6, 65536, v6\n"
"v_mfma_f32_16x16x32_bf16 v[76:79], a[152:155], a[24:27], v[76:79]\n"
"ds_read_b128 a[36:39], v13 offset:32768\n"
"v_lshl_add_u32 v6, v3, 3, v6\n"
"v_mfma_f32_16x16x32_bf16 v[76:79], a[156:159], a[28:31], v[76:79]\n"
"s_waitcnt lgkmcnt(6)\n"
"buffer_load_dwordx4 v11, s[52:55], s58 offen offset:3072 lds\n"
"v_xor_b32 v3, 0, v5\n"
"v_mfma_f32_16x16x32_bf16 v[80:83], a[128:131], a[0:3], v[80:83]\n"
"ds_read_b64_tr_b16 a[144:145], v170 offset:32768\n"
"v_lshl_add_u32 v153, v3, 5, v6\n"
"v_mfma_f32_16x16x32_bf16 v[80:83], a[132:135], a[4:7], v[80:83]\n"
"ds_read_b64_tr_b16 a[146:147], v170 offset:33792\n"
"v_xor_b32 v3, 1, v5\n"
"v_mfma_f32_16x16x32_bf16 v[84:87], a[128:131], a[8:11], v[84:87]\n"
"ds_read_b64_tr_b16 a[148:149], v170 offset:40960\n"
"v_lshl_add_u32 v154, v3, 5, v6\n"
"v_mfma_f32_16x16x32_bf16 v[84:87], a[132:135], a[12:15], v[84:87]\n"
"ds_read_b64_tr_b16 a[150:151], v170 offset:41984\n"
"v_xor_b32 v3, 2, v5\n"
"v_mfma_f32_16x16x32_bf16 v[88:91], a[128:131], a[16:19], v[88:91]\n"
"ds_read_b128 a[40:43], v12 offset:34816\n"
"v_lshl_add_u32 v155, v3, 5, v6\n"
"v_mfma_f32_16x16x32_bf16 v[88:91], a[132:135], a[20:23], v[88:91]\n"
"buffer_store_dwordx4 v[244:247], v241, s[96:99], 0 offen sc1\n"
"v_xor_b32 v3, 3, v5\n"
"v_mfma_f32_16x16x32_bf16 v[92:95], a[128:131], a[24:27], v[92:95]\n"
"ds_read_b128 a[44:47], v13 offset:34816\n"
"v_lshl_add_u32 v156, v3, 5, v6\n"
"v_mfma_f32_16x16x32_bf16 v[92:95], a[132:135], a[28:31], v[92:95]\n"
"s_waitcnt lgkmcnt(8)\n"
"v_xor_b32 v3, 4, v5\n"
"v_mfma_f32_16x16x32_bf16 v[96:99], a[136:139], a[0:3], v[96:99]\n"
"ds_read_b64_tr_b16 a[152:153], v170 offset:32776\n"
"v_lshl_add_u32 v157, v3, 5, v6\n"
"v_mfma_f32_16x16x32_bf16 v[96:99], a[140:143], a[4:7], v[96:99]\n"
"ds_read_b64_tr_b16 a[154:155], v170 offset:33800\n"
"v_xor_b32 v3, 5, v5\n"
"v_mfma_f32_16x16x32_bf16 v[100:103], a[136:139], a[8:11], v[100:103]\n"
"ds_read_b64_tr_b16 a[156:157], v170 offset:40968\n"
"v_lshl_add_u32 v158, v3, 5, v6\n"
"v_mfma_f32_16x16x32_bf16 v[100:103], a[140:143], a[12:15], v[100:103]\n"
"ds_read_b64_tr_b16 a[158:159], v170 offset:41992\n"
"v_xor_b32 v3, 6, v5\n"
"v_mfma_f32_16x16x32_bf16 v[104:107], a[136:139], a[16:19], v[104:107]\n"
"ds_read_b128 a[48:51], v12 offset:36864\n"
"v_lshl_add_u32 v159, v3, 5, v6\n"
"v_mfma_f32_16x16x32_bf16 v[104:107], a[140:143], a[20:23], v[104:107]\n"
"buffer_store_dwordx4 v[248:251], v242, s[96:99], 0 offen sc1\n"
"v_xor_b32 v3, 7, v5\n"
"v_mfma_f32_16x16x32_bf16 v[108:111], a[136:139], a[24:27], v[108:111]\n"
"ds_read_b128 a[52:55], v13 offset:36864\n"
"v_lshl_add_u32 v160, v3, 5, v6\n"
"v_mfma_f32_16x16x32_bf16 v[108:111], a[140:143], a[28:31], v[108:111]\n"
"s_waitcnt vmcnt(27) lgkmcnt(8)\n"
"buffer_store_dwordx4 v[252:255], v243, s[96:99], 0 offen sc1\n"
"v_accvgpr_write_b32 a98, 0\n"
"v_mfma_f32_16x16x32_bf16 v[112:115], a[144:147], a[0:3], v[112:115]\n"
"s_barrier\n"
"ds_read_b64_tr_b16 a[128:129], v167 offset:49152\n"
"v_accvgpr_write_b32 a99, 0\n"
"v_mfma_f32_16x16x32_bf16 v[112:115], a[148:151], a[4:7], v[112:115]\n"
"s_add_u32 s63, s63, 128\n"
"s_cmp_eq_u32 s63, 1280\n"
"s_cselect_b32 s63, 0, s63\n"
"s_add_u32 s58, s63, 40960\n"
"ds_read_b64_tr_b16 a[130:131], v167 offset:50176\n"
"v_accvgpr_write_b32 a102, 0\n"
"v_mfma_f32_16x16x32_bf16 v[116:119], a[144:147], a[8:11], v[116:119]\n"
"ds_read_b64_tr_b16 a[132:133], v167 offset:57344\n"
"v_accvgpr_write_b32 a103, 0\n"
"v_mfma_f32_16x16x32_bf16 v[116:119], a[148:151], a[12:15], v[116:119]\n"
"s_add_u32 m0, s65, 16384\n"
"ds_read_b64_tr_b16 a[134:135], v167 offset:58368\n"
"v_accvgpr_write_b32 a106, 0\n"
"v_mfma_f32_16x16x32_bf16 v[120:123], a[144:147], a[16:19], v[120:123]\n"
"s_add_u32 s62, s62, 524288\n"
"s_cmp_eq_u32 s62, 5242880\n"
"s_cselect_b32 s62, 0, s62\n"
"ds_read_b128 a[56:59], v12 offset:38912\n"
"v_accvgpr_write_b32 a107, 0\n"
"v_mfma_f32_16x16x32_bf16 v[120:123], a[148:151], a[20:23], v[120:123]\n"
"buffer_load_dwordx4 v171, s[44:47], s62 offen offset:0 lds\n"
"v_accvgpr_write_b32 a110, 0\n"
"v_mfma_f32_16x16x32_bf16 v[124:127], a[144:147], a[24:27], v[124:127]\n"
"ds_read_b128 a[60:63], v13 offset:38912\n"
"v_accvgpr_write_b32 a111, 0\n"
"v_mfma_f32_16x16x32_bf16 v[124:127], a[148:151], a[28:31], v[124:127]\n"
"s_waitcnt lgkmcnt(8)\n"
"buffer_load_dwordx4 v172, s[44:47], s62 offen offset:1024 lds\n"
"v_accvgpr_write_b32 a162, 0\n"
"v_mfma_f32_16x16x32_bf16 v[128:131], a[152:155], a[0:3], v[128:131]\n"
"ds_read_b64_tr_b16 a[136:137], v167 offset:49160\n"
"v_accvgpr_write_b32 a163, 0\n"
"v_mfma_f32_16x16x32_bf16 v[128:131], a[156:159], a[4:7], v[128:131]\n"
"ds_read_b64_tr_b16 a[138:139], v167 offset:50184\n"
"v_accvgpr_write_b32 a166, 0\n"
"v_mfma_f32_16x16x32_bf16 v[132:135], a[152:155], a[8:11], v[132:135]\n"
"ds_read_b64_tr_b16 a[140:141], v167 offset:57352\n"
"v_accvgpr_write_b32 a167, 0\n"
"v_mfma_f32_16x16x32_bf16 v[132:135], a[156:159], a[12:15], v[132:135]\n"
"ds_read_b64_tr_b16 a[142:143], v167 offset:58376\n"
"v_accvgpr_write_b32 a170, 0\n"
"v_mfma_f32_16x16x32_bf16 v[136:139], a[152:155], a[16:19], v[136:139]\n"
"v_accvgpr_write_b32 a171, 0\n"
"v_mfma_f32_16x16x32_bf16 v[136:139], a[156:159], a[20:23], v[136:139]\n"
"buffer_load_dwordx4 v173, s[44:47], s62 offen offset:2048 lds\n"
"v_accvgpr_write_b32 a174, 0\n"
"v_mfma_f32_16x16x32_bf16 v[140:143], a[152:155], a[24:27], v[140:143]\n"
"v_accvgpr_write_b32 a175, 0\n"
"v_mfma_f32_16x16x32_bf16 v[140:143], a[156:159], a[28:31], v[140:143]\n"
"s_waitcnt lgkmcnt(4)\n"
"buffer_load_dwordx4 v174, s[44:47], s62 offen offset:3072 lds\n"
"v_accvgpr_write_b32 a178, 0\n"
"v_mfma_f32_16x16x32_bf16 v[16:19], a[128:131], a[32:35], v[16:19]\n"
"ds_read_b64_tr_b16 a[144:145], v168 offset:49152\n"
"v_accvgpr_write_b32 a179, 0\n"
"v_mfma_f32_16x16x32_bf16 v[16:19], a[132:135], a[36:39], v[16:19]\n"
"ds_read_b64_tr_b16 a[146:147], v168 offset:50176\n"
"v_accvgpr_write_b32 a182, 0\n"
"v_mfma_f32_16x16x32_bf16 v[20:23], a[128:131], a[40:43], v[20:23]\n"
"ds_read_b64_tr_b16 a[148:149], v168 offset:57344\n"
"v_accvgpr_write_b32 a183, 0\n"
"v_mfma_f32_16x16x32_bf16 v[20:23], a[132:135], a[44:47], v[20:23]\n"
"ds_read_b64_tr_b16 a[150:151], v168 offset:58368\n"
"v_accvgpr_write_b32 a186, 0\n"
"v_mfma_f32_16x16x32_bf16 v[24:27], a[128:131], a[48:51], v[24:27]\n"
"s_add_u32 m0, s59, 32768\n"
"v_accvgpr_write_b32 a187, 0\n"
"v_mfma_f32_16x16x32_bf16 v[24:27], a[132:135], a[52:55], v[24:27]\n"
"buffer_load_dwordx4 v8, s[52:55], s63 offen offset:0 lds\n"
"v_accvgpr_write_b32 a190, 0\n"
"v_mfma_f32_16x16x32_bf16 v[28:31], a[128:131], a[56:59], v[28:31]\n"
"v_accvgpr_write_b32 a191, 0\n"
"v_mfma_f32_16x16x32_bf16 v[28:31], a[132:135], a[60:63], v[28:31]\n"
"s_waitcnt lgkmcnt(4)\n"
"buffer_load_dwordx4 v9, s[52:55], s63 offen offset:1024 lds\n"
"v_mfma_f32_16x16x32_bf16 v[32:35], a[136:139], a[32:35], v[32:35]\n"
"s_lshl_b32 s69, s95, 3\n"
"ds_read_b64_tr_b16 a[152:153], v168 offset:49160\n"
"v_mfma_f32_16x16x32_bf16 v[32:35], a[140:143], a[36:39], v[32:35]\n"
"s_add_u32 s69, s69, 8192\n"
"ds_read_b64_tr_b16 a[154:155], v168 offset:50184\n"
"v_mov_b32 v208, s69\n"
"v_mfma_f32_16x16x32_bf16 v[36:39], a[136:139], a[40:43], v[36:39]\n"
"ds_read_b64_tr_b16 a[156:157], v168 offset:57352\n"
"v_mov_b32 v210, s34\n"
"v_mfma_f32_16x16x32_bf16 v[36:39], a[140:143], a[44:47], v[36:39]\n"
"ds_read_b64_tr_b16 a[158:159], v168 offset:58376\n"
"v_mov_b32 v211, s35\n"
"v_mfma_f32_16x16x32_bf16 v[40:43], a[136:139], a[48:51], v[40:43]\n"
"v_mfma_f32_16x16x32_bf16 v[40:43], a[140:143], a[52:55], v[40:43]\n"
"buffer_load_dwordx4 v10, s[52:55], s63 offen offset:2048 lds\n"
"v_mfma_f32_16x16x32_bf16 v[44:47], a[136:139], a[56:59], v[44:47]\n"
"v_mfma_f32_16x16x32_bf16 v[44:47], a[140:143], a[60:63], v[44:47]\n"
"s_waitcnt lgkmcnt(4)\n"
"buffer_load_dwordx4 v11, s[52:55], s63 offen offset:3072 lds\n"
"v_mfma_f32_16x16x32_bf16 v[48:51], a[144:147], a[32:35], v[48:51]\n"
"ds_read_b64_tr_b16 a[128:129], v169 offset:49152\n"
"v_mfma_f32_16x16x32_bf16 v[48:51], a[148:151], a[36:39], v[48:51]\n"
"ds_read_b64_tr_b16 a[130:131], v169 offset:50176\n"
"v_mfma_f32_16x16x32_bf16 v[52:55], a[144:147], a[40:43], v[52:55]\n"
"ds_read_b64_tr_b16 a[132:133], v169 offset:57344\n"
"v_mfma_f32_16x16x32_bf16 v[52:55], a[148:151], a[44:47], v[52:55]\n"
"ds_read_b64_tr_b16 a[134:135], v169 offset:58368\n"
"v_mfma_f32_16x16x32_bf16 v[56:59], a[144:147], a[48:51], v[56:59]\n"
"s_add_u32 m0, s59, 36864\n"
"v_mfma_f32_16x16x32_bf16 v[56:59], a[148:151], a[52:55], v[56:59]\n"
"buffer_load_dwordx4 v8, s[52:55], s58 offen offset:0 lds\n"
"v_mfma_f32_16x16x32_bf16 v[60:63], a[144:147], a[56:59], v[60:63]\n"
"v_mfma_f32_16x16x32_bf16 v[60:63], a[148:151], a[60:63], v[60:63]\n"
"s_waitcnt lgkmcnt(4)\n"
"buffer_load_dwordx4 v9, s[52:55], s58 offen offset:1024 lds\n"
"v_mfma_f32_16x16x32_bf16 v[64:67], a[152:155], a[32:35], v[64:67]\n"
"ds_read_b64_tr_b16 a[136:137], v169 offset:49160\n"
"v_mfma_f32_16x16x32_bf16 v[64:67], a[156:159], a[36:39], v[64:67]\n"
"ds_read_b64_tr_b16 a[138:139], v169 offset:50184\n"
"v_mfma_f32_16x16x32_bf16 v[68:71], a[152:155], a[40:43], v[68:71]\n"
"ds_read_b64_tr_b16 a[140:141], v169 offset:57352\n"
"v_mfma_f32_16x16x32_bf16 v[68:71], a[156:159], a[44:47], v[68:71]\n"
"s_waitcnt vmcnt(13)\n"
"ds_read_b64_tr_b16 a[142:143], v169 offset:58376\n"
"v_mfma_f32_16x16x32_bf16 v[72:75], a[152:155], a[48:51], v[72:75]\n"
"ds_read_b128 a[0:3], v12 offset:0\n"
"v_mfma_f32_16x16x32_bf16 v[72:75], a[156:159], a[52:55], v[72:75]\n"
"buffer_load_dwordx4 v10, s[52:55], s58 offen offset:2048 lds\n"
"v_mfma_f32_16x16x32_bf16 v[76:79], a[152:155], a[56:59], v[76:79]\n"
"ds_read_b128 a[4:7], v13 offset:0\n"
"v_mfma_f32_16x16x32_bf16 v[76:79], a[156:159], a[60:63], v[76:79]\n"
"s_waitcnt lgkmcnt(6)\n"
"buffer_load_dwordx4 v11, s[52:55], s58 offen offset:3072 lds\n"
"v_mfma_f32_16x16x32_bf16 v[80:83], a[128:131], a[32:35], v[80:83]\n"
"ds_read_b64_tr_b16 a[144:145], v170 offset:49152\n"
"v_mfma_f32_16x16x32_bf16 v[80:83], a[132:135], a[36:39], v[80:83]\n"
"ds_read_b64_tr_b16 a[146:147], v170 offset:50176\n"
"global_load_dwordx4 v[176:179], v221, s[28:29] offset:-2048 sc1\n"
"v_mfma_f32_16x16x32_bf16 v[84:87], a[128:131], a[40:43], v[84:87]\n"
"ds_read_b64_tr_b16 a[148:149], v170 offset:57344\n"
"v_mfma_f32_16x16x32_bf16 v[84:87], a[132:135], a[44:47], v[84:87]\n"
"ds_read_b64_tr_b16 a[150:151], v170 offset:58368\n"
"v_mfma_f32_16x16x32_bf16 v[88:91], a[128:131], a[48:51], v[88:91]\n"
"ds_read_b128 a[8:11], v12 offset:2048\n"
"v_mfma_f32_16x16x32_bf16 v[88:91], a[132:135], a[52:55], v[88:91]\n"
"global_load_dwordx4 v[180:183], v221, s[28:29] offset:-1024 sc1\n"
"v_mfma_f32_16x16x32_bf16 v[92:95], a[128:131], a[56:59], v[92:95]\n"
"ds_read_b128 a[12:15], v13 offset:2048\n"
"v_mfma_f32_16x16x32_bf16 v[92:95], a[132:135], a[60:63], v[92:95]\n"
"s_waitcnt lgkmcnt(8)\n"
"global_load_dwordx4 v[184:187], v221, s[28:29] offset:0 sc1\n"
"v_mfma_f32_16x16x32_bf16 v[96:99], a[136:139], a[32:35], v[96:99]\n"
"ds_read_b64_tr_b16 a[152:153], v170 offset:49160\n"
"v_mfma_f32_16x16x32_bf16 v[96:99], a[140:143], a[36:39], v[96:99]\n"
"ds_read_b64_tr_b16 a[154:155], v170 offset:50184\n"
"v_mfma_f32_16x16x32_bf16 v[100:103], a[136:139], a[40:43], v[100:103]\n"
"ds_read_b64_tr_b16 a[156:157], v170 offset:57352\n"
"v_mfma_f32_16x16x32_bf16 v[100:103], a[140:143], a[44:47], v[100:103]\n"
"ds_read_b64_tr_b16 a[158:159], v170 offset:58376\n"
"v_mfma_f32_16x16x32_bf16 v[104:107], a[136:139], a[48:51], v[104:107]\n"
"ds_read_b128 a[16:19], v12 offset:4096\n"
"v_mfma_f32_16x16x32_bf16 v[104:107], a[140:143], a[52:55], v[104:107]\n"
"global_load_dwordx4 v[188:191], v221, s[28:29] offset:1024 sc1\n"
"v_mfma_f32_16x16x32_bf16 v[108:111], a[136:139], a[56:59], v[108:111]\n"
"ds_read_b128 a[20:23], v13 offset:4096\n"
"v_mfma_f32_16x16x32_bf16 v[108:111], a[140:143], a[60:63], v[108:111]\n"
"s_waitcnt vmcnt(27) lgkmcnt(8)\n"
"global_load_dwordx4 v[192:195], v221, s[28:29] offset:2048 sc1\n"
"v_mfma_f32_16x16x32_bf16 v[112:115], a[144:147], a[32:35], v[112:115]\n"
"s_barrier\n"
"ds_read_b64_tr_b16 a[128:129], v167 offset:0\n"
"v_mfma_f32_16x16x32_bf16 v[112:115], a[148:151], a[36:39], v[112:115]\n"
"s_add_u32 s63, s63, 128\n"
"s_cmp_eq_u32 s63, 1280\n"
"s_cselect_b32 s63, 0, s63\n"
"s_add_u32 s58, s63, 40960\n"
"ds_read_b64_tr_b16 a[130:131], v167 offset:1024\n"
"v_mfma_f32_16x16x32_bf16 v[116:119], a[144:147], a[40:43], v[116:119]\n"
"s_and_b32 s96, s33, 1\n"
"ds_read_b64_tr_b16 a[132:133], v167 offset:8192\n"
"v_mfma_f32_16x16x32_bf16 v[116:119], a[148:151], a[44:47], v[116:119]\n"
"s_add_u32 m0, s65, 32768\n"
"ds_read_b64_tr_b16 a[134:135], v167 offset:9216\n"
"v_mfma_f32_16x16x32_bf16 v[120:123], a[144:147], a[48:51], v[120:123]\n"
"s_add_u32 s62, s62, 524288\n"
"s_cmp_eq_u32 s62, 5242880\n"
"s_cselect_b32 s62, 0, s62\n"
"ds_read_b128 a[24:27], v12 offset:6144\n"
"v_mfma_f32_16x16x32_bf16 v[120:123], a[148:151], a[52:55], v[120:123]\n"
"s_lshl_b32 s96, s96, 2\n"
"buffer_load_dwordx4 v171, s[44:47], s62 offen offset:0 lds\n"
"v_mfma_f32_16x16x32_bf16 v[124:127], a[144:147], a[56:59], v[124:127]\n"
"s_and_b32 s69, s5, 3\n"
"ds_read_b128 a[28:31], v13 offset:6144\n"
"v_mfma_f32_16x16x32_bf16 v[124:127], a[148:151], a[60:63], v[124:127]\n"
"s_waitcnt lgkmcnt(8)\n"
"buffer_load_dwordx4 v172, s[44:47], s62 offen offset:1024 lds\n"
"v_mfma_f32_16x16x32_bf16 v[128:131], a[152:155], a[32:35], v[128:131]\n"
"s_add_u32 s96, s96, s69\n"
"ds_read_b64_tr_b16 a[136:137], v167 offset:8\n"
"v_mfma_f32_16x16x32_bf16 v[128:131], a[156:159], a[36:39], v[128:131]\n"
"s_lshr_b32 s97, s33, 1\n"
"ds_read_b64_tr_b16 a[138:139], v167 offset:1032\n"
"v_mfma_f32_16x16x32_bf16 v[132:135], a[152:155], a[40:43], v[132:135]\n"
"s_mul_i32 s97, s97, 7\n"
"ds_read_b64_tr_b16 a[140:141], v167 offset:8200\n"
"v_mfma_f32_16x16x32_bf16 v[132:135], a[156:159], a[44:47], v[132:135]\n"
"s_lshr_b32 s69, s5, 2\n"
"ds_read_b64_tr_b16 a[142:143], v167 offset:9224\n"
"v_mfma_f32_16x16x32_bf16 v[136:139], a[152:155], a[48:51], v[136:139]\n"
"s_add_u32 s97, s97, s69\n"
"v_mfma_f32_16x16x32_bf16 v[136:139], a[156:159], a[52:55], v[136:139]\n"
"s_cmp_lt_u32 s69, 7\n"
"s_cselect_b32 s98, -1, 0\n"
"buffer_load_dwordx4 v173, s[44:47], s62 offen offset:2048 lds\n"
"v_mfma_f32_16x16x32_bf16 v[140:143], a[152:155], a[56:59], v[140:143]\n"
"s_cmp_lt_u32 s97, 27\n"
"s_cselect_b32 s98, s98, 0\n"
"v_mfma_f32_16x16x32_bf16 v[140:143], a[156:159], a[60:63], v[140:143]\n"
"s_waitcnt lgkmcnt(4)\n"
"buffer_load_dwordx4 v174, s[44:47], s62 offen offset:3072 lds\n"
"v_mfma_f32_16x16x32_bf16 v[16:19], a[128:131], a[0:3], v[16:19]\n"
"s_lshl_b32 s70, s97, 7\n"
"ds_read_b64_tr_b16 a[144:145], v168 offset:0\n"
"v_mfma_f32_16x16x32_bf16 v[16:19], a[132:135], a[4:7], v[16:19]\n"
"s_mul_i32 s71, s30, 1769472\n"
"ds_read_b64_tr_b16 a[146:147], v168 offset:1024\n"
"v_mfma_f32_16x16x32_bf16 v[20:23], a[128:131], a[8:11], v[20:23]\n"
"s_add_u32 s70, s70, s71\n"
"ds_read_b64_tr_b16 a[148:149], v168 offset:8192\n"
"v_mfma_f32_16x16x32_bf16 v[20:23], a[132:135], a[12:15], v[20:23]\n"
"s_sub_u32 s71, 7077888, s70\n"
"ds_read_b64_tr_b16 a[150:151], v168 offset:9216\n"
"v_mfma_f32_16x16x32_bf16 v[24:27], a[128:131], a[16:19], v[24:27]\n"
"s_add_u32 m0, s59, 0\n"
"v_mfma_f32_16x16x32_bf16 v[24:27], a[132:135], a[20:23], v[24:27]\n"
"s_and_b32 s71, s71, s98\n"
"buffer_load_dwordx4 v8, s[52:55], s63 offen offset:0 lds\n"
"v_mfma_f32_16x16x32_bf16 v[28:31], a[128:131], a[24:27], v[28:31]\n"
"s_add_u32 s48, s18, s70\n"
"s_addc_u32 s49, s19, 0\n"
"v_mfma_f32_16x16x32_bf16 v[28:31], a[132:135], a[28:31], v[28:31]\n"
"s_waitcnt lgkmcnt(4)\n"
"buffer_load_dwordx4 v9, s[52:55], s63 offen offset:1024 lds\n"
"v_mfma_f32_16x16x32_bf16 v[32:35], a[136:139], a[0:3], v[32:35]\n"
"s_mov_b32 s50, s71\n"
"ds_read_b64_tr_b16 a[152:153], v168 offset:8\n"
"v_mfma_f32_16x16x32_bf16 v[32:35], a[140:143], a[4:7], v[32:35]\n"
"s_mov_b32 s51, 0x20000\n"
"ds_read_b64_tr_b16 a[154:155], v168 offset:1032\n"
"v_mfma_f32_16x16x32_bf16 v[36:39], a[136:139], a[8:11], v[36:39]\n"
"s_lshl_b32 s61, s30, 13\n"
"ds_read_b64_tr_b16 a[156:157], v168 offset:8200\n"
"v_lshrrev_b32 v1, 3, v0\n"
"v_mfma_f32_16x16x32_bf16 v[36:39], a[140:143], a[12:15], v[36:39]\n"
"ds_read_b64_tr_b16 a[158:159], v168 offset:9224\n"
"v_and_b32 v2, 7, v0\n"
"v_mfma_f32_16x16x32_bf16 v[40:43], a[136:139], a[16:19], v[40:43]\n"
"s_mov_b32 s92, 3456\n"
"v_mul_lo_u32 v3, v1, s92\n"
"v_mfma_f32_16x16x32_bf16 v[40:43], a[140:143], a[20:23], v[40:43]\n"
"buffer_load_dwordx4 v10, s[52:55], s63 offen offset:2048 lds\n"
"v_bfe_u32 v4, v0, 4, 1\n"
"v_mfma_f32_16x16x32_bf16 v[44:47], a[136:139], a[24:27], v[44:47]\n"
"v_lshlrev_b32 v4, 1, v4\n"
"v_mfma_f32_16x16x32_bf16 v[44:47], a[140:143], a[28:31], v[44:47]\n"
"s_waitcnt lgkmcnt(4)\n"
"buffer_load_dwordx4 v11, s[52:55], s63 offen offset:3072 lds\n"
"v_xor_b32 v5, v4, v2\n"
"v_mfma_f32_16x16x32_bf16 v[48:51], a[144:147], a[0:3], v[48:51]\n"
"ds_read_b64_tr_b16 a[128:129], v169 offset:0\n"
"v_lshl_add_u32 v5, v5, 4, v3\n"
"v_mfma_f32_16x16x32_bf16 v[48:51], a[148:151], a[4:7], v[48:51]\n"
"ds_read_b64_tr_b16 a[130:131], v169 offset:1024\n"
"v_add_u32 v240, 0, v5\n"
"v_mfma_f32_16x16x32_bf16 v[52:55], a[144:147], a[8:11], v[52:55]\n"
"ds_read_b64_tr_b16 a[132:133], v169 offset:8192\n"
"v_or_b32 v5, 4, v4\n"
"v_mfma_f32_16x16x32_bf16 v[52:55], a[148:151], a[12:15], v[52:55]\n"
"ds_read_b64_tr_b16 a[134:135], v169 offset:9216\n"
"v_xor_b32 v5, v5, v2\n"
"v_mfma_f32_16x16x32_bf16 v[56:59], a[144:147], a[16:19], v[56:59]\n"
"s_add_u32 m0, s59, 4096\n"
"v_lshl_add_u32 v5, v5, 4, v3\n"
"v_mfma_f32_16x16x32_bf16 v[56:59], a[148:151], a[20:23], v[56:59]\n"
"buffer_load_dwordx4 v8, s[52:55], s58 offen offset:0 lds\n"
"v_add_u32 v241, 26624, v5\n"
"v_mfma_f32_16x16x32_bf16 v[60:63], a[144:147], a[24:27], v[60:63]\n"
"v_xor_b32 v5, v4, v2\n"
"v_mfma_f32_16x16x32_bf16 v[60:63], a[148:151], a[28:31], v[60:63]\n"
"s_waitcnt lgkmcnt(4)\n"
"buffer_load_dwordx4 v9, s[52:55], s58 offen offset:1024 lds\n"
"v_lshl_add_u32 v5, v5, 4, v3\n"
"v_mfma_f32_16x16x32_bf16 v[64:67], a[152:155], a[0:3], v[64:67]\n"
"ds_read_b64_tr_b16 a[136:137], v169 offset:8\n"
"v_add_u32 v242, 53248, v5\n"
"v_mfma_f32_16x16x32_bf16 v[64:67], a[156:159], a[4:7], v[64:67]\n"
"ds_read_b64_tr_b16 a[138:139], v169 offset:1032\n"
"v_or_b32 v5, 4, v4\n"
"v_mfma_f32_16x16x32_bf16 v[68:71], a[152:155], a[8:11], v[68:71]\n"
"ds_read_b64_tr_b16 a[140:141], v169 offset:8200\n"
"v_xor_b32 v5, v5, v2\n"
"v_mfma_f32_16x16x32_bf16 v[68:71], a[156:159], a[12:15], v[68:71]\n"
"s_waitcnt vmcnt(15)\n"
"ds_read_b64_tr_b16 a[142:143], v169 offset:9224\n"
"v_lshl_add_u32 v5, v5, 4, v3\n"
"v_mfma_f32_16x16x32_bf16 v[72:75], a[152:155], a[16:19], v[72:75]\n"
"ds_read_b128 a[32:35], v12 offset:32768\n"
"v_add_u32 v243, 79872, v5\n"
"v_mfma_f32_16x16x32_bf16 v[72:75], a[156:159], a[20:23], v[72:75]\n"
"s_lshl_b32 s70, s30, 9\n"
"buffer_load_dwordx4 v10, s[52:55], s58 offen offset:2048 lds\n"
"v_mfma_f32_16x16x32_bf16 v[76:79], a[152:155], a[24:27], v[76:79]\n"
"s_add_u32 s70, s70, 8192\n"
"ds_read_b128 a[36:39], v13 offset:32768\n"
"v_lshlrev_b32 v254, 3, v0\n"
"v_mfma_f32_16x16x32_bf16 v[76:79], a[156:159], a[28:31], v[76:79]\n"
"s_waitcnt lgkmcnt(6)\n"
"buffer_load_dwordx4 v11, s[52:55], s58 offen offset:3072 lds\n"
"v_add_u32 v254, s70, v254\n"
"v_mfma_f32_16x16x32_bf16 v[80:83], a[128:131], a[0:3], v[80:83]\n"
"ds_read_b64_tr_b16 a[144:145], v170 offset:0\n"
"v_lshlrev_b32 v253, 7, v0\n"
"v_mfma_f32_16x16x32_bf16 v[80:83], a[132:135], a[4:7], v[80:83]\n"
"s_mul_i32 s96, s96, 327680\n"
"ds_read_b64_tr_b16 a[146:147], v170 offset:1024\n"
"v_mfma_f32_16x16x32_bf16 v[84:87], a[128:131], a[8:11], v[84:87]\n"
"s_mul_i32 s70, s30, 81920\n"
"ds_read_b64_tr_b16 a[148:149], v170 offset:8192\n"
"v_mfma_f32_16x16x32_bf16 v[84:87], a[132:135], a[12:15], v[84:87]\n"
"s_add_u32 s96, s96, s70\n"
"ds_read_b64_tr_b16 a[150:151], v170 offset:9216\n"
"v_mfma_f32_16x16x32_bf16 v[88:91], a[128:131], a[16:19], v[88:91]\n"
"s_add_u32 s96, s96, 4194304\n"
"ds_read_b128 a[40:43], v12 offset:34816\n"
"v_mfma_f32_16x16x32_bf16 v[88:91], a[132:135], a[20:23], v[88:91]\n"
"s_and_b32 s99, s98, 81920\n"
"v_mfma_f32_16x16x32_bf16 v[92:95], a[128:131], a[24:27], v[92:95]\n"
"ds_read_b128 a[44:47], v13 offset:34816\n"
"v_mfma_f32_16x16x32_bf16 v[92:95], a[132:135], a[28:31], v[92:95]\n"
"s_waitcnt lgkmcnt(8)\n"
"v_mfma_f32_16x16x32_bf16 v[96:99], a[136:139], a[0:3], v[96:99]\n"
"ds_read_b64_tr_b16 a[152:153], v170 offset:8\n"
"v_mfma_f32_16x16x32_bf16 v[96:99], a[140:143], a[4:7], v[96:99]\n"
"ds_read_b64_tr_b16 a[154:155], v170 offset:1032\n"
"v_mfma_f32_16x16x32_bf16 v[100:103], a[136:139], a[8:11], v[100:103]\n"
"ds_read_b64_tr_b16 a[156:157], v170 offset:8200\n"
"v_mfma_f32_16x16x32_bf16 v[100:103], a[140:143], a[12:15], v[100:103]\n"
"ds_read_b64_tr_b16 a[158:159], v170 offset:9224\n"
"v_mfma_f32_16x16x32_bf16 v[104:107], a[136:139], a[16:19], v[104:107]\n"
"ds_read_b128 a[48:51], v12 offset:36864\n"
"v_mfma_f32_16x16x32_bf16 v[104:107], a[140:143], a[20:23], v[104:107]\n"
"v_mfma_f32_16x16x32_bf16 v[108:111], a[136:139], a[24:27], v[108:111]\n"
"ds_read_b128 a[52:55], v13 offset:36864\n"
"v_mfma_f32_16x16x32_bf16 v[108:111], a[140:143], a[28:31], v[108:111]\n"
"s_waitcnt vmcnt(25) lgkmcnt(8)\n"
"v_mfma_f32_16x16x32_bf16 v[112:115], a[144:147], a[0:3], v[112:115]\n"
"s_barrier\n"
"ds_read_b64_tr_b16 a[128:129], v167 offset:16384\n"
"v_mfma_f32_16x16x32_bf16 v[112:115], a[148:151], a[4:7], v[112:115]\n"
"s_add_u32 s63, s63, 128\n"
"s_cmp_eq_u32 s63, 1280\n"
"s_cselect_b32 s63, 0, s63\n"
"s_add_u32 s58, s63, 40960\n"
"ds_read_b64_tr_b16 a[130:131], v167 offset:17408\n"
"v_mfma_f32_16x16x32_bf16 v[116:119], a[144:147], a[8:11], v[116:119]\n"
"ds_read_b64_tr_b16 a[132:133], v167 offset:24576\n"
"v_mfma_f32_16x16x32_bf16 v[116:119], a[148:151], a[12:15], v[116:119]\n"
"s_add_u32 m0, s65, 49152\n"
"ds_read_b64_tr_b16 a[134:135], v167 offset:25600\n"
"v_mfma_f32_16x16x32_bf16 v[120:123], a[144:147], a[16:19], v[120:123]\n"
"s_add_u32 s62, s62, 524288\n"
"s_cmp_eq_u32 s62, 5242880\n"
"s_cselect_b32 s62, 0, s62\n"
"ds_read_b128 a[56:59], v12 offset:38912\n"
"v_mfma_f32_16x16x32_bf16 v[120:123], a[148:151], a[20:23], v[120:123]\n"
"buffer_load_dwordx4 v171, s[44:47], s62 offen offset:0 lds\n"
"v_mfma_f32_16x16x32_bf16 v[124:127], a[144:147], a[24:27], v[124:127]\n"
"ds_read_b128 a[60:63], v13 offset:38912\n"
"v_mfma_f32_16x16x32_bf16 v[124:127], a[148:151], a[28:31], v[124:127]\n"
"s_waitcnt lgkmcnt(8)\n"
"buffer_load_dwordx4 v172, s[44:47], s62 offen offset:1024 lds\n"
"v_mfma_f32_16x16x32_bf16 v[128:131], a[152:155], a[0:3], v[128:131]\n"
"ds_read_b64_tr_b16 a[136:137], v167 offset:16392\n"
"v_mfma_f32_16x16x32_bf16 v[128:131], a[156:159], a[4:7], v[128:131]\n"
"ds_read_b64_tr_b16 a[138:139], v167 offset:17416\n"
"v_mfma_f32_16x16x32_bf16 v[132:135], a[152:155], a[8:11], v[132:135]\n"
"s_cmp_lg_u32 s30, 0\n"
"s_cbranch_scc1 L_nohf_%=\n"
"s_mov_b64 exec, 1\n"
"global_store_dwordx2 v208, v[210:211], s[6:7] sc1\n"
"s_mov_b64 exec, -1\n"
"L_nohf_%=:\n"
"ds_read_b64_tr_b16 a[140:141], v167 offset:24584\n"
"v_mfma_f32_16x16x32_bf16 v[132:135], a[156:159], a[12:15], v[132:135]\n"
"ds_read_b64_tr_b16 a[142:143], v167 offset:25608\n"
"v_mfma_f32_16x16x32_bf16 v[136:139], a[152:155], a[16:19], v[136:139]\n"
"v_mfma_f32_16x16x32_bf16 v[136:139], a[156:159], a[20:23], v[136:139]\n"
"buffer_load_dwordx4 v173, s[44:47], s62 offen offset:2048 lds\n"
"v_mfma_f32_16x16x32_bf16 v[140:143], a[152:155], a[24:27], v[140:143]\n"
"v_mfma_f32_16x16x32_bf16 v[140:143], a[156:159], a[28:31], v[140:143]\n"
"s_waitcnt lgkmcnt(4)\n"
"buffer_load_dwordx4 v174, s[44:47], s62 offen offset:3072 lds\n"
"v_mfma_f32_16x16x32_bf16 v[16:19], a[128:131], a[32:35], v[16:19]\n"
"ds_read_b64_tr_b16 a[144:145], v168 offset:16384\n"
"v_mfma_f32_16x16x32_bf16 v[16:19], a[132:135], a[36:39], v[16:19]\n"
"ds_read_b64_tr_b16 a[146:147], v168 offset:17408\n"
"v_mfma_f32_16x16x32_bf16 v[20:23], a[128:131], a[40:43], v[20:23]\n"
"ds_read_b64_tr_b16 a[148:149], v168 offset:24576\n"
"v_mfma_f32_16x16x32_bf16 v[20:23], a[132:135], a[44:47], v[20:23]\n"
"ds_read_b64_tr_b16 a[150:151], v168 offset:25600\n"
"v_mfma_f32_16x16x32_bf16 v[24:27], a[128:131], a[48:51], v[24:27]\n"
"s_add_u32 m0, s59, 32768\n"
"v_mfma_f32_16x16x32_bf16 v[24:27], a[132:135], a[52:55], v[24:27]\n"
"buffer_load_dwordx4 v8, s[52:55], s63 offen offset:0 lds\n"
"v_mfma_f32_16x16x32_bf16 v[28:31], a[128:131], a[56:59], v[28:31]\n"
"v_mfma_f32_16x16x32_bf16 v[28:31], a[132:135], a[60:63], v[28:31]\n"
"s_waitcnt lgkmcnt(4)\n"
"buffer_load_dwordx4 v9, s[52:55], s63 offen offset:1024 lds\n"
"v_mfma_f32_16x16x32_bf16 v[32:35], a[136:139], a[32:35], v[32:35]\n"
"ds_read_b64_tr_b16 a[152:153], v168 offset:16392\n"
"v_mfma_f32_16x16x32_bf16 v[32:35], a[140:143], a[36:39], v[32:35]\n"
"ds_read_b64_tr_b16 a[154:155], v168 offset:17416\n"
"v_mfma_f32_16x16x32_bf16 v[36:39], a[136:139], a[40:43], v[36:39]\n"
"s_waitcnt vmcnt(18)\n"
"ds_read_b64_tr_b16 a[156:157], v168 offset:24584\n"
"v_xor_b32 v14, s34, v225\n"
"v_mfma_f32_16x16x32_bf16 v[36:39], a[140:143], a[44:47], v[36:39]\n"
"ds_read_b64_tr_b16 a[158:159], v168 offset:25608\n"
"v_bitop3_b32 v14, v14, v227, s34 bitop3:0xf6\n"
"v_mfma_f32_16x16x32_bf16 v[40:43], a[136:139], a[48:51], v[40:43]\n"
"v_bitop3_b32 v14, v14, v229, s34 bitop3:0xf6\n"
"v_mfma_f32_16x16x32_bf16 v[40:43], a[140:143], a[52:55], v[40:43]\n"
"buffer_load_dwordx4 v10, s[52:55], s63 offen offset:2048 lds\n"
"v_bitop3_b32 v14, v14, v231, s34 bitop3:0xf6\n"
"v_mfma_f32_16x16x32_bf16 v[44:47], a[136:139], a[56:59], v[44:47]\n"
"v_bitop3_b32 v14, v14, v233, s34 bitop3:0xf6\n"
"v_mfma_f32_16x16x32_bf16 v[44:47], a[140:143], a[60:63], v[44:47]\n"
"s_waitcnt lgkmcnt(4)\n"
"buffer_load_dwordx4 v11, s[52:55], s63 offen offset:3072 lds\n"
"v_bitop3_b32 v14, v14, v235, s34 bitop3:0xf6\n"
"v_mfma_f32_16x16x32_bf16 v[48:51], a[144:147], a[32:35], v[48:51]\n"
"ds_read_b64_tr_b16 a[128:129], v169 offset:16384\n"
"v_bitop3_b32 v14, v14, v237, s34 bitop3:0xf6\n"
"v_mfma_f32_16x16x32_bf16 v[48:51], a[148:151], a[36:39], v[48:51]\n"
"ds_read_b64_tr_b16 a[130:131], v169 offset:17408\n"
"v_bitop3_b32 v14, v14, v239, s34 bitop3:0xf6\n"
"v_mfma_f32_16x16x32_bf16 v[52:55], a[144:147], a[40:43], v[52:55]\n"
"ds_read_b64_tr_b16 a[132:133], v169 offset:24576\n"
"v_bitop3_b32 v14, v14, v177, s34 bitop3:0xf6\n"
"v_mfma_f32_16x16x32_bf16 v[52:55], a[148:151], a[44:47], v[52:55]\n"
"ds_read_b64_tr_b16 a[134:135], v169 offset:25600\n"
"v_bitop3_b32 v14, v14, v179, s34 bitop3:0xf6\n"
"v_mfma_f32_16x16x32_bf16 v[56:59], a[144:147], a[48:51], v[56:59]\n"
"s_add_u32 m0, s59, 36864\n"
"v_bitop3_b32 v14, v14, v181, s34 bitop3:0xf6\n"
"v_mfma_f32_16x16x32_bf16 v[56:59], a[148:151], a[52:55], v[56:59]\n"
"buffer_load_dwordx4 v8, s[52:55], s58 offen offset:0 lds\n"
"v_bitop3_b32 v14, v14, v183, s34 bitop3:0xf6\n"
"v_mfma_f32_16x16x32_bf16 v[60:63], a[144:147], a[56:59], v[60:63]\n"
"v_bitop3_b32 v14, v14, v185, s34 bitop3:0xf6\n"
"v_mfma_f32_16x16x32_bf16 v[60:63], a[148:151], a[60:63], v[60:63]\n"
"s_waitcnt lgkmcnt(4)\n"
"buffer_load_dwordx4 v9, s[52:55], s58 offen offset:1024 lds\n"
"v_bitop3_b32 v14, v14, v187, s34 bitop3:0xf6\n"
"v_mfma_f32_16x16x32_bf16 v[64:67], a[152:155], a[32:35], v[64:67]\n"
"ds_read_b64_tr_b16 a[136:137], v169 offset:16392\n"
"v_bitop3_b32 v14, v14, v189, s34 bitop3:0xf6\n"
"v_mfma_f32_16x16x32_bf16 v[64:67], a[156:159], a[36:39], v[64:67]\n"
"ds_read_b64_tr_b16 a[138:139], v169 offset:17416\n"
"v_bitop3_b32 v14, v14, v191, s34 bitop3:0xf6\n"
"v_mfma_f32_16x16x32_bf16 v[68:71], a[152:155], a[40:43], v[68:71]\n"
"ds_read_b64_tr_b16 a[140:141], v169 offset:24584\n"
"v_bitop3_b32 v14, v14, v193, s34 bitop3:0xf6\n"
"v_mfma_f32_16x16x32_bf16 v[68:71], a[156:159], a[44:47], v[68:71]\n"
"s_waitcnt vmcnt(10)\n"
"ds_read_b64_tr_b16 a[142:143], v169 offset:25608\n"
"v_bitop3_b32 v14, v14, v195, s34 bitop3:0xf6\n"
"v_mfma_f32_16x16x32_bf16 v[72:75], a[152:155], a[48:51], v[72:75]\n"
"ds_read_b128 a[0:3], v12 offset:0\n"
"v_mfma_f32_16x16x32_bf16 v[72:75], a[156:159], a[52:55], v[72:75]\n"
"buffer_load_dwordx4 v10, s[52:55], s58 offen offset:2048 lds\n"
"v_mfma_f32_16x16x32_bf16 v[76:79], a[152:155], a[56:59], v[76:79]\n"
"ds_read_b128 a[4:7], v13 offset:0\n"
"v_mfma_f32_16x16x32_bf16 v[76:79], a[156:159], a[60:63], v[76:79]\n"
"s_waitcnt lgkmcnt(6)\n"
"buffer_load_dwordx4 v11, s[52:55], s58 offen offset:3072 lds\n"
"v_mfma_f32_16x16x32_bf16 v[80:83], a[128:131], a[32:35], v[80:83]\n"
"ds_read_b64_tr_b16 a[144:145], v170 offset:16384\n"
"v_mfma_f32_16x16x32_bf16 v[80:83], a[132:135], a[36:39], v[80:83]\n"
"ds_read_b64_tr_b16 a[146:147], v170 offset:17408\n"
"v_mfma_f32_16x16x32_bf16 v[84:87], a[128:131], a[40:43], v[84:87]\n"
"v_cmp_ne_u32 vcc, 0, v14\n"
"s_nop 0\n"
"s_cmp_eq_u64 vcc, 0\n"
"s_cbranch_scc0 L_slow_gr_%=\n"
"L_back_gr_%=:\n"
"ds_read_b64_tr_b16 a[148:149], v170 offset:24576\n"
"v_mfma_f32_16x16x32_bf16 v[84:87], a[132:135], a[44:47], v[84:87]\n"
"s_add_u32 s44, s28, s96\n"
"s_addc_u32 s45, s29, 0\n"
"s_mov_b32 s46, s99\n"
"ds_read_b64_tr_b16 a[150:151], v170 offset:25600\n"
"v_mfma_f32_16x16x32_bf16 v[88:91], a[128:131], a[48:51], v[88:91]\n"
"s_mov_b32 s47, 0x20000\n"
"s_lshl_b32 s62, s31, 2\n"
"ds_read_b128 a[8:11], v12 offset:2048\n"
"v_mfma_f32_16x16x32_bf16 v[88:91], a[132:135], a[52:55], v[88:91]\n"
"s_add_u32 s62, s62, s30\n"
"s_lshl_b32 s62, s62, 1\n"
"v_mfma_f32_16x16x32_bf16 v[92:95], a[128:131], a[56:59], v[92:95]\n"
"s_add_u32 s62, s62, 1\n"
"s_and_b32 s69, s5, 3\n"
"ds_read_b128 a[12:15], v13 offset:2048\n"
"v_mfma_f32_16x16x32_bf16 v[92:95], a[132:135], a[60:63], v[92:95]\n"
"s_waitcnt lgkmcnt(8)\n"
"v_mfma_f32_16x16x32_bf16 v[96:99], a[136:139], a[32:35], v[96:99]\n"
"s_lshl_b32 s69, s69, 1\n"
"s_lshr_b32 s70, s5, 2\n"
"ds_read_b64_tr_b16 a[152:153], v170 offset:16392\n"
"v_mfma_f32_16x16x32_bf16 v[96:99], a[140:143], a[36:39], v[96:99]\n"
"s_add_u32 s69, s69, s70\n"
"s_sub_u32 s70, s69, 8\n"
"s_cselect_b32 s69, s69, s70\n"
"ds_read_b64_tr_b16 a[154:155], v170 offset:17416\n"
"v_mfma_f32_16x16x32_bf16 v[100:103], a[136:139], a[40:43], v[100:103]\n"
"s_mul_i32 s58, s69, 10240\n"
"s_mul_i32 s60, s69, 221184\n"
"ds_read_b64_tr_b16 a[156:157], v170 offset:24584\n"
"v_mfma_f32_16x16x32_bf16 v[100:103], a[140:143], a[44:47], v[100:103]\n"
"s_and_b32 s70, s33, 7\n"
"s_lshl_b32 s70, s70, 2\n"
"ds_read_b64_tr_b16 a[158:159], v170 offset:25608\n"
"v_mfma_f32_16x16x32_bf16 v[104:107], a[136:139], a[48:51], v[104:107]\n"
"s_and_b32 s71, s5, 3\n"
"s_add_u32 s70, s70, s71\n"
"ds_read_b128 a[16:19], v12 offset:4096\n"
"v_mfma_f32_16x16x32_bf16 v[104:107], a[140:143], a[52:55], v[104:107]\n"
"s_lshr_b32 s71, s33, 3\n"
"s_lshl_b32 s71, s71, 5\n"
"v_mfma_f32_16x16x32_bf16 v[108:111], a[136:139], a[56:59], v[108:111]\n"
"s_add_u32 s71, s71, s5\n"
"s_lshr_b32 s71, s71, 2\n"
"ds_read_b128 a[20:23], v13 offset:4096\n"
"v_mfma_f32_16x16x32_bf16 v[108:111], a[140:143], a[60:63], v[108:111]\n"
"s_waitcnt vmcnt(20) lgkmcnt(8)\n"
"v_mfma_f32_16x16x32_bf16 v[112:115], a[144:147], a[32:35], v[112:115]\n"
"s_barrier\n"
"ds_read_b64_tr_b16 a[128:129], v167 offset:32768\n"
"v_mfma_f32_16x16x32_bf16 v[112:115], a[148:151], a[36:39], v[112:115]\n"
"s_lshl_b32 s69, s71, 2\n"
"s_add_u32 s69, s69, s30\n"
"ds_read_b64_tr_b16 a[130:131], v167 offset:33792\n"
"v_mfma_f32_16x16x32_bf16 v[116:119], a[144:147], a[40:43], v[116:119]\n"
"s_lshl_b32 s69, s69, 19\n"
"s_lshl_b32 s70, s70, 12\n"
"ds_read_b64_tr_b16 a[132:133], v167 offset:40960\n"
"v_mfma_f32_16x16x32_bf16 v[116:119], a[148:151], a[44:47], v[116:119]\n"
"s_add_u32 m0, s65, 0\n"
"ds_read_b64_tr_b16 a[134:135], v167 offset:41984\n"
"v_mfma_f32_16x16x32_bf16 v[120:123], a[144:147], a[48:51], v[120:123]\n"
"s_add_u32 s69, s69, s70\n"
"s_add_u32 s69, s69, 16777216\n"
"ds_read_b128 a[24:27], v12 offset:6144\n"
"v_mfma_f32_16x16x32_bf16 v[120:123], a[148:151], a[52:55], v[120:123]\n"
"s_add_u32 s52, s28, s69\n"
"s_addc_u32 s53, s29, 0\n"
"s_mov_b32 s54, 524288\n"
"buffer_load_dwordx4 v165, s[40:43], s57 offen offset:0 lds\n"
"v_mfma_f32_16x16x32_bf16 v[124:127], a[144:147], a[56:59], v[124:127]\n"
"s_mov_b32 s55, 0x20000\n"
"ds_read_b128 a[28:31], v13 offset:6144\n"
"v_sub_f32 v14, 0, v236\n"
"v_mfma_f32_16x16x32_bf16 v[124:127], a[148:151], a[60:63], v[124:127]\n"
"s_waitcnt lgkmcnt(8)\n"
"buffer_load_dwordx4 v166, s[40:43], s57 offen offset:1024 lds\n"
"v_cvt_pk_bf16_f32 v15, v14, v14\n"
"v_mfma_f32_16x16x32_bf16 v[128:131], a[152:155], a[32:35], v[128:131]\n"
"ds_read_b64_tr_b16 a[136:137], v167 offset:32776\n"
"v_accvgpr_write_b32 a108, v15\n"
"v_mfma_f32_16x16x32_bf16 v[128:131], a[156:159], a[36:39], v[128:131]\n"
"ds_read_b64_tr_b16 a[138:139], v167 offset:33800\n"
"v_and_b32 v15, 0xffff0000, v15\n"
"v_mfma_f32_16x16x32_bf16 v[132:135], a[152:155], a[40:43], v[132:135]\n"
"ds_read_b64_tr_b16 a[140:141], v167 offset:40968\n"
"v_sub_f32 v14, v14, v15\n"
"v_mfma_f32_16x16x32_bf16 v[132:135], a[156:159], a[44:47], v[132:135]\n"
"ds_read_b64_tr_b16 a[142:143], v167 offset:41992\n"
"v_cvt_pk_bf16_f32 v15, v14, v14\n"
"v_mfma_f32_16x16x32_bf16 v[136:139], a[152:155], a[48:51], v[136:139]\n"
"v_accvgpr_write_b32 a109, v15\n"
"v_mov_b32 v239, v238\n"
"v_mfma_f32_16x16x32_bf16 v[136:139], a[156:159], a[52:55], v[136:139]\n"
"buffer_load_dwordx4 v165, s[40:43], s57 offen offset:2048 lds\n"
"v_sub_f32 v14, 0, v224\n"
"v_mfma_f32_16x16x32_bf16 v[140:143], a[152:155], a[56:59], v[140:143]\n"
"v_cvt_pk_bf16_f32 v15, v14, v14\n"
"v_accvgpr_write_b32 a96, v15\n"
"v_mfma_f32_16x16x32_bf16 v[140:143], a[156:159], a[60:63], v[140:143]\n"
"s_waitcnt lgkmcnt(4)\n"
"buffer_load_dwordx4 v166, s[40:43], s57 offen offset:3072 lds\n"
"v_and_b32 v15, 0xffff0000, v15\n"
"v_mfma_f32_16x16x32_bf16 v[16:19], a[128:131], a[0:3], v[16:19]\n"
"ds_read_b64_tr_b16 a[144:145], v168 offset:32768\n"
"v_sub_f32 v14, v14, v15\n"
"v_mfma_f32_16x16x32_bf16 v[16:19], a[132:135], a[4:7], v[16:19]\n"
"ds_read_b64_tr_b16 a[146:147], v168 offset:33792\n"
"v_cvt_pk_bf16_f32 v15, v14, v14\n"
"v_mfma_f32_16x16x32_bf16 v[20:23], a[128:131], a[8:11], v[20:23]\n"
"ds_read_b64_tr_b16 a[148:149], v168 offset:40960\n"
"v_accvgpr_write_b32 a97, v15\n"
"v_mfma_f32_16x16x32_bf16 v[20:23], a[132:135], a[12:15], v[20:23]\n"
"ds_read_b64_tr_b16 a[150:151], v168 offset:41984\n"
"v_mov_b32 v236, v226\n"
"v_mfma_f32_16x16x32_bf16 v[24:27], a[128:131], a[16:19], v[24:27]\n"
"v_sub_f32 v14, 0, v228\n"
"v_cvt_pk_bf16_f32 v15, v14, v14\n"
"v_mfma_f32_16x16x32_bf16 v[24:27], a[132:135], a[20:23], v[24:27]\n"
"v_accvgpr_write_b32 a100, v15\n"
"v_and_b32 v15, 0xffff0000, v15\n"
"v_mfma_f32_16x16x32_bf16 v[28:31], a[128:131], a[24:27], v[28:31]\n"
"v_sub_f32 v14, v14, v15\n"
"v_cvt_pk_bf16_f32 v15, v14, v14\n"
"v_mfma_f32_16x16x32_bf16 v[28:31], a[132:135], a[28:31], v[28:31]\n"
"s_waitcnt lgkmcnt(4)\n"
"v_accvgpr_write_b32 a101, v15\n"
"v_mov_b32 v237, v230\n"
"v_mfma_f32_16x16x32_bf16 v[32:35], a[136:139], a[0:3], v[32:35]\n"
"ds_read_b64_tr_b16 a[152:153], v168 offset:32776\n"
"v_sub_f32 v14, 0, v232\n"
"v_mfma_f32_16x16x32_bf16 v[32:35], a[140:143], a[4:7], v[32:35]\n"
"ds_read_b64_tr_b16 a[154:155], v168 offset:33800\n"
"v_cvt_pk_bf16_f32 v15, v14, v14\n"
"v_mfma_f32_16x16x32_bf16 v[36:39], a[136:139], a[8:11], v[36:39]\n"
"ds_read_b64_tr_b16 a[156:157], v168 offset:40968\n"
"v_accvgpr_write_b32 a104, v15\n"
"v_mfma_f32_16x16x32_bf16 v[36:39], a[140:143], a[12:15], v[36:39]\n"
"ds_read_b64_tr_b16 a[158:159], v168 offset:41992\n"
"v_and_b32 v15, 0xffff0000, v15\n"
"v_mfma_f32_16x16x32_bf16 v[40:43], a[136:139], a[16:19], v[40:43]\n"
"v_sub_f32 v14, v14, v15\n"
"v_cvt_pk_bf16_f32 v15, v14, v14\n"
"v_mfma_f32_16x16x32_bf16 v[40:43], a[140:143], a[20:23], v[40:43]\n"
"v_accvgpr_write_b32 a105, v15\n"
"v_mov_b32 v238, v234\n"
"v_mfma_f32_16x16x32_bf16 v[44:47], a[136:139], a[24:27], v[44:47]\n"
"v_add_f32 v176, v176, v180\n"
"v_add_f32 v176, v176, v184\n"
"v_mfma_f32_16x16x32_bf16 v[44:47], a[140:143], a[28:31], v[44:47]\n"
"s_waitcnt lgkmcnt(4)\n"
"v_add_f32 v176, v176, v188\n"
"v_add_f32 v176, v176, v192\n"
"v_mfma_f32_16x16x32_bf16 v[48:51], a[144:147], a[0:3], v[48:51]\n"
"ds_read_b64_tr_b16 a[128:129], v169 offset:32768\n"
"v_cvt_pk_bf16_f32 v15, v176, v176\n"
"v_mfma_f32_16x16x32_bf16 v[48:51], a[148:151], a[4:7], v[48:51]\n"
"ds_read_b64_tr_b16 a[130:131], v169 offset:33792\n"
"v_and_b32 v15, 0xffff0000, v15\n"
"v_mfma_f32_16x16x32_bf16 v[52:55], a[144:147], a[8:11], v[52:55]\n"
"ds_read_b64_tr_b16 a[132:133], v169 offset:40960\n"
"v_sub_f32 v176, v176, v15\n"
"v_mfma_f32_16x16x32_bf16 v[52:55], a[148:151], a[12:15], v[52:55]\n"
"ds_read_b64_tr_b16 a[134:135], v169 offset:41984\n"
"v_cvt_pk_bf16_f32 v176, v15, v176\n"
"v_mfma_f32_16x16x32_bf16 v[56:59], a[144:147], a[16:19], v[56:59]\n"
"v_add_f32 v178, v178, v182\n"
"v_add_f32 v178, v178, v186\n"
"v_mfma_f32_16x16x32_bf16 v[56:59], a[148:151], a[20:23], v[56:59]\n"
"v_add_f32 v178, v178, v190\n"
"v_add_f32 v178, v178, v194\n"
"v_mfma_f32_16x16x32_bf16 v[60:63], a[144:147], a[24:27], v[60:63]\n"
"v_cvt_pk_bf16_f32 v15, v178, v178\n"
"v_and_b32 v15, 0xffff0000, v15\n"
"v_mfma_f32_16x16x32_bf16 v[60:63], a[148:151], a[28:31], v[60:63]\n"
"s_waitcnt lgkmcnt(4)\n"
"v_sub_f32 v178, v178, v15\n"
"v_cvt_pk_bf16_f32 v178, v15, v178\n"
"v_mfma_f32_16x16x32_bf16 v[64:67], a[152:155], a[0:3], v[64:67]\n"
"ds_read_b64_tr_b16 a[136:137], v169 offset:32776\n"
"v_lshrrev_b32 v215, 4, v0\n"
"v_mfma_f32_16x16x32_bf16 v[64:67], a[156:159], a[4:7], v[64:67]\n"
"ds_read_b64_tr_b16 a[138:139], v169 offset:33800\n"
"v_xor_b32 v215, 0, v215\n"
"v_mfma_f32_16x16x32_bf16 v[68:71], a[152:155], a[8:11], v[68:71]\n"
"ds_read_b64_tr_b16 a[140:141], v169 offset:40968\n"
"v_min_u32 v215, 1, v215\n"
"v_mfma_f32_16x16x32_bf16 v[68:71], a[156:159], a[12:15], v[68:71]\n"
"s_waitcnt vmcnt(4)\n"
"ds_read_b64_tr_b16 a[142:143], v169 offset:41992\n"
"v_add_u32 v215, -1, v215\n"
"v_mfma_f32_16x16x32_bf16 v[72:75], a[152:155], a[16:19], v[72:75]\n"
"ds_read_b128 a[32:35], v12 offset:32768\n"
"v_and_b32 v14, v215, v176\n"
"v_mfma_f32_16x16x32_bf16 v[72:75], a[156:159], a[20:23], v[72:75]\n"
"v_accvgpr_write_b32 a160, v14\n"
"v_accvgpr_write_b32 a161, v14\n"
"v_mfma_f32_16x16x32_bf16 v[76:79], a[152:155], a[24:27], v[76:79]\n"
"ds_read_b128 a[36:39], v13 offset:32768\n"
"v_and_b32 v14, v215, v178\n"
"v_mfma_f32_16x16x32_bf16 v[76:79], a[156:159], a[28:31], v[76:79]\n"
"s_waitcnt lgkmcnt(6)\n"
"v_accvgpr_write_b32 a164, v14\n"
"v_accvgpr_write_b32 a165, v14\n"
"v_mfma_f32_16x16x32_bf16 v[80:83], a[128:131], a[0:3], v[80:83]\n"
"ds_read_b64_tr_b16 a[144:145], v170 offset:32768\n"
"v_lshrrev_b32 v215, 4, v0\n"
"v_mfma_f32_16x16x32_bf16 v[80:83], a[132:135], a[4:7], v[80:83]\n"
"ds_read_b64_tr_b16 a[146:147], v170 offset:33792\n"
"v_xor_b32 v215, 1, v215\n"
"v_mfma_f32_16x16x32_bf16 v[84:87], a[128:131], a[8:11], v[84:87]\n"
"ds_read_b64_tr_b16 a[148:149], v170 offset:40960\n"
"v_min_u32 v215, 1, v215\n"
"v_mfma_f32_16x16x32_bf16 v[84:87], a[132:135], a[12:15], v[84:87]\n"
"ds_read_b64_tr_b16 a[150:151], v170 offset:41984\n"
"v_add_u32 v215, -1, v215\n"
"v_mfma_f32_16x16x32_bf16 v[88:91], a[128:131], a[16:19], v[88:91]\n"
"ds_read_b128 a[40:43], v12 offset:34816\n"
"v_and_b32 v14, v215, v176\n"
"v_mfma_f32_16x16x32_bf16 v[88:91], a[132:135], a[20:23], v[88:91]\n"
"v_accvgpr_write_b32 a168, v14\n"
"v_accvgpr_write_b32 a169, v14\n"
"v_mfma_f32_16x16x32_bf16 v[92:95], a[128:131], a[24:27], v[92:95]\n"
"ds_read_b128 a[44:47], v13 offset:34816\n"
"v_and_b32 v14, v215, v178\n"
"v_mfma_f32_16x16x32_bf16 v[92:95], a[132:135], a[28:31], v[92:95]\n"
"s_waitcnt lgkmcnt(8)\n"
"v_accvgpr_write_b32 a172, v14\n"
"v_accvgpr_write_b32 a173, v14\n"
"v_mfma_f32_16x16x32_bf16 v[96:99], a[136:139], a[0:3], v[96:99]\n"
"ds_read_b64_tr_b16 a[152:153], v170 offset:32776\n"
"v_lshrrev_b32 v215, 4, v0\n"
"v_mfma_f32_16x16x32_bf16 v[96:99], a[140:143], a[4:7], v[96:99]\n"
"ds_read_b64_tr_b16 a[154:155], v170 offset:33800\n"
"v_xor_b32 v215, 2, v215\n"
"v_mfma_f32_16x16x32_bf16 v[100:103], a[136:139], a[8:11], v[100:103]\n"
"ds_read_b64_tr_b16 a[156:157], v170 offset:40968\n"
"v_min_u32 v215, 1, v215\n"
"v_mfma_f32_16x16x32_bf16 v[100:103], a[140:143], a[12:15], v[100:103]\n"
"ds_read_b64_tr_b16 a[158:159], v170 offset:41992\n"
"v_add_u32 v215, -1, v215\n"
"v_mfma_f32_16x16x32_bf16 v[104:107], a[136:139], a[16:19], v[104:107]\n"
"ds_read_b128 a[48:51], v12 offset:36864\n"
"v_and_b32 v14, v215, v176\n"
"v_mfma_f32_16x16x32_bf16 v[104:107], a[140:143], a[20:23], v[104:107]\n"
"v_accvgpr_write_b32 a176, v14\n"
"v_accvgpr_write_b32 a177, v14\n"
"v_mfma_f32_16x16x32_bf16 v[108:111], a[136:139], a[24:27], v[108:111]\n"
"ds_read_b128 a[52:55], v13 offset:36864\n"
"v_and_b32 v14, v215, v178\n"
"v_mfma_f32_16x16x32_bf16 v[108:111], a[140:143], a[28:31], v[108:111]\n"
"s_waitcnt vmcnt(12) lgkmcnt(8)\n"
"v_accvgpr_write_b32 a180, v14\n"
"v_accvgpr_write_b32 a181, v14\n"
"v_mfma_f32_16x16x32_bf16 v[112:115], a[144:147], a[0:3], v[112:115]\n"
"s_barrier\n"
"ds_read_b64_tr_b16 a[128:129], v167 offset:49152\n"
"v_lshrrev_b32 v215, 4, v0\n"
"v_mfma_f32_16x16x32_bf16 v[112:115], a[148:151], a[4:7], v[112:115]\n"
"ds_read_b64_tr_b16 a[130:131], v167 offset:50176\n"
"v_xor_b32 v215, 3, v215\n"
"v_mfma_f32_16x16x32_bf16 v[116:119], a[144:147], a[8:11], v[116:119]\n"
"ds_read_b64_tr_b16 a[132:133], v167 offset:57344\n"
"v_min_u32 v215, 1, v215\n"
"v_mfma_f32_16x16x32_bf16 v[116:119], a[148:151], a[12:15], v[116:119]\n"
"s_add_u32 m0, s65, 16384\n"
"ds_read_b64_tr_b16 a[134:135], v167 offset:58368\n"
"v_add_u32 v215, -1, v215\n"
"v_mfma_f32_16x16x32_bf16 v[120:123], a[144:147], a[16:19], v[120:123]\n"
"s_add_u32 s57, s57, 16384\n"
"ds_read_b128 a[56:59], v12 offset:38912\n"
"v_and_b32 v14, v215, v176\n"
"v_mfma_f32_16x16x32_bf16 v[120:123], a[148:151], a[20:23], v[120:123]\n"
"buffer_load_dwordx4 v165, s[40:43], s57 offen offset:0 lds\n"
"v_accvgpr_write_b32 a184, v14\n"
"v_mfma_f32_16x16x32_bf16 v[124:127], a[144:147], a[24:27], v[124:127]\n"
"ds_read_b128 a[60:63], v13 offset:38912\n"
"v_accvgpr_write_b32 a185, v14\n"
"v_mfma_f32_16x16x32_bf16 v[124:127], a[148:151], a[28:31], v[124:127]\n"
"s_waitcnt lgkmcnt(8)\n"
"buffer_load_dwordx4 v166, s[40:43], s57 offen offset:1024 lds\n"
"v_and_b32 v14, v215, v178\n"
"v_mfma_f32_16x16x32_bf16 v[128:131], a[152:155], a[0:3], v[128:131]\n"
"ds_read_b64_tr_b16 a[136:137], v167 offset:49160\n"
"v_accvgpr_write_b32 a188, v14\n"
"v_mfma_f32_16x16x32_bf16 v[128:131], a[156:159], a[4:7], v[128:131]\n"
"ds_read_b64_tr_b16 a[138:139], v167 offset:50184\n"
"v_accvgpr_write_b32 a189, v14\n"
"v_mfma_f32_16x16x32_bf16 v[132:135], a[152:155], a[8:11], v[132:135]\n"
"ds_read_b64_tr_b16 a[140:141], v167 offset:57352\n"
"v_mfma_f32_16x16x32_bf16 v[132:135], a[156:159], a[12:15], v[132:135]\n"
"ds_read_b64_tr_b16 a[142:143], v167 offset:58376\n"
"v_mfma_f32_16x16x32_bf16 v[136:139], a[152:155], a[16:19], v[136:139]\n"
"v_mfma_f32_16x16x32_bf16 v[136:139], a[156:159], a[20:23], v[136:139]\n"
"buffer_load_dwordx4 v165, s[40:43], s57 offen offset:2048 lds\n"
"v_mfma_f32_16x16x32_bf16 v[140:143], a[152:155], a[24:27], v[140:143]\n"
"v_mfma_f32_16x16x32_bf16 v[140:143], a[156:159], a[28:31], v[140:143]\n"
"s_waitcnt lgkmcnt(4)\n"
"buffer_load_dwordx4 v166, s[40:43], s57 offen offset:3072 lds\n"
"v_mfma_f32_16x16x32_bf16 v[16:19], a[128:131], a[32:35], v[16:19]\n"
"ds_read_b64_tr_b16 a[144:145], v168 offset:49152\n"
"v_mfma_f32_16x16x32_bf16 v[16:19], a[132:135], a[36:39], v[16:19]\n"
"ds_read_b64_tr_b16 a[146:147], v168 offset:50176\n"
"v_mfma_f32_16x16x32_bf16 v[16:19], a[160:163], a[96:99], v[16:19]\n"
"v_mfma_f32_16x16x32_bf16 v[20:23], a[128:131], a[40:43], v[20:23]\n"
"ds_read_b64_tr_b16 a[148:149], v168 offset:57344\n"
"v_mfma_f32_16x16x32_bf16 v[20:23], a[132:135], a[44:47], v[20:23]\n"
"ds_read_b64_tr_b16 a[150:151], v168 offset:58368\n"
"v_mfma_f32_16x16x32_bf16 v[20:23], a[160:163], a[100:103], v[20:23]\n"
"v_mfma_f32_16x16x32_bf16 v[24:27], a[128:131], a[48:51], v[24:27]\n"
"s_add_u32 m0, s64, 0\n"
"v_mfma_f32_16x16x32_bf16 v[24:27], a[132:135], a[52:55], v[24:27]\n"
"buffer_load_dwordx4 v161, s[36:39], s56 offen offset:0 lds\n"
"v_mfma_f32_16x16x32_bf16 v[24:27], a[160:163], a[104:107], v[24:27]\n"
"v_mfma_f32_16x16x32_bf16 v[28:31], a[128:131], a[56:59], v[28:31]\n"
"v_mfma_f32_16x16x32_bf16 v[28:31], a[132:135], a[60:63], v[28:31]\n"
"s_waitcnt lgkmcnt(4)\n"
"buffer_load_dwordx4 v162, s[36:39], s56 offen offset:1024 lds\n"
"v_mfma_f32_16x16x32_bf16 v[28:31], a[160:163], a[108:111], v[28:31]\n"
"v_mfma_f32_16x16x32_bf16 v[32:35], a[136:139], a[32:35], v[32:35]\n"
"ds_read_b64_tr_b16 a[152:153], v168 offset:49160\n"
"v_mfma_f32_16x16x32_bf16 v[32:35], a[140:143], a[36:39], v[32:35]\n"
"ds_read_b64_tr_b16 a[154:155], v168 offset:50184\n"
"v_mfma_f32_16x16x32_bf16 v[32:35], a[164:167], a[96:99], v[32:35]\n"
"v_mfma_f32_16x16x32_bf16 v[36:39], a[136:139], a[40:43], v[36:39]\n"
"ds_read_b64_tr_b16 a[156:157], v168 offset:57352\n"
"v_mfma_f32_16x16x32_bf16 v[36:39], a[140:143], a[44:47], v[36:39]\n"
"ds_read_b64_tr_b16 a[158:159], v168 offset:58376\n"
"v_mfma_f32_16x16x32_bf16 v[36:39], a[164:167], a[100:103], v[36:39]\n"
"v_mfma_f32_16x16x32_bf16 v[40:43], a[136:139], a[48:51], v[40:43]\n"
"v_mfma_f32_16x16x32_bf16 v[40:43], a[140:143], a[52:55], v[40:43]\n"
"buffer_load_dwordx4 v163, s[36:39], s56 offen offset:2048 lds\n"
"v_mfma_f32_16x16x32_bf16 v[40:43], a[164:167], a[104:107], v[40:43]\n"
"v_mfma_f32_16x16x32_bf16 v[44:47], a[136:139], a[56:59], v[44:47]\n"
"v_mfma_f32_16x16x32_bf16 v[44:47], a[140:143], a[60:63], v[44:47]\n"
"s_waitcnt lgkmcnt(4)\n"
"buffer_load_dwordx4 v164, s[36:39], s56 offen offset:3072 lds\n"
"v_mfma_f32_16x16x32_bf16 v[44:47], a[164:167], a[108:111], v[44:47]\n"
"v_mfma_f32_16x16x32_bf16 v[48:51], a[144:147], a[32:35], v[48:51]\n"
"ds_read_b64_tr_b16 a[128:129], v169 offset:49152\n"
"v_mul_f32 v16, v16, v236\n"
"v_mul_f32 v17, v17, v236\n"
"v_mfma_f32_16x16x32_bf16 v[48:51], a[148:151], a[36:39], v[48:51]\n"
"ds_read_b64_tr_b16 a[130:131], v169 offset:50176\n"
"v_mul_f32 v18, v18, v236\n"
"v_mul_f32 v19, v19, v236\n"
"v_mfma_f32_16x16x32_bf16 v[48:51], a[168:171], a[96:99], v[48:51]\n"
"v_mul_f32 v32, v32, v236\n"
"v_mul_f32 v33, v33, v236\n"
"v_mfma_f32_16x16x32_bf16 v[52:55], a[144:147], a[40:43], v[52:55]\n"
"s_add_u32 s56, s56, 16384\n"
"ds_read_b64_tr_b16 a[132:133], v169 offset:57344\n"
"v_mul_f32 v34, v34, v236\n"
"v_mul_f32 v35, v35, v236\n"
"v_mfma_f32_16x16x32_bf16 v[52:55], a[148:151], a[44:47], v[52:55]\n"
"ds_read_b64_tr_b16 a[134:135], v169 offset:58368\n"
"v_cvt_pk_bf16_f32 v176, v16, v17\n"
"v_cvt_pk_bf16_f32 v177, v18, v19\n"
"v_mfma_f32_16x16x32_bf16 v[52:55], a[168:171], a[100:103], v[52:55]\n"
"v_cvt_pk_bf16_f32 v178, v32, v33\n"
"v_cvt_pk_bf16_f32 v179, v34, v35\n"
"v_mfma_f32_16x16x32_bf16 v[56:59], a[144:147], a[48:51], v[56:59]\n"
"s_add_u32 m0, s64, 16384\n"
"v_mul_f32 v20, v20, v237\n"
"v_mul_f32 v21, v21, v237\n"
"v_mfma_f32_16x16x32_bf16 v[56:59], a[148:151], a[52:55], v[56:59]\n"
"buffer_load_dwordx4 v161, s[36:39], s56 offen offset:0 lds\n"
"v_mul_f32 v22, v22, v237\n"
"v_mul_f32 v23, v23, v237\n"
"v_mfma_f32_16x16x32_bf16 v[56:59], a[168:171], a[104:107], v[56:59]\n"
"v_mul_f32 v36, v36, v237\n"
"v_mul_f32 v37, v37, v237\n"
"v_mfma_f32_16x16x32_bf16 v[60:63], a[144:147], a[56:59], v[60:63]\n"
"v_mul_f32 v38, v38, v237\n"
"v_mul_f32 v39, v39, v237\n"
"v_mfma_f32_16x16x32_bf16 v[60:63], a[148:151], a[60:63], v[60:63]\n"
"s_waitcnt lgkmcnt(4)\n"
"buffer_load_dwordx4 v162, s[36:39], s56 offen offset:1024 lds\n"
"v_cvt_pk_bf16_f32 v192, v20, v21\n"
"v_cvt_pk_bf16_f32 v193, v22, v23\n"
"v_mfma_f32_16x16x32_bf16 v[60:63], a[168:171], a[108:111], v[60:63]\n"
"v_cvt_pk_bf16_f32 v194, v36, v37\n"
"v_cvt_pk_bf16_f32 v195, v38, v39\n"
"v_mfma_f32_16x16x32_bf16 v[64:67], a[152:155], a[32:35], v[64:67]\n"
"ds_read_b64_tr_b16 a[136:137], v169 offset:49160\n"
"v_mul_f32 v24, v24, v238\n"
"v_mul_f32 v25, v25, v238\n"
"v_mfma_f32_16x16x32_bf16 v[64:67], a[156:159], a[36:39], v[64:67]\n"
"ds_read_b64_tr_b16 a[138:139], v169 offset:50184\n"
"v_mul_f32 v26, v26, v238\n"
"v_mul_f32 v27, v27, v238\n"
"v_mfma_f32_16x16x32_bf16 v[64:67], a[172:175], a[96:99], v[64:67]\n"
"v_mul_f32 v40, v40, v238\n"
"v_mul_f32 v41, v41, v238\n"
"v_mfma_f32_16x16x32_bf16 v[68:71], a[152:155], a[40:43], v[68:71]\n"
"ds_read_b64_tr_b16 a[140:141], v169 offset:57352\n"
"v_mul_f32 v42, v42, v238\n"
"v_mul_f32 v43, v43, v238\n"
"v_mfma_f32_16x16x32_bf16 v[68:71], a[156:159], a[44:47], v[68:71]\n"
"ds_read_b64_tr_b16 a[142:143], v169 offset:58376\n"
"v_cvt_pk_bf16_f32 v208, v24, v25\n"
"v_cvt_pk_bf16_f32 v209, v26, v27\n"
"v_mfma_f32_16x16x32_bf16 v[68:71], a[172:175], a[100:103], v[68:71]\n"
"v_cvt_pk_bf16_f32 v210, v40, v41\n"
"v_cvt_pk_bf16_f32 v211, v42, v43\n"
"v_mfma_f32_16x16x32_bf16 v[72:75], a[152:155], a[48:51], v[72:75]\n"
"v_mul_f32 v28, v28, v239\n"
"v_mul_f32 v29, v29, v239\n"
"v_mfma_f32_16x16x32_bf16 v[72:75], a[156:159], a[52:55], v[72:75]\n"
"buffer_load_dwordx4 v163, s[36:39], s56 offen offset:2048 lds\n"
"v_mul_f32 v30, v30, v239\n"
"v_mul_f32 v31, v31, v239\n"
"v_mfma_f32_16x16x32_bf16 v[72:75], a[172:175], a[104:107], v[72:75]\n"
"v_mul_f32 v44, v44, v239\n"
"v_mul_f32 v45, v45, v239\n"
"v_mfma_f32_16x16x32_bf16 v[76:79], a[152:155], a[56:59], v[76:79]\n"
"v_mul_f32 v46, v46, v239\n"
"v_mul_f32 v47, v47, v239\n"
"v_mfma_f32_16x16x32_bf16 v[76:79], a[156:159], a[60:63], v[76:79]\n"
"s_waitcnt lgkmcnt(4)\n"
"buffer_load_dwordx4 v164, s[36:39], s56 offen offset:3072 lds\n"
"v_cvt_pk_bf16_f32 v224, v28, v29\n"
"v_cvt_pk_bf16_f32 v225, v30, v31\n"
"v_mfma_f32_16x16x32_bf16 v[76:79], a[172:175], a[108:111], v[76:79]\n"
"v_cvt_pk_bf16_f32 v226, v44, v45\n"
"v_cvt_pk_bf16_f32 v227, v46, v47\n"
"v_mfma_f32_16x16x32_bf16 v[80:83], a[128:131], a[32:35], v[80:83]\n"
"ds_read_b64_tr_b16 a[144:145], v170 offset:49152\n"
"v_mul_f32 v48, v48, v236\n"
"v_mul_f32 v49, v49, v236\n"
"v_mfma_f32_16x16x32_bf16 v[80:83], a[132:135], a[36:39], v[80:83]\n"
"ds_read_b64_tr_b16 a[146:147], v170 offset:50176\n"
"v_mul_f32 v50, v50, v236\n"
"v_mul_f32 v51, v51, v236\n"
"v_mfma_f32_16x16x32_bf16 v[80:83], a[176:179], a[96:99], v[80:83]\n"
"v_mul_f32 v64, v64, v236\n"
"v_mul_f32 v65, v65, v236\n"
"v_mfma_f32_16x16x32_bf16 v[84:87], a[128:131], a[40:43], v[84:87]\n"
"ds_read_b64_tr_b16 a[148:149], v170 offset:57344\n"
"v_mul_f32 v66, v66, v236\n"
"v_mul_f32 v67, v67, v236\n"
"v_mfma_f32_16x16x32_bf16 v[84:87], a[132:135], a[44:47], v[84:87]\n"
"ds_read_b64_tr_b16 a[150:151], v170 offset:58368\n"
"v_cvt_pk_bf16_f32 v180, v48, v49\n"
"v_cvt_pk_bf16_f32 v181, v50, v51\n"
"v_mfma_f32_16x16x32_bf16 v[84:87], a[176:179], a[100:103], v[84:87]\n"
"v_cvt_pk_bf16_f32 v182, v64, v65\n"
"v_cvt_pk_bf16_f32 v183, v66, v67\n"
"v_mfma_f32_16x16x32_bf16 v[88:91], a[128:131], a[48:51], v[88:91]\n"
"v_mul_f32 v52, v52, v237\n"
"v_mul_f32 v53, v53, v237\n"
"v_mfma_f32_16x16x32_bf16 v[88:91], a[132:135], a[52:55], v[88:91]\n"
"v_mul_f32 v54, v54, v237\n"
"v_mul_f32 v55, v55, v237\n"
"v_mfma_f32_16x16x32_bf16 v[88:91], a[176:179], a[104:107], v[88:91]\n"
"v_mul_f32 v68, v68, v237\n"
"v_mul_f32 v69, v69, v237\n"
"v_mfma_f32_16x16x32_bf16 v[92:95], a[128:131], a[56:59], v[92:95]\n"
"v_mul_f32 v70, v70, v237\n"
"v_mul_f32 v71, v71, v237\n"
"v_mfma_f32_16x16x32_bf16 v[92:95], a[132:135], a[60:63], v[92:95]\n"
"s_waitcnt lgkmcnt(4)\n"
"v_cvt_pk_bf16_f32 v196, v52, v53\n"
"v_cvt_pk_bf16_f32 v197, v54, v55\n"
"v_mfma_f32_16x16x32_bf16 v[92:95], a[176:179], a[108:111], v[92:95]\n"
"v_cvt_pk_bf16_f32 v198, v68, v69\n"
"v_cvt_pk_bf16_f32 v199, v70, v71\n"
"v_mfma_f32_16x16x32_bf16 v[96:99], a[136:139], a[32:35], v[96:99]\n"
"ds_read_b64_tr_b16 a[152:153], v170 offset:49160\n"
"v_mul_f32 v56, v56, v238\n"
"v_mul_f32 v57, v57, v238\n"
"v_mfma_f32_16x16x32_bf16 v[96:99], a[140:143], a[36:39], v[96:99]\n"
"ds_read_b64_tr_b16 a[154:155], v170 offset:50184\n"
"v_mul_f32 v58, v58, v238\n"
"v_mul_f32 v59, v59, v238\n"
"v_mfma_f32_16x16x32_bf16 v[96:99], a[180:183], a[96:99], v[96:99]\n"
"v_mul_f32 v72, v72, v238\n"
"v_mul_f32 v73, v73, v238\n"
"v_mfma_f32_16x16x32_bf16 v[100:103], a[136:139], a[40:43], v[100:103]\n"
"ds_read_b64_tr_b16 a[156:157], v170 offset:57352\n"
"v_mul_f32 v74, v74, v238\n"
"v_mul_f32 v75, v75, v238\n"
"v_mfma_f32_16x16x32_bf16 v[100:103], a[140:143], a[44:47], v[100:103]\n"
"ds_read_b64_tr_b16 a[158:159], v170 offset:58376\n"
"v_cvt_pk_bf16_f32 v212, v56, v57\n"
"v_cvt_pk_bf16_f32 v213, v58, v59\n"
"v_mfma_f32_16x16x32_bf16 v[100:103], a[180:183], a[100:103], v[100:103]\n"
"v_cvt_pk_bf16_f32 v214, v72, v73\n"
"v_cvt_pk_bf16_f32 v215, v74, v75\n"
"v_mfma_f32_16x16x32_bf16 v[104:107], a[136:139], a[48:51], v[104:107]\n"
"v_mul_f32 v60, v60, v239\n"
"v_mul_f32 v61, v61, v239\n"
"v_mfma_f32_16x16x32_bf16 v[104:107], a[140:143], a[52:55], v[104:107]\n"
"v_mul_f32 v62, v62, v239\n"
"v_mul_f32 v63, v63, v239\n"
"v_mfma_f32_16x16x32_bf16 v[104:107], a[180:183], a[104:107], v[104:107]\n"
"v_mul_f32 v76, v76, v239\n"
"v_mul_f32 v77, v77, v239\n"
"v_mfma_f32_16x16x32_bf16 v[108:111], a[136:139], a[56:59], v[108:111]\n"
"v_mul_f32 v78, v78, v239\n"
"v_mul_f32 v79, v79, v239\n"
"v_mfma_f32_16x16x32_bf16 v[108:111], a[140:143], a[60:63], v[108:111]\n"
"s_waitcnt vmcnt(4) lgkmcnt(4)\n"
"v_cvt_pk_bf16_f32 v228, v60, v61\n"
"v_cvt_pk_bf16_f32 v229, v62, v63\n"
"v_mfma_f32_16x16x32_bf16 v[108:111], a[180:183], a[108:111], v[108:111]\n"
"v_cvt_pk_bf16_f32 v230, v76, v77\n"
"v_cvt_pk_bf16_f32 v231, v78, v79\n"
"v_mfma_f32_16x16x32_bf16 v[112:115], a[144:147], a[32:35], v[112:115]\n"
"s_barrier\n"
"ds_read_b128 a[192:195], v149 offset:0\n"
"v_mul_f32 v80, v80, v236\n"
"v_mul_f32 v81, v81, v236\n"
"v_mfma_f32_16x16x32_bf16 v[112:115], a[148:151], a[36:39], v[112:115]\n"
"ds_read_b128 a[196:199], v150 offset:0\n"
"v_mul_f32 v82, v82, v236\n"
"v_mul_f32 v83, v83, v236\n"
"v_mfma_f32_16x16x32_bf16 v[112:115], a[184:187], a[96:99], v[112:115]\n"
"v_mul_f32 v96, v96, v236\n"
"v_mul_f32 v97, v97, v236\n"
"v_mfma_f32_16x16x32_bf16 v[116:119], a[144:147], a[40:43], v[116:119]\n"
"s_add_u32 s56, s56, 16384\n"
"ds_read_b128 a[200:203], v151 offset:0\n"
"v_mul_f32 v98, v98, v236\n"
"v_mul_f32 v99, v99, v236\n"
"v_mfma_f32_16x16x32_bf16 v[116:119], a[148:151], a[44:47], v[116:119]\n"
"ds_read_b128 a[204:207], v152 offset:0\n"
"v_cvt_pk_bf16_f32 v184, v80, v81\n"
"v_cvt_pk_bf16_f32 v185, v82, v83\n"
"v_mfma_f32_16x16x32_bf16 v[116:119], a[184:187], a[100:103], v[116:119]\n"
"v_cvt_pk_bf16_f32 v186, v96, v97\n"
"v_cvt_pk_bf16_f32 v187, v98, v99\n"
"v_mfma_f32_16x16x32_bf16 v[120:123], a[144:147], a[48:51], v[120:123]\n"
"s_add_u32 m0, s64, 32768\n"
"v_mul_f32 v84, v84, v237\n"
"v_mul_f32 v85, v85, v237\n"
"v_mfma_f32_16x16x32_bf16 v[120:123], a[148:151], a[52:55], v[120:123]\n"
"buffer_load_dwordx4 v161, s[36:39], s56 offen offset:0 lds\n"
"v_mul_f32 v86, v86, v237\n"
"v_mul_f32 v87, v87, v237\n"
"v_mfma_f32_16x16x32_bf16 v[120:123], a[184:187], a[104:107], v[120:123]\n"
"v_mul_f32 v100, v100, v237\n"
"v_mul_f32 v101, v101, v237\n"
"v_mfma_f32_16x16x32_bf16 v[124:127], a[144:147], a[56:59], v[124:127]\n"
"v_mul_f32 v102, v102, v237\n"
"v_mul_f32 v103, v103, v237\n"
"v_mfma_f32_16x16x32_bf16 v[124:127], a[148:151], a[60:63], v[124:127]\n"
"s_waitcnt lgkmcnt(4)\n"
"buffer_load_dwordx4 v162, s[36:39], s56 offen offset:1024 lds\n"
"v_cvt_pk_bf16_f32 v200, v84, v85\n"
"v_cvt_pk_bf16_f32 v201, v86, v87\n"
"v_mfma_f32_16x16x32_bf16 v[124:127], a[184:187], a[108:111], v[124:127]\n"
"v_cvt_pk_bf16_f32 v202, v100, v101\n"
"v_cvt_pk_bf16_f32 v203, v102, v103\n"
"v_mfma_f32_16x16x32_bf16 v[128:131], a[152:155], a[32:35], v[128:131]\n"
"v_mul_f32 v88, v88, v238\n"
"v_mul_f32 v89, v89, v238\n"
"v_mfma_f32_16x16x32_bf16 v[128:131], a[156:159], a[36:39], v[128:131]\n"
"v_mul_f32 v90, v90, v238\n"
"v_mul_f32 v91, v91, v238\n"
"v_mfma_f32_16x16x32_bf16 v[128:131], a[188:191], a[96:99], v[128:131]\n"
"v_mul_f32 v104, v104, v238\n"
"v_mul_f32 v105, v105, v238\n"
"v_mfma_f32_16x16x32_bf16 v[132:135], a[152:155], a[40:43], v[132:135]\n"
"v_mul_f32 v106, v106, v238\n"
"v_mul_f32 v107, v107, v238\n"
"v_mfma_f32_16x16x32_bf16 v[132:135], a[156:159], a[44:47], v[132:135]\n"
"v_cvt_pk_bf16_f32 v216, v88, v89\n"
"v_cvt_pk_bf16_f32 v217, v90, v91\n"
"v_mfma_f32_16x16x32_bf16 v[132:135], a[188:191], a[100:103], v[132:135]\n"
"v_cvt_pk_bf16_f32 v218, v104, v105\n"
"v_cvt_pk_bf16_f32 v219, v106, v107\n"
"v_mfma_f32_16x16x32_bf16 v[136:139], a[152:155], a[48:51], v[136:139]\n"
"v_mul_f32 v92, v92, v239\n"
"v_mul_f32 v93, v93, v239\n"
"v_mfma_f32_16x16x32_bf16 v[136:139], a[156:159], a[52:55], v[136:139]\n"
"buffer_load_dwordx4 v163, s[36:39], s56 offen offset:2048 lds\n"
"v_mul_f32 v94, v94, v239\n"
"v_mul_f32 v95, v95, v239\n"
"v_mfma_f32_16x16x32_bf16 v[136:139], a[188:191], a[104:107], v[136:139]\n"
"v_mul_f32 v108, v108, v239\n"
"v_mul_f32 v109, v109, v239\n"
"v_mfma_f32_16x16x32_bf16 v[140:143], a[152:155], a[56:59], v[140:143]\n"
"v_mul_f32 v110, v110, v239\n"
"v_mul_f32 v111, v111, v239\n"
"v_mfma_f32_16x16x32_bf16 v[140:143], a[156:159], a[60:63], v[140:143]\n"
"buffer_load_dwordx4 v164, s[36:39], s56 offen offset:3072 lds\n"
"v_cvt_pk_bf16_f32 v232, v92, v93\n"
"v_cvt_pk_bf16_f32 v233, v94, v95\n"
"v_mfma_f32_16x16x32_bf16 v[140:143], a[188:191], a[108:111], v[140:143]\n"
"v_cvt_pk_bf16_f32 v234, v108, v109\n"
"v_cvt_pk_bf16_f32 v235, v110, v111\n"
"s_nop 5\n"
"v_pk_mul_f32 v[112:113], v[112:113], v[236:237] op_sel_hi:[1,0]\n"
"v_pk_mul_f32 v[114:115], v[114:115], v[236:237] op_sel_hi:[1,0]\n"
"v_pk_mul_f32 v[128:129], v[128:129], v[236:237] op_sel_hi:[1,0]\n"
"v_pk_mul_f32 v[130:131], v[130:131], v[236:237] op_sel_hi:[1,0]\n"
"v_cvt_pk_bf16_f32 v188, v112, v113\n"
"v_cvt_pk_bf16_f32 v189, v114, v115\n"
"v_cvt_pk_bf16_f32 v190, v128, v129\n"
"v_cvt_pk_bf16_f32 v191, v130, v131\n"
"v_pk_mul_f32 v[116:117], v[116:117], v[236:237] op_sel:[0,1]\n"
"v_pk_mul_f32 v[118:119], v[118:119], v[236:237] op_sel:[0,1]\n"
"v_pk_mul_f32 v[132:133], v[132:133], v[236:237] op_sel:[0,1]\n"
"v_pk_mul_f32 v[134:135], v[134:135], v[236:237] op_sel:[0,1]\n"
"v_cvt_pk_bf16_f32 v204, v116, v117\n"
"v_cvt_pk_bf16_f32 v205, v118, v119\n"
"v_cvt_pk_bf16_f32 v206, v132, v133\n"
"v_cvt_pk_bf16_f32 v207, v134, v135\n"
"v_pk_mul_f32 v[120:121], v[120:121], v[238:239] op_sel_hi:[1,0]\n"
"v_pk_mul_f32 v[122:123], v[122:123], v[238:239] op_sel_hi:[1,0]\n"
"v_pk_mul_f32 v[136:137], v[136:137], v[238:239] op_sel_hi:[1,0]\n"
"v_pk_mul_f32 v[138:139], v[138:139], v[238:239] op_sel_hi:[1,0]\n"
"v_cvt_pk_bf16_f32 v220, v120, v121\n"
"v_cvt_pk_bf16_f32 v221, v122, v123\n"
"v_cvt_pk_bf16_f32 v222, v136, v137\n"
"v_cvt_pk_bf16_f32 v223, v138, v139\n"
"v_pk_mul_f32 v[124:125], v[124:125], v[238:239] op_sel:[0,1]\n"
"v_pk_mul_f32 v[126:127], v[126:127], v[238:239] op_sel:[0,1]\n"
"v_pk_mul_f32 v[140:141], v[140:141], v[238:239] op_sel:[0,1]\n"
"v_pk_mul_f32 v[142:143], v[142:143], v[238:239] op_sel:[0,1]\n"
"v_cvt_pk_bf16_f32 v236, v124, v125\n"
"v_cvt_pk_bf16_f32 v237, v126, v127\n"
"v_cvt_pk_bf16_f32 v238, v140, v141\n"
"v_cvt_pk_bf16_f32 v239, v142, v143\n"
"s_add_u32 s56, s56, 16384\n"
"s_add_u32 s57, s57, 16384\n"
"s_waitcnt lgkmcnt(0)\n"
"v_mfma_f32_16x16x32_bf16 v[64:67], a[192:195], v[176:179], 0\n"
"ds_read_b128 a[128:131], v149 offset:4096\n"
"v_mov_b32 v128, 0\n"
"v_mfma_f32_16x16x32_bf16 v[64:67], a[196:199], v[180:183], v[64:67]\n"
"ds_read_b128 a[132:135], v150 offset:4096\n"
"v_mov_b32 v129, 0\n"
"v_mfma_f32_16x16x32_bf16 v[64:67], a[200:203], v[184:187], v[64:67]\n"
"ds_read_b128 a[136:139], v151 offset:4096\n"
"v_mov_b32 v130, 0\n"
"v_mfma_f32_16x16x32_bf16 v[64:67], a[204:207], v[188:191], v[64:67]\n"
"ds_read_b128 a[140:143], v152 offset:4096\n"
"v_mov_b32 v131, 0\n"
"v_mfma_f32_16x16x32_bf16 v[68:71], a[192:195], v[192:195], 0\n"
"v_mov_b32 v132, 0\n"
"v_mfma_f32_16x16x32_bf16 v[68:71], a[196:199], v[196:199], v[68:71]\n"
"v_mov_b32 v133, 0\n"
"v_mfma_f32_16x16x32_bf16 v[68:71], a[200:203], v[200:203], v[68:71]\n"
"v_mov_b32 v134, 0\n"
"v_mfma_f32_16x16x32_bf16 v[68:71], a[204:207], v[204:207], v[68:71]\n"
"v_mov_b32 v135, 0\n"
"v_mfma_f32_16x16x32_bf16 v[72:75], a[192:195], v[208:211], 0\n"
"v_mov_b32 v136, 0\n"
"v_mfma_f32_16x16x32_bf16 v[72:75], a[196:199], v[212:215], v[72:75]\n"
"v_mov_b32 v137, 0\n"
"v_mfma_f32_16x16x32_bf16 v[72:75], a[200:203], v[216:219], v[72:75]\n"
"v_mov_b32 v138, 0\n"
"v_mfma_f32_16x16x32_bf16 v[72:75], a[204:207], v[220:223], v[72:75]\n"
"v_mov_b32 v139, 0\n"
"v_mfma_f32_16x16x32_bf16 v[76:79], a[192:195], v[224:227], 0\n"
"v_mov_b32 v140, 0\n"
"v_mfma_f32_16x16x32_bf16 v[76:79], a[196:199], v[228:231], v[76:79]\n"
"v_mov_b32 v141, 0\n"
"v_mfma_f32_16x16x32_bf16 v[76:79], a[200:203], v[232:235], v[76:79]\n"
"v_mov_b32 v142, 0\n"
"v_mfma_f32_16x16x32_bf16 v[76:79], a[204:207], v[236:239], v[76:79]\n"
"v_mov_b32 v143, 0\n"
"s_nop 1\n"
"v_max3_f32 v8, v64, v65, v66\n"
"v_max_f32 v8, v8, v67\n"
"v_max3_f32 v9, v68, v69, v70\n"
"v_max_f32 v9, v9, v71\n"
"v_max3_f32 v10, v72, v73, v74\n"
"v_max_f32 v10, v10, v75\n"
"v_max3_f32 v11, v76, v77, v78\n"
"v_max_f32 v11, v11, v79\n"
"v_mov_b32 v4, v8\n"
"v_mov_b32 v5, v9\n"
"v_mov_b32 v6, v10\n"
"v_mov_b32 v7, v11\n"
"s_nop 0\n"
"v_permlane32_swap_b32 v8, v4\n"
"v_permlane32_swap_b32 v9, v5\n"
"v_permlane32_swap_b32 v10, v6\n"
"v_permlane32_swap_b32 v11, v7\n"
"v_max_f32 v8, v8, v4\n"
"v_max_f32 v9, v9, v5\n"
"v_max_f32 v10, v10, v6\n"
"v_max_f32 v11, v11, v7\n"
"v_mov_b32 v4, v8\n"
"v_mov_b32 v5, v9\n"
"v_mov_b32 v6, v10\n"
"v_mov_b32 v7, v11\n"
"s_nop 0\n"
"v_permlane16_swap_b32 v8, v4\n"
"v_permlane16_swap_b32 v9, v5\n"
"v_permlane16_swap_b32 v10, v6\n"
"v_permlane16_swap_b32 v11, v7\n"
"v_max_f32 v8, v8, v4\n"
"v_max_f32 v9, v9, v5\n"
"v_max_f32 v10, v10, v6\n"
"v_max_f32 v11, v11, v7\n"
"v_sub_f32 v112, 0, v8\n"
"v_sub_f32 v113, 0, v8\n"
"v_sub_f32 v114, 0, v8\n"
"v_sub_f32 v115, 0, v8\n"
"v_sub_f32 v116, 0, v9\n"
"v_sub_f32 v117, 0, v9\n"
"v_sub_f32 v118, 0, v9\n"
"v_sub_f32 v119, 0, v9\n"
"v_sub_f32 v120, 0, v10\n"
"v_sub_f32 v121, 0, v10\n"
"v_sub_f32 v122, 0, v10\n"
"v_sub_f32 v123, 0, v10\n"
"v_sub_f32 v124, 0, v11\n"
"v_sub_f32 v125, 0, v11\n"
"v_sub_f32 v126, 0, v11\n"
"v_sub_f32 v127, 0, v11\n"
"v_pk_add_f32 v[64:65], v[64:65], v[112:113]\n"
"v_pk_add_f32 v[66:67], v[66:67], v[114:115]\n"
"v_pk_add_f32 v[68:69], v[68:69], v[116:117]\n"
"v_pk_add_f32 v[70:71], v[70:71], v[118:119]\n"
"v_pk_add_f32 v[72:73], v[72:73], v[120:121]\n"
"v_pk_add_f32 v[74:75], v[74:75], v[122:123]\n"
"v_pk_add_f32 v[76:77], v[76:77], v[124:125]\n"
"v_pk_add_f32 v[78:79], v[78:79], v[126:127]\n"
"s_waitcnt lgkmcnt(0)\n"
"L_att_%=:\n"
"v_mfma_f32_16x16x32_bf16 v[16:19], a[128:131], v[176:179], v[112:115]\n"
"ds_read_b128 a[192:195], v149 offset:8192\n"
"v_exp_f32 v64, v64\n"
"v_mfma_f32_16x16x32_bf16 v[16:19], a[132:135], v[180:183], v[16:19]\n"
"v_exp_f32 v65, v65\n"
"v_mfma_f32_16x16x32_bf16 v[16:19], a[136:139], v[184:187], v[16:19]\n"
"ds_read_b128 a[196:199], v150 offset:8192\n"
"v_cvt_pk_bf16_f32 v32, v64, v65\n"
"v_exp_f32 v66, v66\n"
"v_mfma_f32_16x16x32_bf16 v[16:19], a[140:143], v[188:191], v[16:19]\n"
"v_exp_f32 v67, v67\n"
"v_mfma_f32_16x16x32_bf16 v[20:23], a[128:131], v[192:195], v[116:119]\n"
"ds_read_b128 a[200:203], v151 offset:8192\n"
"v_cvt_pk_bf16_f32 v33, v66, v67\n"
"v_exp_f32 v68, v68\n"
"v_mfma_f32_16x16x32_bf16 v[20:23], a[132:135], v[196:199], v[20:23]\n"
"v_exp_f32 v69, v69\n"
"v_mfma_f32_16x16x32_bf16 v[20:23], a[136:139], v[200:203], v[20:23]\n"
"ds_read_b128 a[204:207], v152 offset:8192\n"
"v_cvt_pk_bf16_f32 v40, v68, v69\n"
"v_exp_f32 v70, v70\n"
"v_mfma_f32_16x16x32_bf16 v[20:23], a[140:143], v[204:207], v[20:23]\n"
"s_add_u32 m0, s64, 49152\n"
"v_exp_f32 v71, v71\n"
"v_mfma_f32_16x16x32_bf16 v[24:27], a[128:131], v[208:211], v[120:123]\n"
"buffer_load_dwordx4 v161, s[36:39], s56 offen offset:0 lds\n"
"v_cvt_pk_bf16_f32 v41, v70, v71\n"
"v_exp_f32 v72, v72\n"
"v_mfma_f32_16x16x32_bf16 v[24:27], a[132:135], v[212:215], v[24:27]\n"
"v_exp_f32 v73, v73\n"
"v_mfma_f32_16x16x32_bf16 v[24:27], a[136:139], v[216:219], v[24:27]\n"
"buffer_load_dwordx4 v162, s[36:39], s56 offen offset:1024 lds\n"
"v_cvt_pk_bf16_f32 v48, v72, v73\n"
"v_exp_f32 v74, v74\n"
"v_mfma_f32_16x16x32_bf16 v[24:27], a[140:143], v[220:223], v[24:27]\n"
"v_exp_f32 v75, v75\n"
"v_mfma_f32_16x16x32_bf16 v[28:31], a[128:131], v[224:227], v[124:127]\n"
"buffer_load_dwordx4 v163, s[36:39], s56 offen offset:2048 lds\n"
"v_cvt_pk_bf16_f32 v49, v74, v75\n"
"v_exp_f32 v76, v76\n"
"v_mfma_f32_16x16x32_bf16 v[28:31], a[132:135], v[228:231], v[28:31]\n"
"v_exp_f32 v77, v77\n"
"v_mfma_f32_16x16x32_bf16 v[28:31], a[136:139], v[232:235], v[28:31]\n"
"buffer_load_dwordx4 v164, s[36:39], s56 offen offset:3072 lds\n"
"v_cvt_pk_bf16_f32 v56, v76, v77\n"
"v_exp_f32 v78, v78\n"
"v_mfma_f32_16x16x32_bf16 v[28:31], a[140:143], v[236:239], v[28:31]\n"
"s_waitcnt lgkmcnt(0)\n"
"v_exp_f32 v79, v79\n"
"s_nop 0\n"
"v_cvt_pk_bf16_f32 v57, v78, v79\n"
"v_mfma_f32_16x16x32_bf16 v[64:67], a[192:195], v[176:179], v[112:115]\n"
"ds_read_b128 a[128:131], v149 offset:12288\n"
"v_exp_f32 v16, v16\n"
"v_mfma_f32_16x16x32_bf16 v[64:67], a[196:199], v[180:183], v[64:67]\n"
"v_exp_f32 v17, v17\n"
"v_mfma_f32_16x16x32_bf16 v[64:67], a[200:203], v[184:187], v[64:67]\n"
"ds_read_b128 a[132:135], v150 offset:12288\n"
"v_cvt_pk_bf16_f32 v34, v16, v17\n"
"v_exp_f32 v18, v18\n"
"v_mfma_f32_16x16x32_bf16 v[64:67], a[204:207], v[188:191], v[64:67]\n"
"v_exp_f32 v19, v19\n"
"v_mfma_f32_16x16x32_bf16 v[68:71], a[192:195], v[192:195], v[116:119]\n"
"ds_read_b128 a[136:139], v151 offset:12288\n"
"v_cvt_pk_bf16_f32 v35, v18, v19\n"
"v_exp_f32 v20, v20\n"
"v_mfma_f32_16x16x32_bf16 v[68:71], a[196:199], v[196:199], v[68:71]\n"
"v_exp_f32 v21, v21\n"
"v_mfma_f32_16x16x32_bf16 v[68:71], a[200:203], v[200:203], v[68:71]\n"
"ds_read_b128 a[140:143], v152 offset:12288\n"
"v_cvt_pk_bf16_f32 v42, v20, v21\n"
"v_exp_f32 v22, v22\n"
"v_mfma_f32_16x16x32_bf16 v[68:71], a[204:207], v[204:207], v[68:71]\n"
"s_add_u32 m0, s65, 32768\n"
"v_exp_f32 v23, v23\n"
"v_mfma_f32_16x16x32_bf16 v[72:75], a[192:195], v[208:211], v[120:123]\n"
"buffer_load_dwordx4 v165, s[40:43], s57 offen offset:0 lds\n"
"v_cvt_pk_bf16_f32 v43, v22, v23\n"
"v_exp_f32 v24, v24\n"
"v_mfma_f32_16x16x32_bf16 v[72:75], a[196:199], v[212:215], v[72:75]\n"
"v_exp_f32 v25, v25\n"
"v_mfma_f32_16x16x32_bf16 v[72:75], a[200:203], v[216:219], v[72:75]\n"
"buffer_load_dwordx4 v166, s[40:43], s57 offen offset:1024 lds\n"
"v_cvt_pk_bf16_f32 v50, v24, v25\n"
"v_exp_f32 v26, v26\n"
"v_mfma_f32_16x16x32_bf16 v[72:75], a[204:207], v[220:223], v[72:75]\n"
"v_exp_f32 v27, v27\n"
"v_mfma_f32_16x16x32_bf16 v[76:79], a[192:195], v[224:227], v[124:127]\n"
"buffer_load_dwordx4 v165, s[40:43], s57 offen offset:2048 lds\n"
"v_cvt_pk_bf16_f32 v51, v26, v27\n"
"v_exp_f32 v28, v28\n"
"v_mfma_f32_16x16x32_bf16 v[76:79], a[196:199], v[228:231], v[76:79]\n"
"v_exp_f32 v29, v29\n"
"v_mfma_f32_16x16x32_bf16 v[76:79], a[200:203], v[232:235], v[76:79]\n"
"buffer_load_dwordx4 v166, s[40:43], s57 offen offset:3072 lds\n"
"v_cvt_pk_bf16_f32 v58, v28, v29\n"
"v_exp_f32 v30, v30\n"
"v_mfma_f32_16x16x32_bf16 v[76:79], a[204:207], v[236:239], v[76:79]\n"
"s_waitcnt lgkmcnt(0)\n"
"v_exp_f32 v31, v31\n"
"s_nop 0\n"
"v_cvt_pk_bf16_f32 v59, v30, v31\n"
"v_mfma_f32_16x16x32_bf16 v[16:19], a[128:131], v[176:179], v[112:115]\n"
"v_exp_f32 v64, v64\n"
"v_mfma_f32_16x16x32_bf16 v[16:19], a[132:135], v[180:183], v[16:19]\n"
"v_exp_f32 v65, v65\n"
"v_mfma_f32_16x16x32_bf16 v[16:19], a[136:139], v[184:187], v[16:19]\n"
"v_cvt_pk_bf16_f32 v36, v64, v65\n"
"v_exp_f32 v66, v66\n"
"v_mfma_f32_16x16x32_bf16 v[16:19], a[140:143], v[188:191], v[16:19]\n"
"v_exp_f32 v67, v67\n"
"v_mfma_f32_16x16x32_bf16 v[20:23], a[128:131], v[192:195], v[116:119]\n"
"v_cvt_pk_bf16_f32 v37, v66, v67\n"
"v_exp_f32 v68, v68\n"
"v_mfma_f32_16x16x32_bf16 v[20:23], a[132:135], v[196:199], v[20:23]\n"
"v_exp_f32 v69, v69\n"
"v_mfma_f32_16x16x32_bf16 v[20:23], a[136:139], v[200:203], v[20:23]\n"
"s_waitcnt vmcnt(12)\n"
"v_cvt_pk_bf16_f32 v44, v68, v69\n"
"v_exp_f32 v70, v70\n"
"v_mfma_f32_16x16x32_bf16 v[20:23], a[140:143], v[204:207], v[20:23]\n"
"s_barrier\n"
"v_exp_f32 v71, v71\n"
"v_mfma_f32_16x16x32_bf16 v[24:27], a[128:131], v[208:211], v[120:123]\n"
"ds_read_b128 a[192:195], v149 offset:16384\n"
"v_cvt_pk_bf16_f32 v45, v70, v71\n"
"v_exp_f32 v72, v72\n"
"v_mfma_f32_16x16x32_bf16 v[24:27], a[132:135], v[212:215], v[24:27]\n"
"v_exp_f32 v73, v73\n"
"v_mfma_f32_16x16x32_bf16 v[24:27], a[136:139], v[216:219], v[24:27]\n"
"ds_read_b128 a[196:199], v150 offset:16384\n"
"v_cvt_pk_bf16_f32 v52, v72, v73\n"
"v_exp_f32 v74, v74\n"
"v_mfma_f32_16x16x32_bf16 v[24:27], a[140:143], v[220:223], v[24:27]\n"
"v_exp_f32 v75, v75\n"
"v_mfma_f32_16x16x32_bf16 v[28:31], a[128:131], v[224:227], v[124:127]\n"
"s_add_u32 s56, s56, 16384\n"
"ds_read_b128 a[200:203], v151 offset:16384\n"
"v_cvt_pk_bf16_f32 v53, v74, v75\n"
"v_exp_f32 v76, v76\n"
"v_mfma_f32_16x16x32_bf16 v[28:31], a[132:135], v[228:231], v[28:31]\n"
"s_add_u32 s57, s57, 16384\n"
"v_exp_f32 v77, v77\n"
"v_mfma_f32_16x16x32_bf16 v[28:31], a[136:139], v[232:235], v[28:31]\n"
"ds_read_b128 a[204:207], v152 offset:16384\n"
"v_cvt_pk_bf16_f32 v60, v76, v77\n"
"v_exp_f32 v78, v78\n"
"v_mfma_f32_16x16x32_bf16 v[28:31], a[140:143], v[236:239], v[28:31]\n"
"v_exp_f32 v79, v79\n"
"s_nop 0\n"
"v_cvt_pk_bf16_f32 v61, v78, v79\n"
"s_nop 1\n"
"v_exp_f32 v16, v16\n"
"v_exp_f32 v17, v17\n"
"v_exp_f32 v18, v18\n"
"v_cvt_pk_bf16_f32 v38, v16, v17\n"
"v_exp_f32 v19, v19\n"
"v_exp_f32 v20, v20\n"
"v_cvt_pk_bf16_f32 v39, v18, v19\n"
"v_exp_f32 v21, v21\n"
"v_exp_f32 v22, v22\n"
"v_cvt_pk_bf16_f32 v46, v20, v21\n"
"v_exp_f32 v23, v23\n"
"v_exp_f32 v24, v24\n"
"v_cvt_pk_bf16_f32 v47, v22, v23\n"
"v_exp_f32 v25, v25\n"
"v_exp_f32 v26, v26\n"
"v_cvt_pk_bf16_f32 v54, v24, v25\n"
"v_exp_f32 v27, v27\n"
"s_nop 0\n"
"v_cvt_pk_bf16_f32 v55, v26, v27\n"
"s_waitcnt lgkmcnt(3)\n"
"v_mfma_f32_16x16x32_bf16 v[16:19], a[192:195], v[176:179], v[112:115]\n"
"s_waitcnt lgkmcnt(2)\n"
"v_exp_f32 v28, v28\n"
"v_mfma_f32_16x16x32_bf16 v[16:19], a[196:199], v[180:183], v[16:19]\n"
"s_waitcnt lgkmcnt(1)\n"
"ds_read_b64_tr_b16 v[96:97], v153 offset:0\n"
"v_mfma_f32_16x16x32_bf16 v[16:19], a[200:203], v[184:187], v[16:19]\n"
"s_waitcnt lgkmcnt(1)\n"
"v_exp_f32 v29, v29\n"
"v_mfma_f32_16x16x32_bf16 v[16:19], a[204:207], v[188:191], v[16:19]\n"
"ds_read_b64_tr_b16 v[98:99], v153 offset:4096\n"
"v_cvt_pk_bf16_f32 v62, v28, v29\n"
"v_mfma_f32_16x16x32_bf16 v[20:23], a[192:195], v[192:195], v[116:119]\n"
"v_exp_f32 v30, v30\n"
"v_mfma_f32_16x16x32_bf16 v[20:23], a[196:199], v[196:199], v[20:23]\n"
"ds_read_b64_tr_b16 v[100:101], v153 offset:8192\n"
"v_mfma_f32_16x16x32_bf16 v[20:23], a[200:203], v[200:203], v[20:23]\n"
"v_exp_f32 v31, v31\n"
"v_mfma_f32_16x16x32_bf16 v[20:23], a[204:207], v[204:207], v[20:23]\n"
"ds_read_b64_tr_b16 v[102:103], v153 offset:12288\n"
"v_cvt_pk_bf16_f32 v63, v30, v31\n"
"v_mfma_f32_16x16x32_bf16 v[24:27], a[192:195], v[208:211], v[120:123]\n"
"v_exp_f32 v16, v16\n"
"v_mfma_f32_16x16x32_bf16 v[24:27], a[196:199], v[212:215], v[24:27]\n"
"ds_read_b64_tr_b16 v[104:105], v154 offset:0\n"
"v_mfma_f32_16x16x32_bf16 v[24:27], a[200:203], v[216:219], v[24:27]\n"
"v_exp_f32 v17, v17\n"
"v_mfma_f32_16x16x32_bf16 v[24:27], a[204:207], v[220:223], v[24:27]\n"
"ds_read_b64_tr_b16 v[106:107], v154 offset:4096\n"
"v_cvt_pk_bf16_f32 v64, v16, v17\n"
"v_mfma_f32_16x16x32_bf16 v[28:31], a[192:195], v[224:227], v[124:127]\n"
"v_exp_f32 v18, v18\n"
"v_mfma_f32_16x16x32_bf16 v[28:31], a[196:199], v[228:231], v[28:31]\n"
"ds_read_b64_tr_b16 v[108:109], v154 offset:8192\n"
"global_load_dwordx2 v[14:15], v254, s[6:7] sc1\n"
"v_mfma_f32_16x16x32_bf16 v[28:31], a[200:203], v[232:235], v[28:31]\n"
"v_exp_f32 v19, v19\n"
"v_mfma_f32_16x16x32_bf16 v[28:31], a[204:207], v[236:239], v[28:31]\n"
"s_waitcnt lgkmcnt(3)\n"
"ds_read_b64_tr_b16 v[110:111], v154 offset:12288\n"
"v_cvt_pk_bf16_f32 v65, v18, v19\n"
"v_mfma_f32_16x16x32_bf16 a[0:3], v[96:99], v[32:35], 0\n"
"s_add_u32 m0, s64, 0\n"
"v_exp_f32 v20, v20\n"
"v_mfma_f32_16x16x32_bf16 a[0:3], v[100:103], v[36:39], a[0:3]\n"
"buffer_load_dwordx4 v161, s[36:39], s56 offen offset:0 lds\n"
"v_mfma_f32_16x16x32_bf16 a[4:7], v[96:99], v[40:43], 0\n"
"v_exp_f32 v21, v21\n"
"v_mfma_f32_16x16x32_bf16 a[4:7], v[100:103], v[44:47], a[4:7]\n"
"v_cvt_pk_bf16_f32 v72, v20, v21\n"
"v_mfma_f32_16x16x32_bf16 a[8:11], v[96:99], v[48:51], 0\n"
"v_exp_f32 v22, v22\n"
"v_mfma_f32_16x16x32_bf16 a[8:11], v[100:103], v[52:55], a[8:11]\n"
"buffer_load_dwordx4 v162, s[36:39], s56 offen offset:1024 lds\n"
"v_mfma_f32_16x16x32_bf16 a[12:15], v[96:99], v[56:59], 0\n"
"s_waitcnt lgkmcnt(0)\n"
"v_exp_f32 v23, v23\n"
"v_mfma_f32_16x16x32_bf16 a[12:15], v[100:103], v[60:63], a[12:15]\n"
"ds_read_b128 a[192:195], v149 offset:20480\n"
"v_cvt_pk_bf16_f32 v73, v22, v23\n"
"v_mfma_f32_16x16x32_bf16 a[16:19], v[104:107], v[32:35], 0\n"
"v_exp_f32 v24, v24\n"
"v_mfma_f32_16x16x32_bf16 a[16:19], v[108:111], v[36:39], a[16:19]\n"
"ds_read_b128 a[196:199], v150 offset:20480\n"
"v_mfma_f32_16x16x32_bf16 a[20:23], v[104:107], v[40:43], 0\n"
"v_exp_f32 v25, v25\n"
"v_mfma_f32_16x16x32_bf16 a[20:23], v[108:111], v[44:47], a[20:23]\n"
"ds_read_b128 a[200:203], v151 offset:20480\n"
"v_cvt_pk_bf16_f32 v80, v24, v25\n"
"v_mfma_f32_16x16x32_bf16 a[24:27], v[104:107], v[48:51], 0\n"
"v_exp_f32 v26, v26\n"
"v_mfma_f32_16x16x32_bf16 a[24:27], v[108:111], v[52:55], a[24:27]\n"
"ds_read_b128 a[204:207], v152 offset:20480\n"
"v_mfma_f32_16x16x32_bf16 a[28:31], v[104:107], v[56:59], 0\n"
"v_exp_f32 v27, v27\n"
"v_mfma_f32_16x16x32_bf16 a[28:31], v[108:111], v[60:63], a[28:31]\n"
"v_cvt_pk_bf16_f32 v81, v26, v27\n"
"v_smfmac_f32_16x16x64_bf16 v[128:131], v[144:147], v[32:39], v148\n"
"s_waitcnt lgkmcnt(3)\n"
"v_mfma_f32_16x16x32_bf16 v[16:19], a[192:195], v[176:179], v[112:115]\n"
"s_waitcnt lgkmcnt(2)\n"
"v_exp_f32 v28, v28\n"
"v_mfma_f32_16x16x32_bf16 v[16:19], a[196:199], v[180:183], v[16:19]\n"
"s_waitcnt lgkmcnt(1)\n"
"ds_read_b64_tr_b16 v[96:97], v155 offset:0\n"
"v_mfma_f32_16x16x32_bf16 v[16:19], a[200:203], v[184:187], v[16:19]\n"
"s_waitcnt lgkmcnt(1)\n"
"v_exp_f32 v29, v29\n"
"v_mfma_f32_16x16x32_bf16 v[16:19], a[204:207], v[188:191], v[16:19]\n"
"ds_read_b64_tr_b16 v[98:99], v155 offset:4096\n"
"v_cvt_pk_bf16_f32 v88, v28, v29\n"
"v_mfma_f32_16x16x32_bf16 v[20:23], a[192:195], v[192:195], v[116:119]\n"
"v_exp_f32 v30, v30\n"
"v_mfma_f32_16x16x32_bf16 v[20:23], a[196:199], v[196:199], v[20:23]\n"
"ds_read_b64_tr_b16 v[100:101], v155 offset:8192\n"
"v_mfma_f32_16x16x32_bf16 v[20:23], a[200:203], v[200:203], v[20:23]\n"
"v_exp_f32 v31, v31\n"
"v_mfma_f32_16x16x32_bf16 v[20:23], a[204:207], v[204:207], v[20:23]\n"
"ds_read_b64_tr_b16 v[102:103], v155 offset:12288\n"
"v_cvt_pk_bf16_f32 v89, v30, v31\n"
"v_mfma_f32_16x16x32_bf16 v[24:27], a[192:195], v[208:211], v[120:123]\n"
"v_exp_f32 v16, v16\n"
"v_mfma_f32_16x16x32_bf16 v[24:27], a[196:199], v[212:215], v[24:27]\n"
"ds_read_b64_tr_b16 v[104:105], v156 offset:0\n"
"v_mfma_f32_16x16x32_bf16 v[24:27], a[200:203], v[216:219], v[24:27]\n"
"v_exp_f32 v17, v17\n"
"v_mfma_f32_16x16x32_bf16 v[24:27], a[204:207], v[220:223], v[24:27]\n"
"ds_read_b64_tr_b16 v[106:107], v156 offset:4096\n"
"v_cvt_pk_bf16_f32 v66, v16, v17\n"
"v_mfma_f32_16x16x32_bf16 v[28:31], a[192:195], v[224:227], v[124:127]\n"
"v_exp_f32 v18, v18\n"
"v_mfma_f32_16x16x32_bf16 v[28:31], a[196:199], v[228:231], v[28:31]\n"
"ds_read_b64_tr_b16 v[108:109], v156 offset:8192\n"
"v_mfma_f32_16x16x32_bf16 v[28:31], a[200:203], v[232:235], v[28:31]\n"
"v_exp_f32 v19, v19\n"
"v_mfma_f32_16x16x32_bf16 v[28:31], a[204:207], v[236:239], v[28:31]\n"
"s_waitcnt lgkmcnt(3)\n"
"ds_read_b64_tr_b16 v[110:111], v156 offset:12288\n"
"v_cvt_pk_bf16_f32 v67, v18, v19\n"
"v_mfma_f32_16x16x32_bf16 a[32:35], v[96:99], v[32:35], 0\n"
"v_exp_f32 v20, v20\n"
"v_mfma_f32_16x16x32_bf16 a[32:35], v[100:103], v[36:39], a[32:35]\n"
"buffer_load_dwordx4 v163, s[36:39], s56 offen offset:2048 lds\n"
"v_mfma_f32_16x16x32_bf16 a[36:39], v[96:99], v[40:43], 0\n"
"v_exp_f32 v21, v21\n"
"v_mfma_f32_16x16x32_bf16 a[36:39], v[100:103], v[44:47], a[36:39]\n"
"v_cvt_pk_bf16_f32 v74, v20, v21\n"
"v_mfma_f32_16x16x32_bf16 a[40:43], v[96:99], v[48:51], 0\n"
"v_exp_f32 v22, v22\n"
"v_mfma_f32_16x16x32_bf16 a[40:43], v[100:103], v[52:55], a[40:43]\n"
"buffer_load_dwordx4 v164, s[36:39], s56 offen offset:3072 lds\n"
"v_mfma_f32_16x16x32_bf16 a[44:47], v[96:99], v[56:59], 0\n"
"s_waitcnt lgkmcnt(0)\n"
"v_exp_f32 v23, v23\n"
"v_mfma_f32_16x16x32_bf16 a[44:47], v[100:103], v[60:63], a[44:47]\n"
"ds_read_b128 a[192:195], v149 offset:24576\n"
"v_cvt_pk_bf16_f32 v75, v22, v23\n"
"v_mfma_f32_16x16x32_bf16 a[48:51], v[104:107], v[32:35], 0\n"
"v_exp_f32 v24, v24\n"
"v_mfma_f32_16x16x32_bf16 a[48:51], v[108:111], v[36:39], a[48:51]\n"
"ds_read_b128 a[196:199], v150 offset:24576\n"
"v_mfma_f32_16x16x32_bf16 a[52:55], v[104:107], v[40:43], 0\n"
"v_exp_f32 v25, v25\n"
"v_mfma_f32_16x16x32_bf16 a[52:55], v[108:111], v[44:47], a[52:55]\n"
"ds_read_b128 a[200:203], v151 offset:24576\n"
"v_cvt_pk_bf16_f32 v82, v24, v25\n"
"v_mfma_f32_16x16x32_bf16 a[56:59], v[104:107], v[48:51], 0\n"
"v_exp_f32 v26, v26\n"
"v_mfma_f32_16x16x32_bf16 a[56:59], v[108:111], v[52:55], a[56:59]\n"
"ds_read_b128 a[204:207], v152 offset:24576\n"
"v_mfma_f32_16x16x32_bf16 a[60:63], v[104:107], v[56:59], 0\n"
"v_exp_f32 v27, v27\n"
"v_mfma_f32_16x16x32_bf16 a[60:63], v[108:111], v[60:63], a[60:63]\n"
"v_cvt_pk_bf16_f32 v83, v26, v27\n"
"v_smfmac_f32_16x16x64_bf16 v[132:135], v[144:147], v[40:47], v148\n"
"s_waitcnt lgkmcnt(3)\n"
"v_mfma_f32_16x16x32_bf16 v[16:19], a[192:195], v[176:179], v[112:115]\n"
"s_waitcnt lgkmcnt(2)\n"
"v_exp_f32 v28, v28\n"
"v_mfma_f32_16x16x32_bf16 v[16:19], a[196:199], v[180:183], v[16:19]\n"
"s_waitcnt lgkmcnt(1)\n"
"ds_read_b64_tr_b16 v[96:97], v157 offset:0\n"
"v_mfma_f32_16x16x32_bf16 v[16:19], a[200:203], v[184:187], v[16:19]\n"
"s_waitcnt lgkmcnt(1)\n"
"v_exp_f32 v29, v29\n"
"v_mfma_f32_16x16x32_bf16 v[16:19], a[204:207], v[188:191], v[16:19]\n"
"ds_read_b64_tr_b16 v[98:99], v157 offset:4096\n"
"v_cvt_pk_bf16_f32 v90, v28, v29\n"
"v_mfma_f32_16x16x32_bf16 v[20:23], a[192:195], v[192:195], v[116:119]\n"
"v_exp_f32 v30, v30\n"
"v_mfma_f32_16x16x32_bf16 v[20:23], a[196:199], v[196:199], v[20:23]\n"
"ds_read_b64_tr_b16 v[100:101], v157 offset:8192\n"
"v_mfma_f32_16x16x32_bf16 v[20:23], a[200:203], v[200:203], v[20:23]\n"
"v_exp_f32 v31, v31\n"
"v_mfma_f32_16x16x32_bf16 v[20:23], a[204:207], v[204:207], v[20:23]\n"
"ds_read_b64_tr_b16 v[102:103], v157 offset:12288\n"
"v_cvt_pk_bf16_f32 v91, v30, v31\n"
"v_mfma_f32_16x16x32_bf16 v[24:27], a[192:195], v[208:211], v[120:123]\n"
"v_exp_f32 v16, v16\n"
"v_mfma_f32_16x16x32_bf16 v[24:27], a[196:199], v[212:215], v[24:27]\n"
"ds_read_b64_tr_b16 v[104:105], v158 offset:0\n"
"v_mfma_f32_16x16x32_bf16 v[24:27], a[200:203], v[216:219], v[24:27]\n"
"v_exp_f32 v17, v17\n"
"v_mfma_f32_16x16x32_bf16 v[24:27], a[204:207], v[220:223], v[24:27]\n"
"ds_read_b64_tr_b16 v[106:107], v158 offset:4096\n"
"v_cvt_pk_bf16_f32 v68, v16, v17\n"
"v_mfma_f32_16x16x32_bf16 v[28:31], a[192:195], v[224:227], v[124:127]\n"
"v_exp_f32 v18, v18\n"
"v_mfma_f32_16x16x32_bf16 v[28:31], a[196:199], v[228:231], v[28:31]\n"
"ds_read_b64_tr_b16 v[108:109], v158 offset:8192\n"
"v_mfma_f32_16x16x32_bf16 v[28:31], a[200:203], v[232:235], v[28:31]\n"
"v_exp_f32 v19, v19\n"
"v_mfma_f32_16x16x32_bf16 v[28:31], a[204:207], v[236:239], v[28:31]\n"
"s_waitcnt lgkmcnt(3)\n"
"ds_read_b64_tr_b16 v[110:111], v158 offset:12288\n"
"v_cvt_pk_bf16_f32 v69, v18, v19\n"
"v_mfma_f32_16x16x32_bf16 a[64:67], v[96:99], v[32:35], 0\n"
"s_add_u32 m0, s65, 49152\n"
"v_exp_f32 v20, v20\n"
"v_mfma_f32_16x16x32_bf16 a[64:67], v[100:103], v[36:39], a[64:67]\n"
"buffer_load_dwordx4 v165, s[40:43], s57 offen offset:0 lds\n"
"v_mfma_f32_16x16x32_bf16 a[68:71], v[96:99], v[40:43], 0\n"
"v_exp_f32 v21, v21\n"
"v_mfma_f32_16x16x32_bf16 a[68:71], v[100:103], v[44:47], a[68:71]\n"
"v_cvt_pk_bf16_f32 v76, v20, v21\n"
"v_mfma_f32_16x16x32_bf16 a[72:75], v[96:99], v[48:51], 0\n"
"v_exp_f32 v22, v22\n"
"v_mfma_f32_16x16x32_bf16 a[72:75], v[100:103], v[52:55], a[72:75]\n"
"buffer_load_dwordx4 v166, s[40:43], s57 offen offset:1024 lds\n"
"v_mfma_f32_16x16x32_bf16 a[76:79], v[96:99], v[56:59], 0\n"
"s_waitcnt lgkmcnt(0)\n"
"v_exp_f32 v23, v23\n"
"v_mfma_f32_16x16x32_bf16 a[76:79], v[100:103], v[60:63], a[76:79]\n"
"ds_read_b128 a[192:195], v149 offset:28672\n"
"v_cvt_pk_bf16_f32 v77, v22, v23\n"
"v_mfma_f32_16x16x32_bf16 a[80:83], v[104:107], v[32:35], 0\n"
"v_exp_f32 v24, v24\n"
"v_mfma_f32_16x16x32_bf16 a[80:83], v[108:111], v[36:39], a[80:83]\n"
"ds_read_b128 a[196:199], v150 offset:28672\n"
"v_mfma_f32_16x16x32_bf16 a[84:87], v[104:107], v[40:43], 0\n"
"v_exp_f32 v25, v25\n"
"v_mfma_f32_16x16x32_bf16 a[84:87], v[108:111], v[44:47], a[84:87]\n"
"ds_read_b128 a[200:203], v151 offset:28672\n"
"v_cvt_pk_bf16_f32 v84, v24, v25\n"
"v_mfma_f32_16x16x32_bf16 a[88:91], v[104:107], v[48:51], 0\n"
"v_exp_f32 v26, v26\n"
"v_mfma_f32_16x16x32_bf16 a[88:91], v[108:111], v[52:55], a[88:91]\n"
"ds_read_b128 a[204:207], v152 offset:28672\n"
"v_mfma_f32_16x16x32_bf16 a[92:95], v[104:107], v[56:59], 0\n"
"v_exp_f32 v27, v27\n"
"v_mfma_f32_16x16x32_bf16 a[92:95], v[108:111], v[60:63], a[92:95]\n"
"v_cvt_pk_bf16_f32 v85, v26, v27\n"
"v_smfmac_f32_16x16x64_bf16 v[136:139], v[144:147], v[48:55], v148\n"
"s_waitcnt lgkmcnt(3)\n"
"v_mfma_f32_16x16x32_bf16 v[16:19], a[192:195], v[176:179], v[112:115]\n"
"s_waitcnt lgkmcnt(2)\n"
"v_exp_f32 v28, v28\n"
"v_mfma_f32_16x16x32_bf16 v[16:19], a[196:199], v[180:183], v[16:19]\n"
"s_waitcnt lgkmcnt(1)\n"
"ds_read_b64_tr_b16 v[96:97], v159 offset:0\n"
"v_mfma_f32_16x16x32_bf16 v[16:19], a[200:203], v[184:187], v[16:19]\n"
"s_waitcnt lgkmcnt(1)\n"
"v_exp_f32 v29, v29\n"
"v_mfma_f32_16x16x32_bf16 v[16:19], a[204:207], v[188:191], v[16:19]\n"
"ds_read_b64_tr_b16 v[98:99], v159 offset:4096\n"
"v_cvt_pk_bf16_f32 v92, v28, v29\n"
"v_mfma_f32_16x16x32_bf16 v[20:23], a[192:195], v[192:195], v[116:119]\n"
"v_exp_f32 v30, v30\n"
"v_mfma_f32_16x16x32_bf16 v[20:23], a[196:199], v[196:199], v[20:23]\n"
"ds_read_b64_tr_b16 v[100:101], v159 offset:8192\n"
"v_mfma_f32_16x16x32_bf16 v[20:23], a[200:203], v[200:203], v[20:23]\n"
"v_exp_f32 v31, v31\n"
"v_mfma_f32_16x16x32_bf16 v[20:23], a[204:207], v[204:207], v[20:23]\n"
"ds_read_b64_tr_b16 v[102:103], v159 offset:12288\n"
"v_cvt_pk_bf16_f32 v93, v30, v31\n"
"v_mfma_f32_16x16x32_bf16 v[24:27], a[192:195], v[208:211], v[120:123]\n"
"v_exp_f32 v16, v16\n"
"v_mfma_f32_16x16x32_bf16 v[24:27], a[196:199], v[212:215], v[24:27]\n"
"ds_read_b64_tr_b16 v[104:105], v160 offset:0\n"
"v_mfma_f32_16x16x32_bf16 v[24:27], a[200:203], v[216:219], v[24:27]\n"
"v_exp_f32 v17, v17\n"
"v_mfma_f32_16x16x32_bf16 v[24:27], a[204:207], v[220:223], v[24:27]\n"
"ds_read_b64_tr_b16 v[106:107], v160 offset:4096\n"
"v_cvt_pk_bf16_f32 v70, v16, v17\n"
"v_mfma_f32_16x16x32_bf16 v[28:31], a[192:195], v[224:227], v[124:127]\n"
"v_exp_f32 v18, v18\n"
"v_mfma_f32_16x16x32_bf16 v[28:31], a[196:199], v[228:231], v[28:31]\n"
"ds_read_b64_tr_b16 v[108:109], v160 offset:8192\n"
"v_mfma_f32_16x16x32_bf16 v[28:31], a[200:203], v[232:235], v[28:31]\n"
"v_exp_f32 v19, v19\n"
"v_mfma_f32_16x16x32_bf16 v[28:31], a[204:207], v[236:239], v[28:31]\n"
"s_waitcnt lgkmcnt(3)\n"
"ds_read_b64_tr_b16 v[110:111], v160 offset:12288\n"
"v_cvt_pk_bf16_f32 v71, v18, v19\n"
"v_mfma_f32_16x16x32_bf16 a[96:99], v[96:99], v[32:35], 0\n"
"v_exp_f32 v20, v20\n"
"v_mfma_f32_16x16x32_bf16 a[96:99], v[100:103], v[36:39], a[96:99]\n"
"buffer_load_dwordx4 v165, s[40:43], s57 offen offset:2048 lds\n"
"v_mfma_f32_16x16x32_bf16 a[100:103], v[96:99], v[40:43], 0\n"
"v_exp_f32 v21, v21\n"
"v_mfma_f32_16x16x32_bf16 a[100:103], v[100:103], v[44:47], a[100:103]\n"
"v_cvt_pk_bf16_f32 v78, v20, v21\n"
"v_mfma_f32_16x16x32_bf16 a[104:107], v[96:99], v[48:51], 0\n"
"v_exp_f32 v22, v22\n"
"v_mfma_f32_16x16x32_bf16 a[104:107], v[100:103], v[52:55], a[104:107]\n"
"buffer_load_dwordx4 v166, s[40:43], s57 offen offset:3072 lds\n"
"v_mfma_f32_16x16x32_bf16 a[108:111], v[96:99], v[56:59], 0\n"
"s_waitcnt vmcnt(16) lgkmcnt(0)\n"
"v_exp_f32 v23, v23\n"
"v_mfma_f32_16x16x32_bf16 a[108:111], v[100:103], v[60:63], a[108:111]\n"
"s_barrier\n"
"ds_read_b128 a[192:195], v149 offset:32768\n"
"v_cvt_pk_bf16_f32 v79, v22, v23\n"
"v_mfma_f32_16x16x32_bf16 a[112:115], v[104:107], v[32:35], 0\n"
"s_add_u32 s56, s56, 16384\n"
"v_exp_f32 v24, v24\n"
"v_mfma_f32_16x16x32_bf16 a[112:115], v[108:111], v[36:39], a[112:115]\n"
"ds_read_b128 a[196:199], v150 offset:32768\n"
"v_mfma_f32_16x16x32_bf16 a[116:119], v[104:107], v[40:43], 0\n"
"s_add_u32 s57, s57, 16384\n"
"v_exp_f32 v25, v25\n"
"v_mfma_f32_16x16x32_bf16 a[116:119], v[108:111], v[44:47], a[116:119]\n"
"ds_read_b128 a[200:203], v151 offset:32768\n"
"v_cvt_pk_bf16_f32 v86, v24, v25\n"
"v_mfma_f32_16x16x32_bf16 a[120:123], v[104:107], v[48:51], 0\n"
"v_exp_f32 v26, v26\n"
"v_mfma_f32_16x16x32_bf16 a[120:123], v[108:111], v[52:55], a[120:123]\n"
"ds_read_b128 a[204:207], v152 offset:32768\n"
"v_mfma_f32_16x16x32_bf16 a[124:127], v[104:107], v[56:59], 0\n"
"v_exp_f32 v27, v27\n"
"v_mfma_f32_16x16x32_bf16 a[124:127], v[108:111], v[60:63], a[124:127]\n"
"s_waitcnt vmcnt(8)\n"
"v_cmp_ne_u64 vcc, s[34:35], v[14:15]\n"
"s_nop 0\n"
"s_cmp_eq_u64 vcc, 0\n"
"s_cbranch_scc0 L_slow_pollH_%=\n"
"L_back_pollH_%=:\n"
"v_cvt_pk_bf16_f32 v87, v26, v27\n"
"v_smfmac_f32_16x16x64_bf16 v[140:143], v[144:147], v[56:63], v148\n"
"s_waitcnt lgkmcnt(3)\n"
"s_mov_b32 s63, 7\n"
"s_movk_i32 s68, 3456\n"
"s_mov_b32 s99, 0x20000\n"
".p2align 6\n"
"L_loop_%=:\n"
"v_mfma_f32_16x16x32_bf16 v[16:19], a[192:195], v[176:179], v[112:115]\n"
"s_waitcnt lgkmcnt(2)\n"
"v_exp_f32 v28, v28\n"
"v_mfma_f32_16x16x32_bf16 v[16:19], a[196:199], v[180:183], v[16:19]\n"
"s_waitcnt lgkmcnt(1)\n"
"ds_read_b64_tr_b16 v[96:97], v153 offset:16384\n"
"v_mfma_f32_16x16x32_bf16 v[16:19], a[200:203], v[184:187], v[16:19]\n"
"s_waitcnt lgkmcnt(1)\n"
"v_exp_f32 v29, v29\n"
"v_mfma_f32_16x16x32_bf16 v[16:19], a[204:207], v[188:191], v[16:19]\n"
"ds_read_b64_tr_b16 v[98:99], v153 offset:20480\n"
"v_cvt_pk_bf16_f32 v94, v28, v29\n"
"v_mfma_f32_16x16x32_bf16 v[20:23], a[192:195], v[192:195], v[116:119]\n"
"s_cmp_gt_u32 s63, 3\n"
"s_cselect_b32 s96, s16, s18\n"
"s_cselect_b32 s97, s17, s19\n"
"s_cselect_b32 s98, 2560, s68\n"
"v_exp_f32 v30, v30\n"
"v_mfma_f32_16x16x32_bf16 v[20:23], a[196:199], v[196:199], v[20:23]\n"
"ds_read_b64_tr_b16 v[100:101], v153 offset:24576\n"
"v_mfma_f32_16x16x32_bf16 v[20:23], a[200:203], v[200:203], v[20:23]\n"
"s_cmp_gt_u32 s63, 5\n"
"s_cselect_b32 s96, s22, s96\n"
"s_cselect_b32 s97, s23, s97\n"
"s_cselect_b32 s98, s68, s98\n"
"v_exp_f32 v31, v31\n"
"v_mfma_f32_16x16x32_bf16 v[20:23], a[204:207], v[204:207], v[20:23]\n"
"ds_read_b64_tr_b16 v[102:103], v153 offset:28672\n"
"v_cvt_pk_bf16_f32 v95, v30, v31\n"
"v_mfma_f32_16x16x32_bf16 v[24:27], a[192:195], v[208:211], v[120:123]\n"
"s_and_b32 s69, s63, 1\n"
"v_exp_f32 v16, v16\n"
"v_mfma_f32_16x16x32_bf16 v[24:27], a[196:199], v[212:215], v[24:27]\n"
"ds_read_b64_tr_b16 v[104:105], v154 offset:16384\n"
"v_mfma_f32_16x16x32_bf16 v[24:27], a[200:203], v[216:219], v[24:27]\n"
"s_sub_u32 s69, s62, s69\n"
"v_exp_f32 v17, v17\n"
"v_mfma_f32_16x16x32_bf16 v[24:27], a[204:207], v[220:223], v[24:27]\n"
"ds_read_b64_tr_b16 v[106:107], v154 offset:20480\n"
"v_cvt_pk_bf16_f32 v32, v16, v17\n"
"v_mfma_f32_16x16x32_bf16 v[28:31], a[192:195], v[224:227], v[124:127]\n"
"s_mul_i32 s69, s69, s98\n"
"v_exp_f32 v18, v18\n"
"v_mfma_f32_16x16x32_bf16 v[28:31], a[196:199], v[228:231], v[28:31]\n"
"ds_read_b64_tr_b16 v[108:109], v154 offset:24576\n"
"v_mfma_f32_16x16x32_bf16 v[28:31], a[200:203], v[232:235], v[28:31]\n"
"s_add_u32 s96, s96, s69\n"
"s_addc_u32 s97, s97, 0\n"
"v_exp_f32 v19, v19\n"
"v_mfma_f32_16x16x32_bf16 v[28:31], a[204:207], v[236:239], v[28:31]\n"
"s_waitcnt lgkmcnt(3)\n"
"ds_read_b64_tr_b16 v[110:111], v154 offset:28672\n"
"v_cvt_pk_bf16_f32 v33, v18, v19\n"
"v_mfma_f32_16x16x32_bf16 a[0:3], v[96:99], v[64:67], a[0:3]\n"
"s_add_u32 m0, s64, 16384\n"
"v_exp_f32 v20, v20\n"
"v_mfma_f32_16x16x32_bf16 a[0:3], v[100:103], v[68:71], a[0:3]\n"
"buffer_load_dwordx4 v161, s[36:39], s56 offen offset:0 lds\n"
"v_mfma_f32_16x16x32_bf16 a[4:7], v[96:99], v[72:75], a[4:7]\n"
"s_cmp_eq_u32 s63, 1\n"
"s_cselect_b32 s98, 0, s98\n"
"v_exp_f32 v21, v21\n"
"v_mfma_f32_16x16x32_bf16 a[4:7], v[100:103], v[76:79], a[4:7]\n"
"v_cvt_pk_bf16_f32 v40, v20, v21\n"
"v_mfma_f32_16x16x32_bf16 a[8:11], v[96:99], v[80:83], a[8:11]\n"
"v_exp_f32 v22, v22\n"
"v_mfma_f32_16x16x32_bf16 a[8:11], v[100:103], v[84:87], a[8:11]\n"
"buffer_load_dwordx4 v162, s[36:39], s56 offen offset:1024 lds\n"
"v_mfma_f32_16x16x32_bf16 a[12:15], v[96:99], v[88:91], a[12:15]\n"
"s_waitcnt lgkmcnt(0)\n"
"v_exp_f32 v23, v23\n"
"v_mfma_f32_16x16x32_bf16 a[12:15], v[100:103], v[92:95], a[12:15]\n"
"ds_read_b128 a[192:195], v149 offset:36864\n"
"v_cvt_pk_bf16_f32 v41, v22, v23\n"
"v_mfma_f32_16x16x32_bf16 a[16:19], v[104:107], v[64:67], a[16:19]\n"
"v_exp_f32 v24, v24\n"
"v_mfma_f32_16x16x32_bf16 a[16:19], v[108:111], v[68:71], a[16:19]\n"
"ds_read_b128 a[196:199], v150 offset:36864\n"
"v_mfma_f32_16x16x32_bf16 a[20:23], v[104:107], v[72:75], a[20:23]\n"
"v_exp_f32 v25, v25\n"
"v_mfma_f32_16x16x32_bf16 a[20:23], v[108:111], v[76:79], a[20:23]\n"
"ds_read_b128 a[200:203], v151 offset:36864\n"
"v_cvt_pk_bf16_f32 v48, v24, v25\n"
"v_mfma_f32_16x16x32_bf16 a[24:27], v[104:107], v[80:83], a[24:27]\n"
"v_exp_f32 v26, v26\n"
"v_mfma_f32_16x16x32_bf16 a[24:27], v[108:111], v[84:87], a[24:27]\n"
"ds_read_b128 a[204:207], v152 offset:36864\n"
"v_mfma_f32_16x16x32_bf16 a[28:31], v[104:107], v[88:91], a[28:31]\n"
"v_exp_f32 v27, v27\n"
"v_mfma_f32_16x16x32_bf16 a[28:31], v[108:111], v[92:95], a[28:31]\n"
"v_cvt_pk_bf16_f32 v49, v26, v27\n"
"v_smfmac_f32_16x16x64_bf16 v[128:131], v[144:147], v[64:71], v148\n"
"s_waitcnt lgkmcnt(3)\n"
"buffer_load_dword v1, v253, s[96:99], 0 offen\n"
"v_mfma_f32_16x16x32_bf16 v[16:19], a[192:195], v[176:179], v[112:115]\n"
"s_waitcnt lgkmcnt(2)\n"
"v_exp_f32 v28, v28\n"
"v_mfma_f32_16x16x32_bf16 v[16:19], a[196:199], v[180:183], v[16:19]\n"
"s_waitcnt lgkmcnt(1)\n"
"ds_read_b64_tr_b16 v[96:97], v155 offset:16384\n"
"v_mfma_f32_16x16x32_bf16 v[16:19], a[200:203], v[184:187], v[16:19]\n"
"s_waitcnt lgkmcnt(1)\n"
"v_exp_f32 v29, v29\n"
"v_mfma_f32_16x16x32_bf16 v[16:19], a[204:207], v[188:191], v[16:19]\n"
"ds_read_b64_tr_b16 v[98:99], v155 offset:20480\n"
"v_cvt_pk_bf16_f32 v56, v28, v29\n"
"v_mfma_f32_16x16x32_bf16 v[20:23], a[192:195], v[192:195], v[116:119]\n"
"v_exp_f32 v30, v30\n"
"v_mfma_f32_16x16x32_bf16 v[20:23], a[196:199], v[196:199], v[20:23]\n"
"ds_read_b64_tr_b16 v[100:101], v155 offset:24576\n"
"v_mfma_f32_16x16x32_bf16 v[20:23], a[200:203], v[200:203], v[20:23]\n"
"v_exp_f32 v31, v31\n"
"v_mfma_f32_16x16x32_bf16 v[20:23], a[204:207], v[204:207], v[20:23]\n"
"ds_read_b64_tr_b16 v[102:103], v155 offset:28672\n"
"v_cvt_pk_bf16_f32 v57, v30, v31\n"
"v_mfma_f32_16x16x32_bf16 v[24:27], a[192:195], v[208:211], v[120:123]\n"
"v_exp_f32 v16, v16\n"
"v_mfma_f32_16x16x32_bf16 v[24:27], a[196:199], v[212:215], v[24:27]\n"
"ds_read_b64_tr_b16 v[104:105], v156 offset:16384\n"
"v_mfma_f32_16x16x32_bf16 v[24:27], a[200:203], v[216:219], v[24:27]\n"
"v_exp_f32 v17, v17\n"
"v_mfma_f32_16x16x32_bf16 v[24:27], a[204:207], v[220:223], v[24:27]\n"
"ds_read_b64_tr_b16 v[106:107], v156 offset:20480\n"
"v_cvt_pk_bf16_f32 v34, v16, v17\n"
"v_mfma_f32_16x16x32_bf16 v[28:31], a[192:195], v[224:227], v[124:127]\n"
"v_exp_f32 v18, v18\n"
"v_mfma_f32_16x16x32_bf16 v[28:31], a[196:199], v[228:231], v[28:31]\n"
"ds_read_b64_tr_b16 v[108:109], v156 offset:24576\n"
"v_mfma_f32_16x16x32_bf16 v[28:31], a[200:203], v[232:235], v[28:31]\n"
"v_exp_f32 v19, v19\n"
"v_mfma_f32_16x16x32_bf16 v[28:31], a[204:207], v[236:239], v[28:31]\n"
"s_waitcnt lgkmcnt(3)\n"
"ds_read_b64_tr_b16 v[110:111], v156 offset:28672\n"
"v_cvt_pk_bf16_f32 v35, v18, v19\n"
"v_mfma_f32_16x16x32_bf16 a[32:35], v[96:99], v[64:67], a[32:35]\n"
"v_exp_f32 v20, v20\n"
"v_mfma_f32_16x16x32_bf16 a[32:35], v[100:103], v[68:71], a[32:35]\n"
"buffer_load_dwordx4 v163, s[36:39], s56 offen offset:2048 lds\n"
"v_mfma_f32_16x16x32_bf16 a[36:39], v[96:99], v[72:75], a[36:39]\n"
"v_exp_f32 v21, v21\n"
"v_mfma_f32_16x16x32_bf16 a[36:39], v[100:103], v[76:79], a[36:39]\n"
"v_cvt_pk_bf16_f32 v42, v20, v21\n"
"v_mfma_f32_16x16x32_bf16 a[40:43], v[96:99], v[80:83], a[40:43]\n"
"v_exp_f32 v22, v22\n"
"v_mfma_f32_16x16x32_bf16 a[40:43], v[100:103], v[84:87], a[40:43]\n"
"buffer_load_dwordx4 v164, s[36:39], s56 offen offset:3072 lds\n"
"v_mfma_f32_16x16x32_bf16 a[44:47], v[96:99], v[88:91], a[44:47]\n"
"s_waitcnt lgkmcnt(0)\n"
"v_exp_f32 v23, v23\n"
"v_mfma_f32_16x16x32_bf16 a[44:47], v[100:103], v[92:95], a[44:47]\n"
"ds_read_b128 a[192:195], v149 offset:40960\n"
"v_cvt_pk_bf16_f32 v43, v22, v23\n"
"v_mfma_f32_16x16x32_bf16 a[48:51], v[104:107], v[64:67], a[48:51]\n"
"v_exp_f32 v24, v24\n"
"v_mfma_f32_16x16x32_bf16 a[48:51], v[108:111], v[68:71], a[48:51]\n"
"ds_read_b128 a[196:199], v150 offset:40960\n"
"v_mfma_f32_16x16x32_bf16 a[52:55], v[104:107], v[72:75], a[52:55]\n"
"v_exp_f32 v25, v25\n"
"v_mfma_f32_16x16x32_bf16 a[52:55], v[108:111], v[76:79], a[52:55]\n"
"ds_read_b128 a[200:203], v151 offset:40960\n"
"v_cvt_pk_bf16_f32 v50, v24, v25\n"
"v_mfma_f32_16x16x32_bf16 a[56:59], v[104:107], v[80:83], a[56:59]\n"
"v_exp_f32 v26, v26\n"
"v_mfma_f32_16x16x32_bf16 a[56:59], v[108:111], v[84:87], a[56:59]\n"
"ds_read_b128 a[204:207], v152 offset:40960\n"
"v_mfma_f32_16x16x32_bf16 a[60:63], v[104:107], v[88:91], a[60:63]\n"
"v_exp_f32 v27, v27\n"
"v_mfma_f32_16x16x32_bf16 a[60:63], v[108:111], v[92:95], a[60:63]\n"
"v_cvt_pk_bf16_f32 v51, v26, v27\n"
"v_smfmac_f32_16x16x64_bf16 v[132:135], v[144:147], v[72:79], v148\n"
"s_waitcnt lgkmcnt(3)\n"
"v_mfma_f32_16x16x32_bf16 v[16:19], a[192:195], v[176:179], v[112:115]\n"
"s_waitcnt lgkmcnt(2)\n"
"v_exp_f32 v28, v28\n"
"v_mfma_f32_16x16x32_bf16 v[16:19], a[196:199], v[180:183], v[16:19]\n"
"s_waitcnt lgkmcnt(1)\n"
"ds_read_b64_tr_b16 v[96:97], v157 offset:16384\n"
"v_mfma_f32_16x16x32_bf16 v[16:19], a[200:203], v[184:187], v[16:19]\n"
"s_waitcnt lgkmcnt(1)\n"
"v_exp_f32 v29, v29\n"
"v_mfma_f32_16x16x32_bf16 v[16:19], a[204:207], v[188:191], v[16:19]\n"
"ds_read_b64_tr_b16 v[98:99], v157 offset:20480\n"
"v_cvt_pk_bf16_f32 v58, v28, v29\n"
"v_mfma_f32_16x16x32_bf16 v[20:23], a[192:195], v[192:195], v[116:119]\n"
"v_exp_f32 v30, v30\n"
"v_mfma_f32_16x16x32_bf16 v[20:23], a[196:199], v[196:199], v[20:23]\n"
"ds_read_b64_tr_b16 v[100:101], v157 offset:24576\n"
"v_mfma_f32_16x16x32_bf16 v[20:23], a[200:203], v[200:203], v[20:23]\n"
"v_exp_f32 v31, v31\n"
"v_mfma_f32_16x16x32_bf16 v[20:23], a[204:207], v[204:207], v[20:23]\n"
"ds_read_b64_tr_b16 v[102:103], v157 offset:28672\n"
"v_cvt_pk_bf16_f32 v59, v30, v31\n"
"v_mfma_f32_16x16x32_bf16 v[24:27], a[192:195], v[208:211], v[120:123]\n"
"v_exp_f32 v16, v16\n"
"v_mfma_f32_16x16x32_bf16 v[24:27], a[196:199], v[212:215], v[24:27]\n"
"ds_read_b64_tr_b16 v[104:105], v158 offset:16384\n"
"v_mfma_f32_16x16x32_bf16 v[24:27], a[200:203], v[216:219], v[24:27]\n"
"v_exp_f32 v17, v17\n"
"v_mfma_f32_16x16x32_bf16 v[24:27], a[204:207], v[220:223], v[24:27]\n"
"ds_read_b64_tr_b16 v[106:107], v158 offset:20480\n"
"v_cvt_pk_bf16_f32 v36, v16, v17\n"
"v_mfma_f32_16x16x32_bf16 v[28:31], a[192:195], v[224:227], v[124:127]\n"
"v_exp_f32 v18, v18\n"
"v_mfma_f32_16x16x32_bf16 v[28:31], a[196:199], v[228:231], v[28:31]\n"
"ds_read_b64_tr_b16 v[108:109], v158 offset:24576\n"
"v_mfma_f32_16x16x32_bf16 v[28:31], a[200:203], v[232:235], v[28:31]\n"
"v_exp_f32 v19, v19\n"
"v_mfma_f32_16x16x32_bf16 v[28:31], a[204:207], v[236:239], v[28:31]\n"
"s_waitcnt lgkmcnt(3)\n"
"ds_read_b64_tr_b16 v[110:111], v158 offset:28672\n"
"v_cvt_pk_bf16_f32 v37, v18, v19\n"
"v_mfma_f32_16x16x32_bf16 a[64:67], v[96:99], v[64:67], a[64:67]\n"
"s_add_u32 m0, s65, 0\n"
"v_exp_f32 v20, v20\n"
"v_mfma_f32_16x16x32_bf16 a[64:67], v[100:103], v[68:71], a[64:67]\n"
"buffer_load_dwordx4 v165, s[40:43], s57 offen offset:0 lds\n"
"v_mfma_f32_16x16x32_bf16 a[68:71], v[96:99], v[72:75], a[68:71]\n"
"v_exp_f32 v21, v21\n"
"v_mfma_f32_16x16x32_bf16 a[68:71], v[100:103], v[76:79], a[68:71]\n"
"v_cvt_pk_bf16_f32 v44, v20, v21\n"
"v_mfma_f32_16x16x32_bf16 a[72:75], v[96:99], v[80:83], a[72:75]\n"
"v_exp_f32 v22, v22\n"
"v_mfma_f32_16x16x32_bf16 a[72:75], v[100:103], v[84:87], a[72:75]\n"
"buffer_load_dwordx4 v166, s[40:43], s57 offen offset:1024 lds\n"
"v_mfma_f32_16x16x32_bf16 a[76:79], v[96:99], v[88:91], a[76:79]\n"
"s_waitcnt lgkmcnt(0)\n"
"v_exp_f32 v23, v23\n"
"v_mfma_f32_16x16x32_bf16 a[76:79], v[100:103], v[92:95], a[76:79]\n"
"ds_read_b128 a[192:195], v149 offset:45056\n"
"v_cvt_pk_bf16_f32 v45, v22, v23\n"
"v_mfma_f32_16x16x32_bf16 a[80:83], v[104:107], v[64:67], a[80:83]\n"
"v_exp_f32 v24, v24\n"
"v_mfma_f32_16x16x32_bf16 a[80:83], v[108:111], v[68:71], a[80:83]\n"
"ds_read_b128 a[196:199], v150 offset:45056\n"
"v_mfma_f32_16x16x32_bf16 a[84:87], v[104:107], v[72:75], a[84:87]\n"
"v_exp_f32 v25, v25\n"
"v_mfma_f32_16x16x32_bf16 a[84:87], v[108:111], v[76:79], a[84:87]\n"
"ds_read_b128 a[200:203], v151 offset:45056\n"
"v_cvt_pk_bf16_f32 v52, v24, v25\n"
"v_mfma_f32_16x16x32_bf16 a[88:91], v[104:107], v[80:83], a[88:91]\n"
"v_exp_f32 v26, v26\n"
"v_mfma_f32_16x16x32_bf16 a[88:91], v[108:111], v[84:87], a[88:91]\n"
"ds_read_b128 a[204:207], v152 offset:45056\n"
"v_mfma_f32_16x16x32_bf16 a[92:95], v[104:107], v[88:91], a[92:95]\n"
"v_exp_f32 v27, v27\n"
"v_mfma_f32_16x16x32_bf16 a[92:95], v[108:111], v[92:95], a[92:95]\n"
"v_cvt_pk_bf16_f32 v53, v26, v27\n"
"v_smfmac_f32_16x16x64_bf16 v[136:139], v[144:147], v[80:87], v148\n"
"s_waitcnt lgkmcnt(3)\n"
"v_mfma_f32_16x16x32_bf16 v[16:19], a[192:195], v[176:179], v[112:115]\n"
"s_waitcnt lgkmcnt(2)\n"
"v_exp_f32 v28, v28\n"
"v_mfma_f32_16x16x32_bf16 v[16:19], a[196:199], v[180:183], v[16:19]\n"
"s_waitcnt lgkmcnt(1)\n"
"ds_read_b64_tr_b16 v[96:97], v159 offset:16384\n"
"v_mfma_f32_16x16x32_bf16 v[16:19], a[200:203], v[184:187], v[16:19]\n"
"s_waitcnt lgkmcnt(1)\n"
"v_exp_f32 v29, v29\n"
"v_mfma_f32_16x16x32_bf16 v[16:19], a[204:207], v[188:191], v[16:19]\n"
"ds_read_b64_tr_b16 v[98:99], v159 offset:20480\n"
"v_cvt_pk_bf16_f32 v60, v28, v29\n"
"v_mfma_f32_16x16x32_bf16 v[20:23], a[192:195], v[192:195], v[116:119]\n"
"v_exp_f32 v30, v30\n"
"v_mfma_f32_16x16x32_bf16 v[20:23], a[196:199], v[196:199], v[20:23]\n"
"ds_read_b64_tr_b16 v[100:101], v159 offset:24576\n"
"v_mfma_f32_16x16x32_bf16 v[20:23], a[200:203], v[200:203], v[20:23]\n"
"v_exp_f32 v31, v31\n"
"v_mfma_f32_16x16x32_bf16 v[20:23], a[204:207], v[204:207], v[20:23]\n"
"ds_read_b64_tr_b16 v[102:103], v159 offset:28672\n"
"v_cvt_pk_bf16_f32 v61, v30, v31\n"
"v_mfma_f32_16x16x32_bf16 v[24:27], a[192:195], v[208:211], v[120:123]\n"
"v_exp_f32 v16, v16\n"
"v_mfma_f32_16x16x32_bf16 v[24:27], a[196:199], v[212:215], v[24:27]\n"
"ds_read_b64_tr_b16 v[104:105], v160 offset:16384\n"
"v_mfma_f32_16x16x32_bf16 v[24:27], a[200:203], v[216:219], v[24:27]\n"
"v_exp_f32 v17, v17\n"
"v_mfma_f32_16x16x32_bf16 v[24:27], a[204:207], v[220:223], v[24:27]\n"
"ds_read_b64_tr_b16 v[106:107], v160 offset:20480\n"
"v_cvt_pk_bf16_f32 v38, v16, v17\n"
"v_mfma_f32_16x16x32_bf16 v[28:31], a[192:195], v[224:227], v[124:127]\n"
"v_exp_f32 v18, v18\n"
"v_mfma_f32_16x16x32_bf16 v[28:31], a[196:199], v[228:231], v[28:31]\n"
"ds_read_b64_tr_b16 v[108:109], v160 offset:24576\n"
"v_mfma_f32_16x16x32_bf16 v[28:31], a[200:203], v[232:235], v[28:31]\n"
"v_exp_f32 v19, v19\n"
"v_mfma_f32_16x16x32_bf16 v[28:31], a[204:207], v[236:239], v[28:31]\n"
"s_waitcnt lgkmcnt(3)\n"
"ds_read_b64_tr_b16 v[110:111], v160 offset:28672\n"
"v_cvt_pk_bf16_f32 v39, v18, v19\n"
"v_mfma_f32_16x16x32_bf16 a[96:99], v[96:99], v[64:67], a[96:99]\n"
"v_exp_f32 v20, v20\n"
"v_mfma_f32_16x16x32_bf16 a[96:99], v[100:103], v[68:71], a[96:99]\n"
"buffer_load_dwordx4 v165, s[40:43], s57 offen offset:2048 lds\n"
"v_mfma_f32_16x16x32_bf16 a[100:103], v[96:99], v[72:75], a[100:103]\n"
"v_exp_f32 v21, v21\n"
"v_mfma_f32_16x16x32_bf16 a[100:103], v[100:103], v[76:79], a[100:103]\n"
"v_cvt_pk_bf16_f32 v46, v20, v21\n"
"v_mfma_f32_16x16x32_bf16 a[104:107], v[96:99], v[80:83], a[104:107]\n"
"v_exp_f32 v22, v22\n"
"v_mfma_f32_16x16x32_bf16 a[104:107], v[100:103], v[84:87], a[104:107]\n"
"buffer_load_dwordx4 v166, s[40:43], s57 offen offset:3072 lds\n"
"v_mfma_f32_16x16x32_bf16 a[108:111], v[96:99], v[88:91], a[108:111]\n"
"s_waitcnt vmcnt(16) lgkmcnt(0)\n"
"v_exp_f32 v23, v23\n"
"v_mfma_f32_16x16x32_bf16 a[108:111], v[100:103], v[92:95], a[108:111]\n"
"s_barrier\n"
"ds_read_b128 a[192:195], v149 offset:49152\n"
"v_cvt_pk_bf16_f32 v47, v22, v23\n"
"v_mfma_f32_16x16x32_bf16 a[112:115], v[104:107], v[64:67], a[112:115]\n"
"s_add_u32 s56, s56, 16384\n"
"v_exp_f32 v24, v24\n"
"v_mfma_f32_16x16x32_bf16 a[112:115], v[108:111], v[68:71], a[112:115]\n"
"ds_read_b128 a[196:199], v150 offset:49152\n"
"v_mfma_f32_16x16x32_bf16 a[116:119], v[104:107], v[72:75], a[116:119]\n"
"s_add_u32 s57, s57, 16384\n"
"v_exp_f32 v25, v25\n"
"v_mfma_f32_16x16x32_bf16 a[116:119], v[108:111], v[76:79], a[116:119]\n"
"ds_read_b128 a[200:203], v151 offset:49152\n"
"v_cvt_pk_bf16_f32 v54, v24, v25\n"
"v_mfma_f32_16x16x32_bf16 a[120:123], v[104:107], v[80:83], a[120:123]\n"
"v_exp_f32 v26, v26\n"
"v_mfma_f32_16x16x32_bf16 a[120:123], v[108:111], v[84:87], a[120:123]\n"
"ds_read_b128 a[204:207], v152 offset:49152\n"
"v_mfma_f32_16x16x32_bf16 a[124:127], v[104:107], v[88:91], a[124:127]\n"
"v_exp_f32 v27, v27\n"
"v_mfma_f32_16x16x32_bf16 a[124:127], v[108:111], v[92:95], a[124:127]\n"
"v_cvt_pk_bf16_f32 v55, v26, v27\n"
"v_smfmac_f32_16x16x64_bf16 v[140:143], v[144:147], v[88:95], v148\n"
"s_waitcnt lgkmcnt(3)\n"
"v_mfma_f32_16x16x32_bf16 v[16:19], a[192:195], v[176:179], v[112:115]\n"
"s_waitcnt lgkmcnt(2)\n"
"v_exp_f32 v28, v28\n"
"v_mfma_f32_16x16x32_bf16 v[16:19], a[196:199], v[180:183], v[16:19]\n"
"s_waitcnt lgkmcnt(1)\n"
"ds_read_b64_tr_b16 v[96:97], v153 offset:32768\n"
"v_mfma_f32_16x16x32_bf16 v[16:19], a[200:203], v[184:187], v[16:19]\n"
"s_waitcnt lgkmcnt(1)\n"
"v_exp_f32 v29, v29\n"
"v_mfma_f32_16x16x32_bf16 v[16:19], a[204:207], v[188:191], v[16:19]\n"
"ds_read_b64_tr_b16 v[98:99], v153 offset:36864\n"
"v_cvt_pk_bf16_f32 v62, v28, v29\n"
"v_mfma_f32_16x16x32_bf16 v[20:23], a[192:195], v[192:195], v[116:119]\n"
"v_exp_f32 v30, v30\n"
"v_mfma_f32_16x16x32_bf16 v[20:23], a[196:199], v[196:199], v[20:23]\n"
"ds_read_b64_tr_b16 v[100:101], v153 offset:40960\n"
"v_mfma_f32_16x16x32_bf16 v[20:23], a[200:203], v[200:203], v[20:23]\n"
"v_exp_f32 v31, v31\n"
"v_mfma_f32_16x16x32_bf16 v[20:23], a[204:207], v[204:207], v[20:23]\n"
"ds_read_b64_tr_b16 v[102:103], v153 offset:45056\n"
"v_cvt_pk_bf16_f32 v63, v30, v31\n"
"v_mfma_f32_16x16x32_bf16 v[24:27], a[192:195], v[208:211], v[120:123]\n"
"v_exp_f32 v16, v16\n"
"v_mfma_f32_16x16x32_bf16 v[24:27], a[196:199], v[212:215], v[24:27]\n"
"ds_read_b64_tr_b16 v[104:105], v154 offset:32768\n"
"v_mfma_f32_16x16x32_bf16 v[24:27], a[200:203], v[216:219], v[24:27]\n"
"v_exp_f32 v17, v17\n"
"v_mfma_f32_16x16x32_bf16 v[24:27], a[204:207], v[220:223], v[24:27]\n"
"ds_read_b64_tr_b16 v[106:107], v154 offset:36864\n"
"v_cvt_pk_bf16_f32 v64, v16, v17\n"
"v_mfma_f32_16x16x32_bf16 v[28:31], a[192:195], v[224:227], v[124:127]\n"
"v_exp_f32 v18, v18\n"
"v_mfma_f32_16x16x32_bf16 v[28:31], a[196:199], v[228:231], v[28:31]\n"
"ds_read_b64_tr_b16 v[108:109], v154 offset:40960\n"
"v_mfma_f32_16x16x32_bf16 v[28:31], a[200:203], v[232:235], v[28:31]\n"
"v_exp_f32 v19, v19\n"
"v_mfma_f32_16x16x32_bf16 v[28:31], a[204:207], v[236:239], v[28:31]\n"
"s_waitcnt lgkmcnt(3)\n"
"ds_read_b64_tr_b16 v[110:111], v154 offset:45056\n"
"v_cvt_pk_bf16_f32 v65, v18, v19\n"
"v_mfma_f32_16x16x32_bf16 a[0:3], v[96:99], v[32:35], a[0:3]\n"
"s_add_u32 m0, s64, 32768\n"
"v_exp_f32 v20, v20\n"
"v_mfma_f32_16x16x32_bf16 a[0:3], v[100:103], v[36:39], a[0:3]\n"
"buffer_load_dwordx4 v161, s[36:39], s56 offen offset:0 lds\n"
"v_mfma_f32_16x16x32_bf16 a[4:7], v[96:99], v[40:43], a[4:7]\n"
"v_exp_f32 v21, v21\n"
"v_mfma_f32_16x16x32_bf16 a[4:7], v[100:103], v[44:47], a[4:7]\n"
"v_cvt_pk_bf16_f32 v72, v20, v21\n"
"v_mfma_f32_16x16x32_bf16 a[8:11], v[96:99], v[48:51], a[8:11]\n"
"v_exp_f32 v22, v22\n"
"v_mfma_f32_16x16x32_bf16 a[8:11], v[100:103], v[52:55], a[8:11]\n"
"buffer_load_dwordx4 v162, s[36:39], s56 offen offset:1024 lds\n"
"v_mfma_f32_16x16x32_bf16 a[12:15], v[96:99], v[56:59], a[12:15]\n"
"s_waitcnt lgkmcnt(0)\n"
"v_exp_f32 v23, v23\n"
"v_mfma_f32_16x16x32_bf16 a[12:15], v[100:103], v[60:63], a[12:15]\n"
"ds_read_b128 a[192:195], v149 offset:53248\n"
"v_cvt_pk_bf16_f32 v73, v22, v23\n"
"v_mfma_f32_16x16x32_bf16 a[16:19], v[104:107], v[32:35], a[16:19]\n"
"v_exp_f32 v24, v24\n"
"v_mfma_f32_16x16x32_bf16 a[16:19], v[108:111], v[36:39], a[16:19]\n"
"ds_read_b128 a[196:199], v150 offset:53248\n"
"v_mfma_f32_16x16x32_bf16 a[20:23], v[104:107], v[40:43], a[20:23]\n"
"v_exp_f32 v25, v25\n"
"v_mfma_f32_16x16x32_bf16 a[20:23], v[108:111], v[44:47], a[20:23]\n"
"ds_read_b128 a[200:203], v151 offset:53248\n"
"v_cvt_pk_bf16_f32 v80, v24, v25\n"
"v_mfma_f32_16x16x32_bf16 a[24:27], v[104:107], v[48:51], a[24:27]\n"
"v_exp_f32 v26, v26\n"
"v_mfma_f32_16x16x32_bf16 a[24:27], v[108:111], v[52:55], a[24:27]\n"
"ds_read_b128 a[204:207], v152 offset:53248\n"
"v_mfma_f32_16x16x32_bf16 a[28:31], v[104:107], v[56:59], a[28:31]\n"
"v_exp_f32 v27, v27\n"
"v_mfma_f32_16x16x32_bf16 a[28:31], v[108:111], v[60:63], a[28:31]\n"
"v_cvt_pk_bf16_f32 v81, v26, v27\n"
"v_smfmac_f32_16x16x64_bf16 v[128:131], v[144:147], v[32:39], v148\n"
"s_waitcnt lgkmcnt(3)\n"
"v_mfma_f32_16x16x32_bf16 v[16:19], a[192:195], v[176:179], v[112:115]\n"
"s_waitcnt lgkmcnt(2)\n"
"v_exp_f32 v28, v28\n"
"v_mfma_f32_16x16x32_bf16 v[16:19], a[196:199], v[180:183], v[16:19]\n"
"s_waitcnt lgkmcnt(1)\n"
"ds_read_b64_tr_b16 v[96:97], v155 offset:32768\n"
"v_mfma_f32_16x16x32_bf16 v[16:19], a[200:203], v[184:187], v[16:19]\n"
"s_waitcnt lgkmcnt(1)\n"
"v_exp_f32 v29, v29\n"
"v_mfma_f32_16x16x32_bf16 v[16:19], a[204:207], v[188:191], v[16:19]\n"
"ds_read_b64_tr_b16 v[98:99], v155 offset:36864\n"
"v_cvt_pk_bf16_f32 v88, v28, v29\n"
"v_mfma_f32_16x16x32_bf16 v[20:23], a[192:195], v[192:195], v[116:119]\n"
"v_exp_f32 v30, v30\n"
"v_mfma_f32_16x16x32_bf16 v[20:23], a[196:199], v[196:199], v[20:23]\n"
"ds_read_b64_tr_b16 v[100:101], v155 offset:40960\n"
"v_mfma_f32_16x16x32_bf16 v[20:23], a[200:203], v[200:203], v[20:23]\n"
"v_exp_f32 v31, v31\n"
"v_mfma_f32_16x16x32_bf16 v[20:23], a[204:207], v[204:207], v[20:23]\n"
"ds_read_b64_tr_b16 v[102:103], v155 offset:45056\n"
"v_cvt_pk_bf16_f32 v89, v30, v31\n"
"v_mfma_f32_16x16x32_bf16 v[24:27], a[192:195], v[208:211], v[120:123]\n"
"v_exp_f32 v16, v16\n"
"v_mfma_f32_16x16x32_bf16 v[24:27], a[196:199], v[212:215], v[24:27]\n"
"ds_read_b64_tr_b16 v[104:105], v156 offset:32768\n"
"v_mfma_f32_16x16x32_bf16 v[24:27], a[200:203], v[216:219], v[24:27]\n"
"v_exp_f32 v17, v17\n"
"v_mfma_f32_16x16x32_bf16 v[24:27], a[204:207], v[220:223], v[24:27]\n"
"ds_read_b64_tr_b16 v[106:107], v156 offset:36864\n"
"v_cvt_pk_bf16_f32 v66, v16, v17\n"
"v_mfma_f32_16x16x32_bf16 v[28:31], a[192:195], v[224:227], v[124:127]\n"
"v_exp_f32 v18, v18\n"
"v_mfma_f32_16x16x32_bf16 v[28:31], a[196:199], v[228:231], v[28:31]\n"
"ds_read_b64_tr_b16 v[108:109], v156 offset:40960\n"
"v_mfma_f32_16x16x32_bf16 v[28:31], a[200:203], v[232:235], v[28:31]\n"
"v_exp_f32 v19, v19\n"
"v_mfma_f32_16x16x32_bf16 v[28:31], a[204:207], v[236:239], v[28:31]\n"
"s_waitcnt lgkmcnt(3)\n"
"ds_read_b64_tr_b16 v[110:111], v156 offset:45056\n"
"v_cvt_pk_bf16_f32 v67, v18, v19\n"
"v_mfma_f32_16x16x32_bf16 a[32:35], v[96:99], v[32:35], a[32:35]\n"
"v_exp_f32 v20, v20\n"
"v_mfma_f32_16x16x32_bf16 a[32:35], v[100:103], v[36:39], a[32:35]\n"
"buffer_load_dwordx4 v163, s[36:39], s56 offen offset:2048 lds\n"
"v_mfma_f32_16x16x32_bf16 a[36:39], v[96:99], v[40:43], a[36:39]\n"
"v_exp_f32 v21, v21\n"
"v_mfma_f32_16x16x32_bf16 a[36:39], v[100:103], v[44:47], a[36:39]\n"
"v_cvt_pk_bf16_f32 v74, v20, v21\n"
"v_mfma_f32_16x16x32_bf16 a[40:43], v[96:99], v[48:51], a[40:43]\n"
"v_exp_f32 v22, v22\n"
"v_mfma_f32_16x16x32_bf16 a[40:43], v[100:103], v[52:55], a[40:43]\n"
"buffer_load_dwordx4 v164, s[36:39], s56 offen offset:3072 lds\n"
"v_mfma_f32_16x16x32_bf16 a[44:47], v[96:99], v[56:59], a[44:47]\n"
"s_waitcnt lgkmcnt(0)\n"
"v_exp_f32 v23, v23\n"
"v_mfma_f32_16x16x32_bf16 a[44:47], v[100:103], v[60:63], a[44:47]\n"
"ds_read_b128 a[192:195], v149 offset:57344\n"
"v_cvt_pk_bf16_f32 v75, v22, v23\n"
"v_mfma_f32_16x16x32_bf16 a[48:51], v[104:107], v[32:35], a[48:51]\n"
"v_exp_f32 v24, v24\n"
"v_mfma_f32_16x16x32_bf16 a[48:51], v[108:111], v[36:39], a[48:51]\n"
"ds_read_b128 a[196:199], v150 offset:57344\n"
"v_mfma_f32_16x16x32_bf16 a[52:55], v[104:107], v[40:43], a[52:55]\n"
"v_exp_f32 v25, v25\n"
"v_mfma_f32_16x16x32_bf16 a[52:55], v[108:111], v[44:47], a[52:55]\n"
"ds_read_b128 a[200:203], v151 offset:57344\n"
"v_cvt_pk_bf16_f32 v82, v24, v25\n"
"v_mfma_f32_16x16x32_bf16 a[56:59], v[104:107], v[48:51], a[56:59]\n"
"v_exp_f32 v26, v26\n"
"v_mfma_f32_16x16x32_bf16 a[56:59], v[108:111], v[52:55], a[56:59]\n"
"ds_read_b128 a[204:207], v152 offset:57344\n"
"v_mfma_f32_16x16x32_bf16 a[60:63], v[104:107], v[56:59], a[60:63]\n"
"v_exp_f32 v27, v27\n"
"v_mfma_f32_16x16x32_bf16 a[60:63], v[108:111], v[60:63], a[60:63]\n"
"v_cvt_pk_bf16_f32 v83, v26, v27\n"
"v_smfmac_f32_16x16x64_bf16 v[132:135], v[144:147], v[40:47], v148\n"
"s_waitcnt lgkmcnt(3)\n"
"v_mfma_f32_16x16x32_bf16 v[16:19], a[192:195], v[176:179], v[112:115]\n"
"s_waitcnt lgkmcnt(2)\n"
"v_exp_f32 v28, v28\n"
"v_mfma_f32_16x16x32_bf16 v[16:19], a[196:199], v[180:183], v[16:19]\n"
"s_waitcnt lgkmcnt(1)\n"
"ds_read_b64_tr_b16 v[96:97], v157 offset:32768\n"
"v_mfma_f32_16x16x32_bf16 v[16:19], a[200:203], v[184:187], v[16:19]\n"
"s_waitcnt lgkmcnt(1)\n"
"v_exp_f32 v29, v29\n"
"v_mfma_f32_16x16x32_bf16 v[16:19], a[204:207], v[188:191], v[16:19]\n"
"ds_read_b64_tr_b16 v[98:99], v157 offset:36864\n"
"v_cvt_pk_bf16_f32 v90, v28, v29\n"
"v_mfma_f32_16x16x32_bf16 v[20:23], a[192:195], v[192:195], v[116:119]\n"
"v_exp_f32 v30, v30\n"
"v_mfma_f32_16x16x32_bf16 v[20:23], a[196:199], v[196:199], v[20:23]\n"
"ds_read_b64_tr_b16 v[100:101], v157 offset:40960\n"
"v_mfma_f32_16x16x32_bf16 v[20:23], a[200:203], v[200:203], v[20:23]\n"
"v_exp_f32 v31, v31\n"
"v_mfma_f32_16x16x32_bf16 v[20:23], a[204:207], v[204:207], v[20:23]\n"
"ds_read_b64_tr_b16 v[102:103], v157 offset:45056\n"
"v_cvt_pk_bf16_f32 v91, v30, v31\n"
"v_mfma_f32_16x16x32_bf16 v[24:27], a[192:195], v[208:211], v[120:123]\n"
"v_exp_f32 v16, v16\n"
"v_mfma_f32_16x16x32_bf16 v[24:27], a[196:199], v[212:215], v[24:27]\n"
"ds_read_b64_tr_b16 v[104:105], v158 offset:32768\n"
"v_mfma_f32_16x16x32_bf16 v[24:27], a[200:203], v[216:219], v[24:27]\n"
"v_exp_f32 v17, v17\n"
"v_mfma_f32_16x16x32_bf16 v[24:27], a[204:207], v[220:223], v[24:27]\n"
"ds_read_b64_tr_b16 v[106:107], v158 offset:36864\n"
"v_cvt_pk_bf16_f32 v68, v16, v17\n"
"v_mfma_f32_16x16x32_bf16 v[28:31], a[192:195], v[224:227], v[124:127]\n"
"v_exp_f32 v18, v18\n"
"v_mfma_f32_16x16x32_bf16 v[28:31], a[196:199], v[228:231], v[28:31]\n"
"ds_read_b64_tr_b16 v[108:109], v158 offset:40960\n"
"v_mfma_f32_16x16x32_bf16 v[28:31], a[200:203], v[232:235], v[28:31]\n"
"v_exp_f32 v19, v19\n"
"v_mfma_f32_16x16x32_bf16 v[28:31], a[204:207], v[236:239], v[28:31]\n"
"s_waitcnt lgkmcnt(3)\n"
"ds_read_b64_tr_b16 v[110:111], v158 offset:45056\n"
"v_cvt_pk_bf16_f32 v69, v18, v19\n"
"v_mfma_f32_16x16x32_bf16 a[64:67], v[96:99], v[32:35], a[64:67]\n"
"s_add_u32 m0, s65, 16384\n"
"v_exp_f32 v20, v20\n"
"v_mfma_f32_16x16x32_bf16 a[64:67], v[100:103], v[36:39], a[64:67]\n"
"buffer_load_dwordx4 v165, s[40:43], s57 offen offset:0 lds\n"
"v_mfma_f32_16x16x32_bf16 a[68:71], v[96:99], v[40:43], a[68:71]\n"
"v_exp_f32 v21, v21\n"
"v_mfma_f32_16x16x32_bf16 a[68:71], v[100:103], v[44:47], a[68:71]\n"
"v_cvt_pk_bf16_f32 v76, v20, v21\n"
"v_mfma_f32_16x16x32_bf16 a[72:75], v[96:99], v[48:51], a[72:75]\n"
"v_exp_f32 v22, v22\n"
"v_mfma_f32_16x16x32_bf16 a[72:75], v[100:103], v[52:55], a[72:75]\n"
"buffer_load_dwordx4 v166, s[40:43], s57 offen offset:1024 lds\n"
"v_mfma_f32_16x16x32_bf16 a[76:79], v[96:99], v[56:59], a[76:79]\n"
"s_waitcnt lgkmcnt(0)\n"
"v_exp_f32 v23, v23\n"
"v_mfma_f32_16x16x32_bf16 a[76:79], v[100:103], v[60:63], a[76:79]\n"
"ds_read_b128 a[192:195], v149 offset:61440\n"
"v_cvt_pk_bf16_f32 v77, v22, v23\n"
"v_mfma_f32_16x16x32_bf16 a[80:83], v[104:107], v[32:35], a[80:83]\n"
"v_exp_f32 v24, v24\n"
"v_mfma_f32_16x16x32_bf16 a[80:83], v[108:111], v[36:39], a[80:83]\n"
"ds_read_b128 a[196:199], v150 offset:61440\n"
"v_mfma_f32_16x16x32_bf16 a[84:87], v[104:107], v[40:43], a[84:87]\n"
"v_exp_f32 v25, v25\n"
"v_mfma_f32_16x16x32_bf16 a[84:87], v[108:111], v[44:47], a[84:87]\n"
"ds_read_b128 a[200:203], v151 offset:61440\n"
"v_cvt_pk_bf16_f32 v84, v24, v25\n"
"v_mfma_f32_16x16x32_bf16 a[88:91], v[104:107], v[48:51], a[88:91]\n"
"v_exp_f32 v26, v26\n"
"v_mfma_f32_16x16x32_bf16 a[88:91], v[108:111], v[52:55], a[88:91]\n"
"ds_read_b128 a[204:207], v152 offset:61440\n"
"v_mfma_f32_16x16x32_bf16 a[92:95], v[104:107], v[56:59], a[92:95]\n"
"v_exp_f32 v27, v27\n"
"v_mfma_f32_16x16x32_bf16 a[92:95], v[108:111], v[60:63], a[92:95]\n"
"v_cvt_pk_bf16_f32 v85, v26, v27\n"
"v_smfmac_f32_16x16x64_bf16 v[136:139], v[144:147], v[48:55], v148\n"
"s_waitcnt lgkmcnt(3)\n"
"v_mfma_f32_16x16x32_bf16 v[16:19], a[192:195], v[176:179], v[112:115]\n"
"s_waitcnt lgkmcnt(2)\n"
"v_exp_f32 v28, v28\n"
"v_mfma_f32_16x16x32_bf16 v[16:19], a[196:199], v[180:183], v[16:19]\n"
"s_waitcnt lgkmcnt(1)\n"
"ds_read_b64_tr_b16 v[96:97], v159 offset:32768\n"
"v_mfma_f32_16x16x32_bf16 v[16:19], a[200:203], v[184:187], v[16:19]\n"
"s_waitcnt lgkmcnt(1)\n"
"v_exp_f32 v29, v29\n"
"v_mfma_f32_16x16x32_bf16 v[16:19], a[204:207], v[188:191], v[16:19]\n"
"ds_read_b64_tr_b16 v[98:99], v159 offset:36864\n"
"v_cvt_pk_bf16_f32 v92, v28, v29\n"
"v_mfma_f32_16x16x32_bf16 v[20:23], a[192:195], v[192:195], v[116:119]\n"
"v_exp_f32 v30, v30\n"
"v_mfma_f32_16x16x32_bf16 v[20:23], a[196:199], v[196:199], v[20:23]\n"
"ds_read_b64_tr_b16 v[100:101], v159 offset:40960\n"
"v_mfma_f32_16x16x32_bf16 v[20:23], a[200:203], v[200:203], v[20:23]\n"
"v_exp_f32 v31, v31\n"
"v_mfma_f32_16x16x32_bf16 v[20:23], a[204:207], v[204:207], v[20:23]\n"
"ds_read_b64_tr_b16 v[102:103], v159 offset:45056\n"
"v_cvt_pk_bf16_f32 v93, v30, v31\n"
"v_mfma_f32_16x16x32_bf16 v[24:27], a[192:195], v[208:211], v[120:123]\n"
"v_exp_f32 v16, v16\n"
"v_mfma_f32_16x16x32_bf16 v[24:27], a[196:199], v[212:215], v[24:27]\n"
"ds_read_b64_tr_b16 v[104:105], v160 offset:32768\n"
"v_mfma_f32_16x16x32_bf16 v[24:27], a[200:203], v[216:219], v[24:27]\n"
"v_exp_f32 v17, v17\n"
"v_mfma_f32_16x16x32_bf16 v[24:27], a[204:207], v[220:223], v[24:27]\n"
"ds_read_b64_tr_b16 v[106:107], v160 offset:36864\n"
"v_cvt_pk_bf16_f32 v70, v16, v17\n"
"v_mfma_f32_16x16x32_bf16 v[28:31], a[192:195], v[224:227], v[124:127]\n"
"v_exp_f32 v18, v18\n"
"v_mfma_f32_16x16x32_bf16 v[28:31], a[196:199], v[228:231], v[28:31]\n"
"ds_read_b64_tr_b16 v[108:109], v160 offset:40960\n"
"v_mfma_f32_16x16x32_bf16 v[28:31], a[200:203], v[232:235], v[28:31]\n"
"v_exp_f32 v19, v19\n"
"v_mfma_f32_16x16x32_bf16 v[28:31], a[204:207], v[236:239], v[28:31]\n"
"s_waitcnt lgkmcnt(3)\n"
"ds_read_b64_tr_b16 v[110:111], v160 offset:45056\n"
"v_cvt_pk_bf16_f32 v71, v18, v19\n"
"v_mfma_f32_16x16x32_bf16 a[96:99], v[96:99], v[32:35], a[96:99]\n"
"v_exp_f32 v20, v20\n"
"v_mfma_f32_16x16x32_bf16 a[96:99], v[100:103], v[36:39], a[96:99]\n"
"buffer_load_dwordx4 v165, s[40:43], s57 offen offset:2048 lds\n"
"v_mfma_f32_16x16x32_bf16 a[100:103], v[96:99], v[40:43], a[100:103]\n"
"v_exp_f32 v21, v21\n"
"v_mfma_f32_16x16x32_bf16 a[100:103], v[100:103], v[44:47], a[100:103]\n"
"v_cvt_pk_bf16_f32 v78, v20, v21\n"
"v_mfma_f32_16x16x32_bf16 a[104:107], v[96:99], v[48:51], a[104:107]\n"
"v_exp_f32 v22, v22\n"
"v_mfma_f32_16x16x32_bf16 a[104:107], v[100:103], v[52:55], a[104:107]\n"
"buffer_load_dwordx4 v166, s[40:43], s57 offen offset:3072 lds\n"
"v_mfma_f32_16x16x32_bf16 a[108:111], v[96:99], v[56:59], a[108:111]\n"
"s_waitcnt vmcnt(16) lgkmcnt(0)\n"
"v_exp_f32 v23, v23\n"
"v_mfma_f32_16x16x32_bf16 a[108:111], v[100:103], v[60:63], a[108:111]\n"
"s_barrier\n"
"ds_read_b128 a[192:195], v149 offset:0\n"
"v_cvt_pk_bf16_f32 v79, v22, v23\n"
"v_mfma_f32_16x16x32_bf16 a[112:115], v[104:107], v[32:35], a[112:115]\n"
"s_add_u32 s56, s56, 16384\n"
"v_exp_f32 v24, v24\n"
"v_mfma_f32_16x16x32_bf16 a[112:115], v[108:111], v[36:39], a[112:115]\n"
"ds_read_b128 a[196:199], v150 offset:0\n"
"v_mfma_f32_16x16x32_bf16 a[116:119], v[104:107], v[40:43], a[116:119]\n"
"s_add_u32 s57, s57, 16384\n"
"v_exp_f32 v25, v25\n"
"v_mfma_f32_16x16x32_bf16 a[116:119], v[108:111], v[44:47], a[116:119]\n"
"ds_read_b128 a[200:203], v151 offset:0\n"
"v_cvt_pk_bf16_f32 v86, v24, v25\n"
"v_mfma_f32_16x16x32_bf16 a[120:123], v[104:107], v[48:51], a[120:123]\n"
"v_exp_f32 v26, v26\n"
"v_mfma_f32_16x16x32_bf16 a[120:123], v[108:111], v[52:55], a[120:123]\n"
"ds_read_b128 a[204:207], v152 offset:0\n"
"v_mfma_f32_16x16x32_bf16 a[124:127], v[104:107], v[56:59], a[124:127]\n"
"v_exp_f32 v27, v27\n"
"v_mfma_f32_16x16x32_bf16 a[124:127], v[108:111], v[60:63], a[124:127]\n"
"v_cvt_pk_bf16_f32 v87, v26, v27\n"
"v_smfmac_f32_16x16x64_bf16 v[140:143], v[144:147], v[56:63], v148\n"
"s_waitcnt lgkmcnt(3)\n"
"v_mfma_f32_16x16x32_bf16 v[16:19], a[192:195], v[176:179], v[112:115]\n"
"s_waitcnt lgkmcnt(2)\n"
"v_exp_f32 v28, v28\n"
"v_mfma_f32_16x16x32_bf16 v[16:19], a[196:199], v[180:183], v[16:19]\n"
"s_waitcnt lgkmcnt(1)\n"
"ds_read_b64_tr_b16 v[96:97], v153 offset:49152\n"
"v_mfma_f32_16x16x32_bf16 v[16:19], a[200:203], v[184:187], v[16:19]\n"
"s_waitcnt lgkmcnt(1)\n"
"v_exp_f32 v29, v29\n"
"v_mfma_f32_16x16x32_bf16 v[16:19], a[204:207], v[188:191], v[16:19]\n"
"ds_read_b64_tr_b16 v[98:99], v153 offset:53248\n"
"v_cvt_pk_bf16_f32 v94, v28, v29\n"
"v_mfma_f32_16x16x32_bf16 v[20:23], a[192:195], v[192:195], v[116:119]\n"
"v_exp_f32 v30, v30\n"
"v_mfma_f32_16x16x32_bf16 v[20:23], a[196:199], v[196:199], v[20:23]\n"
"ds_read_b64_tr_b16 v[100:101], v153 offset:57344\n"
"v_mfma_f32_16x16x32_bf16 v[20:23], a[200:203], v[200:203], v[20:23]\n"
"v_exp_f32 v31, v31\n"
"v_mfma_f32_16x16x32_bf16 v[20:23], a[204:207], v[204:207], v[20:23]\n"
"ds_read_b64_tr_b16 v[102:103], v153 offset:61440\n"
"v_cvt_pk_bf16_f32 v95, v30, v31\n"
"v_mfma_f32_16x16x32_bf16 v[24:27], a[192:195], v[208:211], v[120:123]\n"
"v_exp_f32 v16, v16\n"
"v_mfma_f32_16x16x32_bf16 v[24:27], a[196:199], v[212:215], v[24:27]\n"
"ds_read_b64_tr_b16 v[104:105], v154 offset:49152\n"
"v_mfma_f32_16x16x32_bf16 v[24:27], a[200:203], v[216:219], v[24:27]\n"
"v_exp_f32 v17, v17\n"
"v_mfma_f32_16x16x32_bf16 v[24:27], a[204:207], v[220:223], v[24:27]\n"
"ds_read_b64_tr_b16 v[106:107], v154 offset:53248\n"
"v_cvt_pk_bf16_f32 v32, v16, v17\n"
"v_mfma_f32_16x16x32_bf16 v[28:31], a[192:195], v[224:227], v[124:127]\n"
"v_exp_f32 v18, v18\n"
"v_mfma_f32_16x16x32_bf16 v[28:31], a[196:199], v[228:231], v[28:31]\n"
"ds_read_b64_tr_b16 v[108:109], v154 offset:57344\n"
"v_mfma_f32_16x16x32_bf16 v[28:31], a[200:203], v[232:235], v[28:31]\n"
"v_exp_f32 v19, v19\n"
"v_mfma_f32_16x16x32_bf16 v[28:31], a[204:207], v[236:239], v[28:31]\n"
"s_waitcnt lgkmcnt(3)\n"
"ds_read_b64_tr_b16 v[110:111], v154 offset:61440\n"
"v_cvt_pk_bf16_f32 v33, v18, v19\n"
"v_mfma_f32_16x16x32_bf16 a[0:3], v[96:99], v[64:67], a[0:3]\n"
"s_add_u32 m0, s64, 49152\n"
"v_exp_f32 v20, v20\n"
"v_mfma_f32_16x16x32_bf16 a[0:3], v[100:103], v[68:71], a[0:3]\n"
"buffer_load_dwordx4 v161, s[36:39], s56 offen offset:0 lds\n"
"v_mfma_f32_16x16x32_bf16 a[4:7], v[96:99], v[72:75], a[4:7]\n"
"v_exp_f32 v21, v21\n"
"v_mfma_f32_16x16x32_bf16 a[4:7], v[100:103], v[76:79], a[4:7]\n"
"v_cvt_pk_bf16_f32 v40, v20, v21\n"
"v_mfma_f32_16x16x32_bf16 a[8:11], v[96:99], v[80:83], a[8:11]\n"
"v_exp_f32 v22, v22\n"
"v_mfma_f32_16x16x32_bf16 a[8:11], v[100:103], v[84:87], a[8:11]\n"
"buffer_load_dwordx4 v162, s[36:39], s56 offen offset:1024 lds\n"
"v_mfma_f32_16x16x32_bf16 a[12:15], v[96:99], v[88:91], a[12:15]\n"
"s_waitcnt lgkmcnt(0)\n"
"v_exp_f32 v23, v23\n"
"v_mfma_f32_16x16x32_bf16 a[12:15], v[100:103], v[92:95], a[12:15]\n"
"ds_read_b128 a[192:195], v149 offset:4096\n"
"v_cvt_pk_bf16_f32 v41, v22, v23\n"
"v_mfma_f32_16x16x32_bf16 a[16:19], v[104:107], v[64:67], a[16:19]\n"
"v_exp_f32 v24, v24\n"
"v_mfma_f32_16x16x32_bf16 a[16:19], v[108:111], v[68:71], a[16:19]\n"
"ds_read_b128 a[196:199], v150 offset:4096\n"
"v_mfma_f32_16x16x32_bf16 a[20:23], v[104:107], v[72:75], a[20:23]\n"
"v_exp_f32 v25, v25\n"
"v_mfma_f32_16x16x32_bf16 a[20:23], v[108:111], v[76:79], a[20:23]\n"
"ds_read_b128 a[200:203], v151 offset:4096\n"
"v_cvt_pk_bf16_f32 v48, v24, v25\n"
"v_mfma_f32_16x16x32_bf16 a[24:27], v[104:107], v[80:83], a[24:27]\n"
"v_exp_f32 v26, v26\n"
"v_mfma_f32_16x16x32_bf16 a[24:27], v[108:111], v[84:87], a[24:27]\n"
"ds_read_b128 a[204:207], v152 offset:4096\n"
"v_mfma_f32_16x16x32_bf16 a[28:31], v[104:107], v[88:91], a[28:31]\n"
"v_exp_f32 v27, v27\n"
"v_mfma_f32_16x16x32_bf16 a[28:31], v[108:111], v[92:95], a[28:31]\n"
"v_cvt_pk_bf16_f32 v49, v26, v27\n"
"v_smfmac_f32_16x16x64_bf16 v[128:131], v[144:147], v[64:71], v148\n"
"s_waitcnt lgkmcnt(3)\n"
"v_mfma_f32_16x16x32_bf16 v[16:19], a[192:195], v[176:179], v[112:115]\n"
"s_waitcnt lgkmcnt(2)\n"
"v_exp_f32 v28, v28\n"
"v_mfma_f32_16x16x32_bf16 v[16:19], a[196:199], v[180:183], v[16:19]\n"
"s_waitcnt lgkmcnt(1)\n"
"ds_read_b64_tr_b16 v[96:97], v155 offset:49152\n"
"v_mfma_f32_16x16x32_bf16 v[16:19], a[200:203], v[184:187], v[16:19]\n"
"s_waitcnt lgkmcnt(1)\n"
"v_exp_f32 v29, v29\n"
"v_mfma_f32_16x16x32_bf16 v[16:19], a[204:207], v[188:191], v[16:19]\n"
"ds_read_b64_tr_b16 v[98:99], v155 offset:53248\n"
"v_cvt_pk_bf16_f32 v56, v28, v29\n"
"v_mfma_f32_16x16x32_bf16 v[20:23], a[192:195], v[192:195], v[116:119]\n"
"v_exp_f32 v30, v30\n"
"v_mfma_f32_16x16x32_bf16 v[20:23], a[196:199], v[196:199], v[20:23]\n"
"ds_read_b64_tr_b16 v[100:101], v155 offset:57344\n"
"v_mfma_f32_16x16x32_bf16 v[20:23], a[200:203], v[200:203], v[20:23]\n"
"v_exp_f32 v31, v31\n"
"v_mfma_f32_16x16x32_bf16 v[20:23], a[204:207], v[204:207], v[20:23]\n"
"ds_read_b64_tr_b16 v[102:103], v155 offset:61440\n"
"v_cvt_pk_bf16_f32 v57, v30, v31\n"
"v_mfma_f32_16x16x32_bf16 v[24:27], a[192:195], v[208:211], v[120:123]\n"
"v_exp_f32 v16, v16\n"
"v_mfma_f32_16x16x32_bf16 v[24:27], a[196:199], v[212:215], v[24:27]\n"
"ds_read_b64_tr_b16 v[104:105], v156 offset:49152\n"
"v_mfma_f32_16x16x32_bf16 v[24:27], a[200:203], v[216:219], v[24:27]\n"
"v_exp_f32 v17, v17\n"
"v_mfma_f32_16x16x32_bf16 v[24:27], a[204:207], v[220:223], v[24:27]\n"
"ds_read_b64_tr_b16 v[106:107], v156 offset:53248\n"
"v_cvt_pk_bf16_f32 v34, v16, v17\n"
"v_mfma_f32_16x16x32_bf16 v[28:31], a[192:195], v[224:227], v[124:127]\n"
"v_exp_f32 v18, v18\n"
"v_mfma_f32_16x16x32_bf16 v[28:31], a[196:199], v[228:231], v[28:31]\n"
"ds_read_b64_tr_b16 v[108:109], v156 offset:57344\n"
"v_mfma_f32_16x16x32_bf16 v[28:31], a[200:203], v[232:235], v[28:31]\n"
"v_exp_f32 v19, v19\n"
"v_mfma_f32_16x16x32_bf16 v[28:31], a[204:207], v[236:239], v[28:31]\n"
"s_waitcnt lgkmcnt(3)\n"
"ds_read_b64_tr_b16 v[110:111], v156 offset:61440\n"
"v_cvt_pk_bf16_f32 v35, v18, v19\n"
"v_mfma_f32_16x16x32_bf16 a[32:35], v[96:99], v[64:67], a[32:35]\n"
"v_exp_f32 v20, v20\n"
"v_mfma_f32_16x16x32_bf16 a[32:35], v[100:103], v[68:71], a[32:35]\n"
"buffer_load_dwordx4 v163, s[36:39], s56 offen offset:2048 lds\n"
"v_mfma_f32_16x16x32_bf16 a[36:39], v[96:99], v[72:75], a[36:39]\n"
"v_exp_f32 v21, v21\n"
"v_mfma_f32_16x16x32_bf16 a[36:39], v[100:103], v[76:79], a[36:39]\n"
"v_cvt_pk_bf16_f32 v42, v20, v21\n"
"v_mfma_f32_16x16x32_bf16 a[40:43], v[96:99], v[80:83], a[40:43]\n"
"v_exp_f32 v22, v22\n"
"v_mfma_f32_16x16x32_bf16 a[40:43], v[100:103], v[84:87], a[40:43]\n"
"buffer_load_dwordx4 v164, s[36:39], s56 offen offset:3072 lds\n"
"v_mfma_f32_16x16x32_bf16 a[44:47], v[96:99], v[88:91], a[44:47]\n"
"s_waitcnt lgkmcnt(0)\n"
"v_exp_f32 v23, v23\n"
"v_mfma_f32_16x16x32_bf16 a[44:47], v[100:103], v[92:95], a[44:47]\n"
"ds_read_b128 a[192:195], v149 offset:8192\n"
"v_cvt_pk_bf16_f32 v43, v22, v23\n"
"v_mfma_f32_16x16x32_bf16 a[48:51], v[104:107], v[64:67], a[48:51]\n"
"v_exp_f32 v24, v24\n"
"v_mfma_f32_16x16x32_bf16 a[48:51], v[108:111], v[68:71], a[48:51]\n"
"ds_read_b128 a[196:199], v150 offset:8192\n"
"v_mfma_f32_16x16x32_bf16 a[52:55], v[104:107], v[72:75], a[52:55]\n"
"v_exp_f32 v25, v25\n"
"v_mfma_f32_16x16x32_bf16 a[52:55], v[108:111], v[76:79], a[52:55]\n"
"ds_read_b128 a[200:203], v151 offset:8192\n"
"v_cvt_pk_bf16_f32 v50, v24, v25\n"
"v_mfma_f32_16x16x32_bf16 a[56:59], v[104:107], v[80:83], a[56:59]\n"
"v_exp_f32 v26, v26\n"
"v_mfma_f32_16x16x32_bf16 a[56:59], v[108:111], v[84:87], a[56:59]\n"
"ds_read_b128 a[204:207], v152 offset:8192\n"
"v_mfma_f32_16x16x32_bf16 a[60:63], v[104:107], v[88:91], a[60:63]\n"
"v_exp_f32 v27, v27\n"
"v_mfma_f32_16x16x32_bf16 a[60:63], v[108:111], v[92:95], a[60:63]\n"
"v_cvt_pk_bf16_f32 v51, v26, v27\n"
"v_smfmac_f32_16x16x64_bf16 v[132:135], v[144:147], v[72:79], v148\n"
"s_waitcnt lgkmcnt(3)\n"
"v_mfma_f32_16x16x32_bf16 v[16:19], a[192:195], v[176:179], v[112:115]\n"
"s_waitcnt lgkmcnt(2)\n"
"v_exp_f32 v28, v28\n"
"v_mfma_f32_16x16x32_bf16 v[16:19], a[196:199], v[180:183], v[16:19]\n"
"s_waitcnt lgkmcnt(1)\n"
"ds_read_b64_tr_b16 v[96:97], v157 offset:49152\n"
"v_mfma_f32_16x16x32_bf16 v[16:19], a[200:203], v[184:187], v[16:19]\n"
"s_waitcnt lgkmcnt(1)\n"
"v_exp_f32 v29, v29\n"
"v_mfma_f32_16x16x32_bf16 v[16:19], a[204:207], v[188:191], v[16:19]\n"
"ds_read_b64_tr_b16 v[98:99], v157 offset:53248\n"
"v_cvt_pk_bf16_f32 v58, v28, v29\n"
"v_mfma_f32_16x16x32_bf16 v[20:23], a[192:195], v[192:195], v[116:119]\n"
"v_exp_f32 v30, v30\n"
"v_mfma_f32_16x16x32_bf16 v[20:23], a[196:199], v[196:199], v[20:23]\n"
"ds_read_b64_tr_b16 v[100:101], v157 offset:57344\n"
"v_mfma_f32_16x16x32_bf16 v[20:23], a[200:203], v[200:203], v[20:23]\n"
"v_exp_f32 v31, v31\n"
"v_mfma_f32_16x16x32_bf16 v[20:23], a[204:207], v[204:207], v[20:23]\n"
"ds_read_b64_tr_b16 v[102:103], v157 offset:61440\n"
"v_cvt_pk_bf16_f32 v59, v30, v31\n"
"v_mfma_f32_16x16x32_bf16 v[24:27], a[192:195], v[208:211], v[120:123]\n"
"v_exp_f32 v16, v16\n"
"v_mfma_f32_16x16x32_bf16 v[24:27], a[196:199], v[212:215], v[24:27]\n"
"ds_read_b64_tr_b16 v[104:105], v158 offset:49152\n"
"v_mfma_f32_16x16x32_bf16 v[24:27], a[200:203], v[216:219], v[24:27]\n"
"v_exp_f32 v17, v17\n"
"v_mfma_f32_16x16x32_bf16 v[24:27], a[204:207], v[220:223], v[24:27]\n"
"ds_read_b64_tr_b16 v[106:107], v158 offset:53248\n"
"v_cvt_pk_bf16_f32 v36, v16, v17\n"
"v_mfma_f32_16x16x32_bf16 v[28:31], a[192:195], v[224:227], v[124:127]\n"
"v_exp_f32 v18, v18\n"
"v_mfma_f32_16x16x32_bf16 v[28:31], a[196:199], v[228:231], v[28:31]\n"
"ds_read_b64_tr_b16 v[108:109], v158 offset:57344\n"
"v_mfma_f32_16x16x32_bf16 v[28:31], a[200:203], v[232:235], v[28:31]\n"
"v_exp_f32 v19, v19\n"
"v_mfma_f32_16x16x32_bf16 v[28:31], a[204:207], v[236:239], v[28:31]\n"
"s_waitcnt lgkmcnt(3)\n"
"ds_read_b64_tr_b16 v[110:111], v158 offset:61440\n"
"v_cvt_pk_bf16_f32 v37, v18, v19\n"
"v_mfma_f32_16x16x32_bf16 a[64:67], v[96:99], v[64:67], a[64:67]\n"
"s_add_u32 m0, s65, 32768\n"
"v_exp_f32 v20, v20\n"
"v_mfma_f32_16x16x32_bf16 a[64:67], v[100:103], v[68:71], a[64:67]\n"
"buffer_load_dwordx4 v165, s[40:43], s57 offen offset:0 lds\n"
"v_mfma_f32_16x16x32_bf16 a[68:71], v[96:99], v[72:75], a[68:71]\n"
"v_exp_f32 v21, v21\n"
"v_mfma_f32_16x16x32_bf16 a[68:71], v[100:103], v[76:79], a[68:71]\n"
"v_cvt_pk_bf16_f32 v44, v20, v21\n"
"v_mfma_f32_16x16x32_bf16 a[72:75], v[96:99], v[80:83], a[72:75]\n"
"v_exp_f32 v22, v22\n"
"v_mfma_f32_16x16x32_bf16 a[72:75], v[100:103], v[84:87], a[72:75]\n"
"buffer_load_dwordx4 v166, s[40:43], s57 offen offset:1024 lds\n"
"v_mfma_f32_16x16x32_bf16 a[76:79], v[96:99], v[88:91], a[76:79]\n"
"s_waitcnt lgkmcnt(0)\n"
"v_exp_f32 v23, v23\n"
"v_mfma_f32_16x16x32_bf16 a[76:79], v[100:103], v[92:95], a[76:79]\n"
"ds_read_b128 a[192:195], v149 offset:12288\n"
"v_cvt_pk_bf16_f32 v45, v22, v23\n"
"v_mfma_f32_16x16x32_bf16 a[80:83], v[104:107], v[64:67], a[80:83]\n"
"v_exp_f32 v24, v24\n"
"v_mfma_f32_16x16x32_bf16 a[80:83], v[108:111], v[68:71], a[80:83]\n"
"ds_read_b128 a[196:199], v150 offset:12288\n"
"v_mfma_f32_16x16x32_bf16 a[84:87], v[104:107], v[72:75], a[84:87]\n"
"v_exp_f32 v25, v25\n"
"v_mfma_f32_16x16x32_bf16 a[84:87], v[108:111], v[76:79], a[84:87]\n"
"ds_read_b128 a[200:203], v151 offset:12288\n"
"v_cvt_pk_bf16_f32 v52, v24, v25\n"
"v_mfma_f32_16x16x32_bf16 a[88:91], v[104:107], v[80:83], a[88:91]\n"
"v_exp_f32 v26, v26\n"
"v_mfma_f32_16x16x32_bf16 a[88:91], v[108:111], v[84:87], a[88:91]\n"
"ds_read_b128 a[204:207], v152 offset:12288\n"
"v_mfma_f32_16x16x32_bf16 a[92:95], v[104:107], v[88:91], a[92:95]\n"
"v_exp_f32 v27, v27\n"
"v_mfma_f32_16x16x32_bf16 a[92:95], v[108:111], v[92:95], a[92:95]\n"
"v_cvt_pk_bf16_f32 v53, v26, v27\n"
"v_smfmac_f32_16x16x64_bf16 v[136:139], v[144:147], v[80:87], v148\n"
"s_waitcnt lgkmcnt(3)\n"
"v_mfma_f32_16x16x32_bf16 v[16:19], a[192:195], v[176:179], v[112:115]\n"
"s_waitcnt lgkmcnt(2)\n"
"v_exp_f32 v28, v28\n"
"v_mfma_f32_16x16x32_bf16 v[16:19], a[196:199], v[180:183], v[16:19]\n"
"s_waitcnt lgkmcnt(1)\n"
"ds_read_b64_tr_b16 v[96:97], v159 offset:49152\n"
"v_mfma_f32_16x16x32_bf16 v[16:19], a[200:203], v[184:187], v[16:19]\n"
"s_waitcnt lgkmcnt(1)\n"
"v_exp_f32 v29, v29\n"
"v_mfma_f32_16x16x32_bf16 v[16:19], a[204:207], v[188:191], v[16:19]\n"
"ds_read_b64_tr_b16 v[98:99], v159 offset:53248\n"
"v_cvt_pk_bf16_f32 v60, v28, v29\n"
"v_mfma_f32_16x16x32_bf16 v[20:23], a[192:195], v[192:195], v[116:119]\n"
"v_exp_f32 v30, v30\n"
"v_mfma_f32_16x16x32_bf16 v[20:23], a[196:199], v[196:199], v[20:23]\n"
"ds_read_b64_tr_b16 v[100:101], v159 offset:57344\n"
"v_mfma_f32_16x16x32_bf16 v[20:23], a[200:203], v[200:203], v[20:23]\n"
"v_exp_f32 v31, v31\n"
"v_mfma_f32_16x16x32_bf16 v[20:23], a[204:207], v[204:207], v[20:23]\n"
"ds_read_b64_tr_b16 v[102:103], v159 offset:61440\n"
"v_cvt_pk_bf16_f32 v61, v30, v31\n"
"v_mfma_f32_16x16x32_bf16 v[24:27], a[192:195], v[208:211], v[120:123]\n"
"v_exp_f32 v16, v16\n"
"v_mfma_f32_16x16x32_bf16 v[24:27], a[196:199], v[212:215], v[24:27]\n"
"ds_read_b64_tr_b16 v[104:105], v160 offset:49152\n"
"v_mfma_f32_16x16x32_bf16 v[24:27], a[200:203], v[216:219], v[24:27]\n"
"v_exp_f32 v17, v17\n"
"v_mfma_f32_16x16x32_bf16 v[24:27], a[204:207], v[220:223], v[24:27]\n"
"ds_read_b64_tr_b16 v[106:107], v160 offset:53248\n"
"v_cvt_pk_bf16_f32 v38, v16, v17\n"
"v_mfma_f32_16x16x32_bf16 v[28:31], a[192:195], v[224:227], v[124:127]\n"
"v_exp_f32 v18, v18\n"
"v_mfma_f32_16x16x32_bf16 v[28:31], a[196:199], v[228:231], v[28:31]\n"
"ds_read_b64_tr_b16 v[108:109], v160 offset:57344\n"
"v_mfma_f32_16x16x32_bf16 v[28:31], a[200:203], v[232:235], v[28:31]\n"
"v_exp_f32 v19, v19\n"
"v_mfma_f32_16x16x32_bf16 v[28:31], a[204:207], v[236:239], v[28:31]\n"
"s_waitcnt lgkmcnt(3)\n"
"ds_read_b64_tr_b16 v[110:111], v160 offset:61440\n"
"v_cvt_pk_bf16_f32 v39, v18, v19\n"
"v_mfma_f32_16x16x32_bf16 a[96:99], v[96:99], v[64:67], a[96:99]\n"
"v_exp_f32 v20, v20\n"
"v_mfma_f32_16x16x32_bf16 a[96:99], v[100:103], v[68:71], a[96:99]\n"
"buffer_load_dwordx4 v165, s[40:43], s57 offen offset:2048 lds\n"
"v_mfma_f32_16x16x32_bf16 a[100:103], v[96:99], v[72:75], a[100:103]\n"
"v_exp_f32 v21, v21\n"
"v_mfma_f32_16x16x32_bf16 a[100:103], v[100:103], v[76:79], a[100:103]\n"
"v_cvt_pk_bf16_f32 v46, v20, v21\n"
"v_mfma_f32_16x16x32_bf16 a[104:107], v[96:99], v[80:83], a[104:107]\n"
"v_exp_f32 v22, v22\n"
"v_mfma_f32_16x16x32_bf16 a[104:107], v[100:103], v[84:87], a[104:107]\n"
"buffer_load_dwordx4 v166, s[40:43], s57 offen offset:3072 lds\n"
"v_mfma_f32_16x16x32_bf16 a[108:111], v[96:99], v[88:91], a[108:111]\n"
"s_waitcnt vmcnt(16) lgkmcnt(0)\n"
"v_exp_f32 v23, v23\n"
"v_mfma_f32_16x16x32_bf16 a[108:111], v[100:103], v[92:95], a[108:111]\n"
"s_barrier\n"
"ds_read_b128 a[192:195], v149 offset:16384\n"
"v_cvt_pk_bf16_f32 v47, v22, v23\n"
"v_mfma_f32_16x16x32_bf16 a[112:115], v[104:107], v[64:67], a[112:115]\n"
"s_add_u32 s56, s56, 16384\n"
"v_exp_f32 v24, v24\n"
"v_mfma_f32_16x16x32_bf16 a[112:115], v[108:111], v[68:71], a[112:115]\n"
"ds_read_b128 a[196:199], v150 offset:16384\n"
"v_mfma_f32_16x16x32_bf16 a[116:119], v[104:107], v[72:75], a[116:119]\n"
"s_add_u32 s57, s57, 16384\n"
"v_exp_f32 v25, v25\n"
"v_mfma_f32_16x16x32_bf16 a[116:119], v[108:111], v[76:79], a[116:119]\n"
"ds_read_b128 a[200:203], v151 offset:16384\n"
"v_cvt_pk_bf16_f32 v54, v24, v25\n"
"v_mfma_f32_16x16x32_bf16 a[120:123], v[104:107], v[80:83], a[120:123]\n"
"v_exp_f32 v26, v26\n"
"v_mfma_f32_16x16x32_bf16 a[120:123], v[108:111], v[84:87], a[120:123]\n"
"ds_read_b128 a[204:207], v152 offset:16384\n"
"v_mfma_f32_16x16x32_bf16 a[124:127], v[104:107], v[88:91], a[124:127]\n"
"v_exp_f32 v27, v27\n"
"v_mfma_f32_16x16x32_bf16 a[124:127], v[108:111], v[92:95], a[124:127]\n"
"v_cvt_pk_bf16_f32 v55, v26, v27\n"
"v_smfmac_f32_16x16x64_bf16 v[140:143], v[144:147], v[88:95], v148\n"
"s_waitcnt lgkmcnt(3)\n"
"v_mfma_f32_16x16x32_bf16 v[16:19], a[192:195], v[176:179], v[112:115]\n"
"s_waitcnt lgkmcnt(2)\n"
"v_exp_f32 v28, v28\n"
"v_mfma_f32_16x16x32_bf16 v[16:19], a[196:199], v[180:183], v[16:19]\n"
"s_waitcnt lgkmcnt(1)\n"
"ds_read_b64_tr_b16 v[96:97], v153 offset:0\n"
"v_mfma_f32_16x16x32_bf16 v[16:19], a[200:203], v[184:187], v[16:19]\n"
"s_waitcnt lgkmcnt(1)\n"
"v_exp_f32 v29, v29\n"
"v_mfma_f32_16x16x32_bf16 v[16:19], a[204:207], v[188:191], v[16:19]\n"
"ds_read_b64_tr_b16 v[98:99], v153 offset:4096\n"
"v_cvt_pk_bf16_f32 v62, v28, v29\n"
"v_mfma_f32_16x16x32_bf16 v[20:23], a[192:195], v[192:195], v[116:119]\n"
"v_exp_f32 v30, v30\n"
"v_mfma_f32_16x16x32_bf16 v[20:23], a[196:199], v[196:199], v[20:23]\n"
"ds_read_b64_tr_b16 v[100:101], v153 offset:8192\n"
"v_mfma_f32_16x16x32_bf16 v[20:23], a[200:203], v[200:203], v[20:23]\n"
"v_exp_f32 v31, v31\n"
"v_mfma_f32_16x16x32_bf16 v[20:23], a[204:207], v[204:207], v[20:23]\n"
"ds_read_b64_tr_b16 v[102:103], v153 offset:12288\n"
"v_cvt_pk_bf16_f32 v63, v30, v31\n"
"v_mfma_f32_16x16x32_bf16 v[24:27], a[192:195], v[208:211], v[120:123]\n"
"v_exp_f32 v16, v16\n"
"v_mfma_f32_16x16x32_bf16 v[24:27], a[196:199], v[212:215], v[24:27]\n"
"ds_read_b64_tr_b16 v[104:105], v154 offset:0\n"
"v_mfma_f32_16x16x32_bf16 v[24:27], a[200:203], v[216:219], v[24:27]\n"
"v_exp_f32 v17, v17\n"
"v_mfma_f32_16x16x32_bf16 v[24:27], a[204:207], v[220:223], v[24:27]\n"
"ds_read_b64_tr_b16 v[106:107], v154 offset:4096\n"
"v_cvt_pk_bf16_f32 v64, v16, v17\n"
"v_mfma_f32_16x16x32_bf16 v[28:31], a[192:195], v[224:227], v[124:127]\n"
"v_exp_f32 v18, v18\n"
"v_mfma_f32_16x16x32_bf16 v[28:31], a[196:199], v[228:231], v[28:31]\n"
"ds_read_b64_tr_b16 v[108:109], v154 offset:8192\n"
"v_mfma_f32_16x16x32_bf16 v[28:31], a[200:203], v[232:235], v[28:31]\n"
"v_exp_f32 v19, v19\n"
"v_mfma_f32_16x16x32_bf16 v[28:31], a[204:207], v[236:239], v[28:31]\n"
"s_waitcnt lgkmcnt(3)\n"
"ds_read_b64_tr_b16 v[110:111], v154 offset:12288\n"
"v_cvt_pk_bf16_f32 v65, v18, v19\n"
"v_mfma_f32_16x16x32_bf16 a[0:3], v[96:99], v[32:35], a[0:3]\n"
"s_add_u32 m0, s64, 0\n"
"v_exp_f32 v20, v20\n"
"v_mfma_f32_16x16x32_bf16 a[0:3], v[100:103], v[36:39], a[0:3]\n"
"buffer_load_dwordx4 v161, s[36:39], s56 offen offset:0 lds\n"
"v_mfma_f32_16x16x32_bf16 a[4:7], v[96:99], v[40:43], a[4:7]\n"
"v_exp_f32 v21, v21\n"
"v_mfma_f32_16x16x32_bf16 a[4:7], v[100:103], v[44:47], a[4:7]\n"
"v_cvt_pk_bf16_f32 v72, v20, v21\n"
"v_mfma_f32_16x16x32_bf16 a[8:11], v[96:99], v[48:51], a[8:11]\n"
"v_exp_f32 v22, v22\n"
"v_mfma_f32_16x16x32_bf16 a[8:11], v[100:103], v[52:55], a[8:11]\n"
"buffer_load_dwordx4 v162, s[36:39], s56 offen offset:1024 lds\n"
"v_mfma_f32_16x16x32_bf16 a[12:15], v[96:99], v[56:59], a[12:15]\n"
"s_waitcnt lgkmcnt(0)\n"
"v_exp_f32 v23, v23\n"
"v_mfma_f32_16x16x32_bf16 a[12:15], v[100:103], v[60:63], a[12:15]\n"
"ds_read_b128 a[192:195], v149 offset:20480\n"
"v_cvt_pk_bf16_f32 v73, v22, v23\n"
"v_mfma_f32_16x16x32_bf16 a[16:19], v[104:107], v[32:35], a[16:19]\n"
"v_exp_f32 v24, v24\n"
"v_mfma_f32_16x16x32_bf16 a[16:19], v[108:111], v[36:39], a[16:19]\n"
"ds_read_b128 a[196:199], v150 offset:20480\n"
"v_mfma_f32_16x16x32_bf16 a[20:23], v[104:107], v[40:43], a[20:23]\n"
"v_exp_f32 v25, v25\n"
"v_mfma_f32_16x16x32_bf16 a[20:23], v[108:111], v[44:47], a[20:23]\n"
"ds_read_b128 a[200:203], v151 offset:20480\n"
"v_cvt_pk_bf16_f32 v80, v24, v25\n"
"v_mfma_f32_16x16x32_bf16 a[24:27], v[104:107], v[48:51], a[24:27]\n"
"v_exp_f32 v26, v26\n"
"v_mfma_f32_16x16x32_bf16 a[24:27], v[108:111], v[52:55], a[24:27]\n"
"ds_read_b128 a[204:207], v152 offset:20480\n"
"v_mfma_f32_16x16x32_bf16 a[28:31], v[104:107], v[56:59], a[28:31]\n"
"v_exp_f32 v27, v27\n"
"v_mfma_f32_16x16x32_bf16 a[28:31], v[108:111], v[60:63], a[28:31]\n"
"v_cvt_pk_bf16_f32 v81, v26, v27\n"
"v_smfmac_f32_16x16x64_bf16 v[128:131], v[144:147], v[32:39], v148\n"
"s_waitcnt lgkmcnt(3)\n"
"v_mfma_f32_16x16x32_bf16 v[16:19], a[192:195], v[176:179], v[112:115]\n"
"s_waitcnt lgkmcnt(2)\n"
"v_exp_f32 v28, v28\n"
"v_mfma_f32_16x16x32_bf16 v[16:19], a[196:199], v[180:183], v[16:19]\n"
"s_waitcnt lgkmcnt(1)\n"
"ds_read_b64_tr_b16 v[96:97], v155 offset:0\n"
"v_mfma_f32_16x16x32_bf16 v[16:19], a[200:203], v[184:187], v[16:19]\n"
"s_waitcnt lgkmcnt(1)\n"
"v_exp_f32 v29, v29\n"
"v_mfma_f32_16x16x32_bf16 v[16:19], a[204:207], v[188:191], v[16:19]\n"
"ds_read_b64_tr_b16 v[98:99], v155 offset:4096\n"
"v_cvt_pk_bf16_f32 v88, v28, v29\n"
"v_mfma_f32_16x16x32_bf16 v[20:23], a[192:195], v[192:195], v[116:119]\n"
"v_exp_f32 v30, v30\n"
"v_mfma_f32_16x16x32_bf16 v[20:23], a[196:199], v[196:199], v[20:23]\n"
"ds_read_b64_tr_b16 v[100:101], v155 offset:8192\n"
"v_mfma_f32_16x16x32_bf16 v[20:23], a[200:203], v[200:203], v[20:23]\n"
"v_exp_f32 v31, v31\n"
"v_mfma_f32_16x16x32_bf16 v[20:23], a[204:207], v[204:207], v[20:23]\n"
"ds_read_b64_tr_b16 v[102:103], v155 offset:12288\n"
"v_cvt_pk_bf16_f32 v89, v30, v31\n"
"v_mfma_f32_16x16x32_bf16 v[24:27], a[192:195], v[208:211], v[120:123]\n"
"v_exp_f32 v16, v16\n"
"v_mfma_f32_16x16x32_bf16 v[24:27], a[196:199], v[212:215], v[24:27]\n"
"ds_read_b64_tr_b16 v[104:105], v156 offset:0\n"
"v_mfma_f32_16x16x32_bf16 v[24:27], a[200:203], v[216:219], v[24:27]\n"
"v_exp_f32 v17, v17\n"
"v_mfma_f32_16x16x32_bf16 v[24:27], a[204:207], v[220:223], v[24:27]\n"
"ds_read_b64_tr_b16 v[106:107], v156 offset:4096\n"
"v_cvt_pk_bf16_f32 v66, v16, v17\n"
"v_mfma_f32_16x16x32_bf16 v[28:31], a[192:195], v[224:227], v[124:127]\n"
"v_exp_f32 v18, v18\n"
"v_mfma_f32_16x16x32_bf16 v[28:31], a[196:199], v[228:231], v[28:31]\n"
"ds_read_b64_tr_b16 v[108:109], v156 offset:8192\n"
"v_mfma_f32_16x16x32_bf16 v[28:31], a[200:203], v[232:235], v[28:31]\n"
"v_exp_f32 v19, v19\n"
"v_mfma_f32_16x16x32_bf16 v[28:31], a[204:207], v[236:239], v[28:31]\n"
"s_waitcnt lgkmcnt(3)\n"
"ds_read_b64_tr_b16 v[110:111], v156 offset:12288\n"
"v_cvt_pk_bf16_f32 v67, v18, v19\n"
"v_mfma_f32_16x16x32_bf16 a[32:35], v[96:99], v[32:35], a[32:35]\n"
"v_exp_f32 v20, v20\n"
"v_mfma_f32_16x16x32_bf16 a[32:35], v[100:103], v[36:39], a[32:35]\n"
"buffer_load_dwordx4 v163, s[36:39], s56 offen offset:2048 lds\n"
"v_mfma_f32_16x16x32_bf16 a[36:39], v[96:99], v[40:43], a[36:39]\n"
"v_exp_f32 v21, v21\n"
"v_mfma_f32_16x16x32_bf16 a[36:39], v[100:103], v[44:47], a[36:39]\n"
"v_cvt_pk_bf16_f32 v74, v20, v21\n"
"v_mfma_f32_16x16x32_bf16 a[40:43], v[96:99], v[48:51], a[40:43]\n"
"v_exp_f32 v22, v22\n"
"v_mfma_f32_16x16x32_bf16 a[40:43], v[100:103], v[52:55], a[40:43]\n"
"buffer_load_dwordx4 v164, s[36:39], s56 offen offset:3072 lds\n"
"v_mfma_f32_16x16x32_bf16 a[44:47], v[96:99], v[56:59], a[44:47]\n"
"s_waitcnt lgkmcnt(0)\n"
"v_exp_f32 v23, v23\n"
"v_mfma_f32_16x16x32_bf16 a[44:47], v[100:103], v[60:63], a[44:47]\n"
"ds_read_b128 a[192:195], v149 offset:24576\n"
"v_cvt_pk_bf16_f32 v75, v22, v23\n"
"v_mfma_f32_16x16x32_bf16 a[48:51], v[104:107], v[32:35], a[48:51]\n"
"v_exp_f32 v24, v24\n"
"v_mfma_f32_16x16x32_bf16 a[48:51], v[108:111], v[36:39], a[48:51]\n"
"ds_read_b128 a[196:199], v150 offset:24576\n"
"v_mfma_f32_16x16x32_bf16 a[52:55], v[104:107], v[40:43], a[52:55]\n"
"v_exp_f32 v25, v25\n"
"v_mfma_f32_16x16x32_bf16 a[52:55], v[108:111], v[44:47], a[52:55]\n"
"ds_read_b128 a[200:203], v151 offset:24576\n"
"v_cvt_pk_bf16_f32 v82, v24, v25\n"
"v_mfma_f32_16x16x32_bf16 a[56:59], v[104:107], v[48:51], a[56:59]\n"
"v_exp_f32 v26, v26\n"
"v_mfma_f32_16x16x32_bf16 a[56:59], v[108:111], v[52:55], a[56:59]\n"
"ds_read_b128 a[204:207], v152 offset:24576\n"
"v_mfma_f32_16x16x32_bf16 a[60:63], v[104:107], v[56:59], a[60:63]\n"
"v_exp_f32 v27, v27\n"
"v_mfma_f32_16x16x32_bf16 a[60:63], v[108:111], v[60:63], a[60:63]\n"
"v_cvt_pk_bf16_f32 v83, v26, v27\n"
"v_smfmac_f32_16x16x64_bf16 v[132:135], v[144:147], v[40:47], v148\n"
"s_waitcnt lgkmcnt(3)\n"
"v_mfma_f32_16x16x32_bf16 v[16:19], a[192:195], v[176:179], v[112:115]\n"
"s_waitcnt lgkmcnt(2)\n"
"v_exp_f32 v28, v28\n"
"v_mfma_f32_16x16x32_bf16 v[16:19], a[196:199], v[180:183], v[16:19]\n"
"s_waitcnt lgkmcnt(1)\n"
"ds_read_b64_tr_b16 v[96:97], v157 offset:0\n"
"v_mfma_f32_16x16x32_bf16 v[16:19], a[200:203], v[184:187], v[16:19]\n"
"s_waitcnt lgkmcnt(1)\n"
"v_exp_f32 v29, v29\n"
"v_mfma_f32_16x16x32_bf16 v[16:19], a[204:207], v[188:191], v[16:19]\n"
"ds_read_b64_tr_b16 v[98:99], v157 offset:4096\n"
"v_cvt_pk_bf16_f32 v90, v28, v29\n"
"v_mfma_f32_16x16x32_bf16 v[20:23], a[192:195], v[192:195], v[116:119]\n"
"v_exp_f32 v30, v30\n"
"v_mfma_f32_16x16x32_bf16 v[20:23], a[196:199], v[196:199], v[20:23]\n"
"ds_read_b64_tr_b16 v[100:101], v157 offset:8192\n"
"v_mfma_f32_16x16x32_bf16 v[20:23], a[200:203], v[200:203], v[20:23]\n"
"v_exp_f32 v31, v31\n"
"v_mfma_f32_16x16x32_bf16 v[20:23], a[204:207], v[204:207], v[20:23]\n"
"ds_read_b64_tr_b16 v[102:103], v157 offset:12288\n"
"v_cvt_pk_bf16_f32 v91, v30, v31\n"
"v_mfma_f32_16x16x32_bf16 v[24:27], a[192:195], v[208:211], v[120:123]\n"
"v_exp_f32 v16, v16\n"
"v_mfma_f32_16x16x32_bf16 v[24:27], a[196:199], v[212:215], v[24:27]\n"
"ds_read_b64_tr_b16 v[104:105], v158 offset:0\n"
"v_mfma_f32_16x16x32_bf16 v[24:27], a[200:203], v[216:219], v[24:27]\n"
"v_exp_f32 v17, v17\n"
"v_mfma_f32_16x16x32_bf16 v[24:27], a[204:207], v[220:223], v[24:27]\n"
"ds_read_b64_tr_b16 v[106:107], v158 offset:4096\n"
"v_cvt_pk_bf16_f32 v68, v16, v17\n"
"v_mfma_f32_16x16x32_bf16 v[28:31], a[192:195], v[224:227], v[124:127]\n"
"v_exp_f32 v18, v18\n"
"v_mfma_f32_16x16x32_bf16 v[28:31], a[196:199], v[228:231], v[28:31]\n"
"ds_read_b64_tr_b16 v[108:109], v158 offset:8192\n"
"v_mfma_f32_16x16x32_bf16 v[28:31], a[200:203], v[232:235], v[28:31]\n"
"v_exp_f32 v19, v19\n"
"v_mfma_f32_16x16x32_bf16 v[28:31], a[204:207], v[236:239], v[28:31]\n"
"s_waitcnt lgkmcnt(3)\n"
"ds_read_b64_tr_b16 v[110:111], v158 offset:12288\n"
"v_cvt_pk_bf16_f32 v69, v18, v19\n"
"v_mfma_f32_16x16x32_bf16 a[64:67], v[96:99], v[32:35], a[64:67]\n"
"s_add_u32 m0, s65, 49152\n"
"v_exp_f32 v20, v20\n"
"v_mfma_f32_16x16x32_bf16 a[64:67], v[100:103], v[36:39], a[64:67]\n"
"buffer_load_dwordx4 v165, s[40:43], s57 offen offset:0 lds\n"
"v_mfma_f32_16x16x32_bf16 a[68:71], v[96:99], v[40:43], a[68:71]\n"
"v_exp_f32 v21, v21\n"
"v_mfma_f32_16x16x32_bf16 a[68:71], v[100:103], v[44:47], a[68:71]\n"
"v_cvt_pk_bf16_f32 v76, v20, v21\n"
"v_mfma_f32_16x16x32_bf16 a[72:75], v[96:99], v[48:51], a[72:75]\n"
"v_exp_f32 v22, v22\n"
"v_mfma_f32_16x16x32_bf16 a[72:75], v[100:103], v[52:55], a[72:75]\n"
"buffer_load_dwordx4 v166, s[40:43], s57 offen offset:1024 lds\n"
"v_mfma_f32_16x16x32_bf16 a[76:79], v[96:99], v[56:59], a[76:79]\n"
"s_waitcnt lgkmcnt(0)\n"
"v_exp_f32 v23, v23\n"
"v_mfma_f32_16x16x32_bf16 a[76:79], v[100:103], v[60:63], a[76:79]\n"
"ds_read_b128 a[192:195], v149 offset:28672\n"
"v_cvt_pk_bf16_f32 v77, v22, v23\n"
"v_mfma_f32_16x16x32_bf16 a[80:83], v[104:107], v[32:35], a[80:83]\n"
"v_exp_f32 v24, v24\n"
"v_mfma_f32_16x16x32_bf16 a[80:83], v[108:111], v[36:39], a[80:83]\n"
"ds_read_b128 a[196:199], v150 offset:28672\n"
"v_mfma_f32_16x16x32_bf16 a[84:87], v[104:107], v[40:43], a[84:87]\n"
"v_exp_f32 v25, v25\n"
"v_mfma_f32_16x16x32_bf16 a[84:87], v[108:111], v[44:47], a[84:87]\n"
"ds_read_b128 a[200:203], v151 offset:28672\n"
"v_cvt_pk_bf16_f32 v84, v24, v25\n"
"v_mfma_f32_16x16x32_bf16 a[88:91], v[104:107], v[48:51], a[88:91]\n"
"v_exp_f32 v26, v26\n"
"v_mfma_f32_16x16x32_bf16 a[88:91], v[108:111], v[52:55], a[88:91]\n"
"ds_read_b128 a[204:207], v152 offset:28672\n"
"v_mfma_f32_16x16x32_bf16 a[92:95], v[104:107], v[56:59], a[92:95]\n"
"v_exp_f32 v27, v27\n"
"v_mfma_f32_16x16x32_bf16 a[92:95], v[108:111], v[60:63], a[92:95]\n"
"v_cvt_pk_bf16_f32 v85, v26, v27\n"
"v_smfmac_f32_16x16x64_bf16 v[136:139], v[144:147], v[48:55], v148\n"
"s_waitcnt lgkmcnt(3)\n"
"v_mfma_f32_16x16x32_bf16 v[16:19], a[192:195], v[176:179], v[112:115]\n"
"s_waitcnt lgkmcnt(2)\n"
"v_exp_f32 v28, v28\n"
"v_mfma_f32_16x16x32_bf16 v[16:19], a[196:199], v[180:183], v[16:19]\n"
"s_waitcnt lgkmcnt(1)\n"
"ds_read_b64_tr_b16 v[96:97], v159 offset:0\n"
"v_mfma_f32_16x16x32_bf16 v[16:19], a[200:203], v[184:187], v[16:19]\n"
"s_waitcnt lgkmcnt(1)\n"
"v_exp_f32 v29, v29\n"
"v_mfma_f32_16x16x32_bf16 v[16:19], a[204:207], v[188:191], v[16:19]\n"
"ds_read_b64_tr_b16 v[98:99], v159 offset:4096\n"
"v_cvt_pk_bf16_f32 v92, v28, v29\n"
"v_mfma_f32_16x16x32_bf16 v[20:23], a[192:195], v[192:195], v[116:119]\n"
"v_exp_f32 v30, v30\n"
"v_mfma_f32_16x16x32_bf16 v[20:23], a[196:199], v[196:199], v[20:23]\n"
"ds_read_b64_tr_b16 v[100:101], v159 offset:8192\n"
"v_mfma_f32_16x16x32_bf16 v[20:23], a[200:203], v[200:203], v[20:23]\n"
"v_exp_f32 v31, v31\n"
"v_mfma_f32_16x16x32_bf16 v[20:23], a[204:207], v[204:207], v[20:23]\n"
"ds_read_b64_tr_b16 v[102:103], v159 offset:12288\n"
"v_cvt_pk_bf16_f32 v93, v30, v31\n"
"v_mfma_f32_16x16x32_bf16 v[24:27], a[192:195], v[208:211], v[120:123]\n"
"v_exp_f32 v16, v16\n"
"v_mfma_f32_16x16x32_bf16 v[24:27], a[196:199], v[212:215], v[24:27]\n"
"ds_read_b64_tr_b16 v[104:105], v160 offset:0\n"
"v_mfma_f32_16x16x32_bf16 v[24:27], a[200:203], v[216:219], v[24:27]\n"
"v_exp_f32 v17, v17\n"
"v_mfma_f32_16x16x32_bf16 v[24:27], a[204:207], v[220:223], v[24:27]\n"
"ds_read_b64_tr_b16 v[106:107], v160 offset:4096\n"
"v_cvt_pk_bf16_f32 v70, v16, v17\n"
"v_mfma_f32_16x16x32_bf16 v[28:31], a[192:195], v[224:227], v[124:127]\n"
"v_exp_f32 v18, v18\n"
"v_mfma_f32_16x16x32_bf16 v[28:31], a[196:199], v[228:231], v[28:31]\n"
"ds_read_b64_tr_b16 v[108:109], v160 offset:8192\n"
"v_mfma_f32_16x16x32_bf16 v[28:31], a[200:203], v[232:235], v[28:31]\n"
"v_exp_f32 v19, v19\n"
"v_mfma_f32_16x16x32_bf16 v[28:31], a[204:207], v[236:239], v[28:31]\n"
"s_waitcnt lgkmcnt(3)\n"
"ds_read_b64_tr_b16 v[110:111], v160 offset:12288\n"
"v_cvt_pk_bf16_f32 v71, v18, v19\n"
"v_mfma_f32_16x16x32_bf16 a[96:99], v[96:99], v[32:35], a[96:99]\n"
"v_exp_f32 v20, v20\n"
"v_mfma_f32_16x16x32_bf16 a[96:99], v[100:103], v[36:39], a[96:99]\n"
"buffer_load_dwordx4 v165, s[40:43], s57 offen offset:2048 lds\n"
"v_mfma_f32_16x16x32_bf16 a[100:103], v[96:99], v[40:43], a[100:103]\n"
"v_exp_f32 v21, v21\n"
"v_mfma_f32_16x16x32_bf16 a[100:103], v[100:103], v[44:47], a[100:103]\n"
"v_cvt_pk_bf16_f32 v78, v20, v21\n"
"v_mfma_f32_16x16x32_bf16 a[104:107], v[96:99], v[48:51], a[104:107]\n"
"v_exp_f32 v22, v22\n"
"v_mfma_f32_16x16x32_bf16 a[104:107], v[100:103], v[52:55], a[104:107]\n"
"buffer_load_dwordx4 v166, s[40:43], s57 offen offset:3072 lds\n"
"v_mfma_f32_16x16x32_bf16 a[108:111], v[96:99], v[56:59], a[108:111]\n"
"s_waitcnt vmcnt(16) lgkmcnt(0)\n"
"v_exp_f32 v23, v23\n"
"v_mfma_f32_16x16x32_bf16 a[108:111], v[100:103], v[60:63], a[108:111]\n"
"s_barrier\n"
"ds_read_b128 a[192:195], v149 offset:32768\n"
"v_cvt_pk_bf16_f32 v79, v22, v23\n"
"v_mfma_f32_16x16x32_bf16 a[112:115], v[104:107], v[32:35], a[112:115]\n"
"s_add_u32 s56, s56, 16384\n"
"v_exp_f32 v24, v24\n"
"v_mfma_f32_16x16x32_bf16 a[112:115], v[108:111], v[36:39], a[112:115]\n"
"ds_read_b128 a[196:199], v150 offset:32768\n"
"v_mfma_f32_16x16x32_bf16 a[116:119], v[104:107], v[40:43], a[116:119]\n"
"s_add_u32 s57, s57, 16384\n"
"v_exp_f32 v25, v25\n"
"v_mfma_f32_16x16x32_bf16 a[116:119], v[108:111], v[44:47], a[116:119]\n"
"ds_read_b128 a[200:203], v151 offset:32768\n"
"v_cvt_pk_bf16_f32 v86, v24, v25\n"
"v_mfma_f32_16x16x32_bf16 a[120:123], v[104:107], v[48:51], a[120:123]\n"
"v_exp_f32 v26, v26\n"
"v_mfma_f32_16x16x32_bf16 a[120:123], v[108:111], v[52:55], a[120:123]\n"
"ds_read_b128 a[204:207], v152 offset:32768\n"
"v_mfma_f32_16x16x32_bf16 a[124:127], v[104:107], v[56:59], a[124:127]\n"
"v_exp_f32 v27, v27\n"
"v_mfma_f32_16x16x32_bf16 a[124:127], v[108:111], v[60:63], a[124:127]\n"
"v_cvt_pk_bf16_f32 v87, v26, v27\n"
"v_smfmac_f32_16x16x64_bf16 v[140:143], v[144:147], v[56:63], v148\n"
"s_waitcnt lgkmcnt(3)\n"
"s_sub_u32 s63, s63, 1\n"
"s_cmp_lg_u32 s63, 0\n"
"s_cbranch_scc1 L_loop_%=\n"
"v_mfma_f32_16x16x32_bf16 v[16:19], a[192:195], v[176:179], v[112:115]\n"
"s_waitcnt lgkmcnt(2)\n"
"v_exp_f32 v28, v28\n"
"v_mfma_f32_16x16x32_bf16 v[16:19], a[196:199], v[180:183], v[16:19]\n"
"s_waitcnt lgkmcnt(1)\n"
"ds_read_b64_tr_b16 v[96:97], v153 offset:16384\n"
"buffer_load_dwordx4 a[128:131], v175, s[44:47], s58 offen offset:0 sc1\n"
"v_mfma_f32_16x16x32_bf16 v[16:19], a[200:203], v[184:187], v[16:19]\n"
"s_waitcnt lgkmcnt(1)\n"
"v_exp_f32 v29, v29\n"
"v_mfma_f32_16x16x32_bf16 v[16:19], a[204:207], v[188:191], v[16:19]\n"
"ds_read_b64_tr_b16 v[98:99], v153 offset:20480\n"
"v_cvt_pk_bf16_f32 v94, v28, v29\n"
"v_mfma_f32_16x16x32_bf16 v[20:23], a[192:195], v[192:195], v[116:119]\n"
"v_exp_f32 v30, v30\n"
"v_mfma_f32_16x16x32_bf16 v[20:23], a[196:199], v[196:199], v[20:23]\n"
"ds_read_b64_tr_b16 v[100:101], v153 offset:24576\n"
"buffer_load_dwordx4 a[132:135], v175, s[44:47], s58 offen offset:1024 sc1\n"
"v_mfma_f32_16x16x32_bf16 v[20:23], a[200:203], v[200:203], v[20:23]\n"
"v_exp_f32 v31, v31\n"
"v_mfma_f32_16x16x32_bf16 v[20:23], a[204:207], v[204:207], v[20:23]\n"
"ds_read_b64_tr_b16 v[102:103], v153 offset:28672\n"
"v_cvt_pk_bf16_f32 v95, v30, v31\n"
"v_mfma_f32_16x16x32_bf16 v[24:27], a[192:195], v[208:211], v[120:123]\n"
"s_add_u32 m0, s61, 131072\n"
"v_exp_f32 v16, v16\n"
"v_mfma_f32_16x16x32_bf16 v[24:27], a[196:199], v[212:215], v[24:27]\n"
"ds_read_b64_tr_b16 v[104:105], v154 offset:16384\n"
"buffer_load_dwordx4 v240, s[48:51], s60 offen offset:0 lds\n"
"v_mfma_f32_16x16x32_bf16 v[24:27], a[200:203], v[216:219], v[24:27]\n"
"v_exp_f32 v17, v17\n"
"v_mfma_f32_16x16x32_bf16 v[24:27], a[204:207], v[220:223], v[24:27]\n"
"ds_read_b64_tr_b16 v[106:107], v154 offset:20480\n"
"v_cvt_pk_bf16_f32 v32, v16, v17\n"
"v_mfma_f32_16x16x32_bf16 v[28:31], a[192:195], v[224:227], v[124:127]\n"
"v_exp_f32 v18, v18\n"
"v_mfma_f32_16x16x32_bf16 v[28:31], a[196:199], v[228:231], v[28:31]\n"
"ds_read_b64_tr_b16 v[108:109], v154 offset:24576\n"
"buffer_load_dwordx4 a[136:139], v175, s[44:47], s58 offen offset:2048 sc1\n"
"v_mfma_f32_16x16x32_bf16 v[28:31], a[200:203], v[232:235], v[28:31]\n"
"v_exp_f32 v19, v19\n"
"v_mfma_f32_16x16x32_bf16 v[28:31], a[204:207], v[236:239], v[28:31]\n"
"s_waitcnt lgkmcnt(3)\n"
"ds_read_b64_tr_b16 v[110:111], v154 offset:28672\n"
"v_cvt_pk_bf16_f32 v33, v18, v19\n"
"v_mfma_f32_16x16x32_bf16 a[0:3], v[96:99], v[64:67], a[0:3]\n"
"v_exp_f32 v20, v20\n"
"v_mfma_f32_16x16x32_bf16 a[0:3], v[100:103], v[68:71], a[0:3]\n"
"buffer_load_dwordx4 a[140:143], v175, s[44:47], s58 offen offset:3072 sc1\n"
"v_mfma_f32_16x16x32_bf16 a[4:7], v[96:99], v[72:75], a[4:7]\n"
"v_exp_f32 v21, v21\n"
"v_mfma_f32_16x16x32_bf16 a[4:7], v[100:103], v[76:79], a[4:7]\n"
"v_cvt_pk_bf16_f32 v40, v20, v21\n"
"v_mfma_f32_16x16x32_bf16 a[8:11], v[96:99], v[80:83], a[8:11]\n"
"s_add_u32 s58, s58, 4096\n"
"v_exp_f32 v22, v22\n"
"v_mfma_f32_16x16x32_bf16 a[8:11], v[100:103], v[84:87], a[8:11]\n"
"buffer_load_dwordx4 v241, s[48:51], s60 offen offset:1024 lds\n"
"v_mfma_f32_16x16x32_bf16 a[12:15], v[96:99], v[88:91], a[12:15]\n"
"s_waitcnt lgkmcnt(0)\n"
"v_exp_f32 v23, v23\n"
"v_mfma_f32_16x16x32_bf16 a[12:15], v[100:103], v[92:95], a[12:15]\n"
"ds_read_b128 a[192:195], v149 offset:36864\n"
"v_cvt_pk_bf16_f32 v41, v22, v23\n"
"v_mfma_f32_16x16x32_bf16 a[16:19], v[104:107], v[64:67], a[16:19]\n"
"v_exp_f32 v24, v24\n"
"v_mfma_f32_16x16x32_bf16 a[16:19], v[108:111], v[68:71], a[16:19]\n"
"ds_read_b128 a[196:199], v150 offset:36864\n"
"buffer_load_dwordx4 a[144:147], v175, s[44:47], s58 offen offset:0 sc1\n"
"v_mfma_f32_16x16x32_bf16 a[20:23], v[104:107], v[72:75], a[20:23]\n"
"v_exp_f32 v25, v25\n"
"v_mfma_f32_16x16x32_bf16 a[20:23], v[108:111], v[76:79], a[20:23]\n"
"ds_read_b128 a[200:203], v151 offset:36864\n"
"v_cvt_pk_bf16_f32 v48, v24, v25\n"
"v_mfma_f32_16x16x32_bf16 a[24:27], v[104:107], v[80:83], a[24:27]\n"
"s_add_u32 s58, s58, 1024\n"
"v_exp_f32 v26, v26\n"
"v_mfma_f32_16x16x32_bf16 a[24:27], v[108:111], v[84:87], a[24:27]\n"
"ds_read_b128 a[204:207], v152 offset:36864\n"
"buffer_load_dwordx4 a[148:151], v175, s[44:47], s58 offen offset:0 sc1\n"
"v_mfma_f32_16x16x32_bf16 a[28:31], v[104:107], v[88:91], a[28:31]\n"
"v_exp_f32 v27, v27\n"
"v_mfma_f32_16x16x32_bf16 a[28:31], v[108:111], v[92:95], a[28:31]\n"
"v_cvt_pk_bf16_f32 v49, v26, v27\n"
"v_smfmac_f32_16x16x64_bf16 v[128:131], v[144:147], v[64:71], v148\n"
"s_waitcnt lgkmcnt(3)\n"
"v_mfma_f32_16x16x32_bf16 v[16:19], a[192:195], v[176:179], v[112:115]\n"
"s_waitcnt lgkmcnt(2)\n"
"v_exp_f32 v28, v28\n"
"v_mfma_f32_16x16x32_bf16 v[16:19], a[196:199], v[180:183], v[16:19]\n"
"s_waitcnt lgkmcnt(1)\n"
"ds_read_b64_tr_b16 v[96:97], v155 offset:16384\n"
"buffer_load_dwordx4 v242, s[48:51], s60 offen offset:2048 lds\n"
"v_mfma_f32_16x16x32_bf16 v[16:19], a[200:203], v[184:187], v[16:19]\n"
"s_waitcnt lgkmcnt(1)\n"
"v_exp_f32 v29, v29\n"
"v_mfma_f32_16x16x32_bf16 v[16:19], a[204:207], v[188:191], v[16:19]\n"
"ds_read_b64_tr_b16 v[98:99], v155 offset:20480\n"
"v_cvt_pk_bf16_f32 v56, v28, v29\n"
"v_mfma_f32_16x16x32_bf16 v[20:23], a[192:195], v[192:195], v[116:119]\n"
"v_exp_f32 v30, v30\n"
"v_mfma_f32_16x16x32_bf16 v[20:23], a[196:199], v[196:199], v[20:23]\n"
"ds_read_b64_tr_b16 v[100:101], v155 offset:24576\n"
"buffer_load_dwordx4 a[152:155], v175, s[44:47], s58 offen offset:1024 sc1\n"
"v_mfma_f32_16x16x32_bf16 v[20:23], a[200:203], v[200:203], v[20:23]\n"
"v_exp_f32 v31, v31\n"
"v_mfma_f32_16x16x32_bf16 v[20:23], a[204:207], v[204:207], v[20:23]\n"
"ds_read_b64_tr_b16 v[102:103], v155 offset:28672\n"
"v_cvt_pk_bf16_f32 v57, v30, v31\n"
"v_mfma_f32_16x16x32_bf16 v[24:27], a[192:195], v[208:211], v[120:123]\n"
"v_exp_f32 v16, v16\n"
"v_mfma_f32_16x16x32_bf16 v[24:27], a[196:199], v[212:215], v[24:27]\n"
"ds_read_b64_tr_b16 v[104:105], v156 offset:16384\n"
"buffer_load_dwordx4 a[156:159], v175, s[44:47], s58 offen offset:2048 sc1\n"
"v_mfma_f32_16x16x32_bf16 v[24:27], a[200:203], v[216:219], v[24:27]\n"
"v_exp_f32 v17, v17\n"
"v_mfma_f32_16x16x32_bf16 v[24:27], a[204:207], v[220:223], v[24:27]\n"
"ds_read_b64_tr_b16 v[106:107], v156 offset:20480\n"
"v_cvt_pk_bf16_f32 v34, v16, v17\n"
"v_mfma_f32_16x16x32_bf16 v[28:31], a[192:195], v[224:227], v[124:127]\n"
"v_exp_f32 v18, v18\n"
"v_mfma_f32_16x16x32_bf16 v[28:31], a[196:199], v[228:231], v[28:31]\n"
"ds_read_b64_tr_b16 v[108:109], v156 offset:24576\n"
"buffer_load_dwordx4 v243, s[48:51], s60 offen offset:3072 lds\n"
"v_mfma_f32_16x16x32_bf16 v[28:31], a[200:203], v[232:235], v[28:31]\n"
"v_exp_f32 v19, v19\n"
"v_mfma_f32_16x16x32_bf16 v[28:31], a[204:207], v[236:239], v[28:31]\n"
"s_waitcnt lgkmcnt(3)\n"
"ds_read_b64_tr_b16 v[110:111], v156 offset:28672\n"
"v_cvt_pk_bf16_f32 v35, v18, v19\n"
"v_mfma_f32_16x16x32_bf16 a[32:35], v[96:99], v[64:67], a[32:35]\n"
"s_add_u32 s60, s60, 110592\n"
"v_exp_f32 v20, v20\n"
"v_mfma_f32_16x16x32_bf16 a[32:35], v[100:103], v[68:71], a[32:35]\n"
"buffer_load_dwordx4 a[160:163], v175, s[44:47], s58 offen offset:3072 sc1\n"
"v_mfma_f32_16x16x32_bf16 a[36:39], v[96:99], v[72:75], a[36:39]\n"
"v_exp_f32 v21, v21\n"
"v_mfma_f32_16x16x32_bf16 a[36:39], v[100:103], v[76:79], a[36:39]\n"
"v_cvt_pk_bf16_f32 v42, v20, v21\n"
"v_mfma_f32_16x16x32_bf16 a[40:43], v[96:99], v[80:83], a[40:43]\n"
"s_add_u32 s58, s58, 4096\n"
"v_exp_f32 v22, v22\n"
"v_mfma_f32_16x16x32_bf16 a[40:43], v[100:103], v[84:87], a[40:43]\n"
"buffer_load_dwordx4 a[164:167], v175, s[44:47], s58 offen offset:0 sc1\n"
"v_mfma_f32_16x16x32_bf16 a[44:47], v[96:99], v[88:91], a[44:47]\n"
"s_waitcnt lgkmcnt(0)\n"
"v_exp_f32 v23, v23\n"
"v_mfma_f32_16x16x32_bf16 a[44:47], v[100:103], v[92:95], a[44:47]\n"
"ds_read_b128 a[192:195], v149 offset:40960\n"
"v_cvt_pk_bf16_f32 v43, v22, v23\n"
"v_mfma_f32_16x16x32_bf16 a[48:51], v[104:107], v[64:67], a[48:51]\n"
"s_add_u32 s58, s58, 1024\n"
"s_cmp_eq_u32 s58, 81920\n"
"s_cselect_b32 s58, 0, s58\n"
"v_exp_f32 v24, v24\n"
"v_mfma_f32_16x16x32_bf16 a[48:51], v[108:111], v[68:71], a[48:51]\n"
"ds_read_b128 a[196:199], v150 offset:40960\n"
"v_mfma_f32_16x16x32_bf16 a[52:55], v[104:107], v[72:75], a[52:55]\n"
"v_exp_f32 v25, v25\n"
"v_mfma_f32_16x16x32_bf16 a[52:55], v[108:111], v[76:79], a[52:55]\n"
"ds_read_b128 a[200:203], v151 offset:40960\n"
"v_cvt_pk_bf16_f32 v50, v24, v25\n"
"v_mfma_f32_16x16x32_bf16 a[56:59], v[104:107], v[80:83], a[56:59]\n"
"s_add_u32 m0, s61, 135168\n"
"v_exp_f32 v26, v26\n"
"v_mfma_f32_16x16x32_bf16 a[56:59], v[108:111], v[84:87], a[56:59]\n"
"ds_read_b128 a[204:207], v152 offset:40960\n"
"buffer_load_dwordx4 v240, s[48:51], s60 offen offset:0 lds\n"
"v_mfma_f32_16x16x32_bf16 a[60:63], v[104:107], v[88:91], a[60:63]\n"
"v_exp_f32 v27, v27\n"
"v_mfma_f32_16x16x32_bf16 a[60:63], v[108:111], v[92:95], a[60:63]\n"
"v_cvt_pk_bf16_f32 v51, v26, v27\n"
"v_smfmac_f32_16x16x64_bf16 v[132:135], v[144:147], v[72:79], v148\n"
"s_waitcnt lgkmcnt(3)\n"
"v_mfma_f32_16x16x32_bf16 v[16:19], a[192:195], v[176:179], v[112:115]\n"
"s_waitcnt lgkmcnt(2)\n"
"v_exp_f32 v28, v28\n"
"v_mfma_f32_16x16x32_bf16 v[16:19], a[196:199], v[180:183], v[16:19]\n"
"s_waitcnt lgkmcnt(1)\n"
"ds_read_b64_tr_b16 v[96:97], v157 offset:16384\n"
"buffer_load_dwordx4 a[168:171], v175, s[44:47], s58 offen offset:0 sc1\n"
"v_mfma_f32_16x16x32_bf16 v[16:19], a[200:203], v[184:187], v[16:19]\n"
"s_waitcnt lgkmcnt(1)\n"
"v_exp_f32 v29, v29\n"
"v_mfma_f32_16x16x32_bf16 v[16:19], a[204:207], v[188:191], v[16:19]\n"
"ds_read_b64_tr_b16 v[98:99], v157 offset:20480\n"
"v_cvt_pk_bf16_f32 v58, v28, v29\n"
"v_mfma_f32_16x16x32_bf16 v[20:23], a[192:195], v[192:195], v[116:119]\n"
"v_exp_f32 v30, v30\n"
"v_mfma_f32_16x16x32_bf16 v[20:23], a[196:199], v[196:199], v[20:23]\n"
"ds_read_b64_tr_b16 v[100:101], v157 offset:24576\n"
"buffer_load_dwordx4 a[172:175], v175, s[44:47], s58 offen offset:1024 sc1\n"
"v_mfma_f32_16x16x32_bf16 v[20:23], a[200:203], v[200:203], v[20:23]\n"
"v_exp_f32 v31, v31\n"
"v_mfma_f32_16x16x32_bf16 v[20:23], a[204:207], v[204:207], v[20:23]\n"
"ds_read_b64_tr_b16 v[102:103], v157 offset:28672\n"
"v_cvt_pk_bf16_f32 v59, v30, v31\n"
"v_mfma_f32_16x16x32_bf16 v[24:27], a[192:195], v[208:211], v[120:123]\n"
"v_exp_f32 v16, v16\n"
"v_mfma_f32_16x16x32_bf16 v[24:27], a[196:199], v[212:215], v[24:27]\n"
"ds_read_b64_tr_b16 v[104:105], v158 offset:16384\n"
"buffer_load_dwordx4 v241, s[48:51], s60 offen offset:1024 lds\n"
"v_mfma_f32_16x16x32_bf16 v[24:27], a[200:203], v[216:219], v[24:27]\n"
"v_exp_f32 v17, v17\n"
"v_mfma_f32_16x16x32_bf16 v[24:27], a[204:207], v[220:223], v[24:27]\n"
"ds_read_b64_tr_b16 v[106:107], v158 offset:20480\n"
"v_cvt_pk_bf16_f32 v36, v16, v17\n"
"v_mfma_f32_16x16x32_bf16 v[28:31], a[192:195], v[224:227], v[124:127]\n"
"v_exp_f32 v18, v18\n"
"v_mfma_f32_16x16x32_bf16 v[28:31], a[196:199], v[228:231], v[28:31]\n"
"ds_read_b64_tr_b16 v[108:109], v158 offset:24576\n"
"buffer_load_dwordx4 a[176:179], v175, s[44:47], s58 offen offset:2048 sc1\n"
"v_mfma_f32_16x16x32_bf16 v[28:31], a[200:203], v[232:235], v[28:31]\n"
"v_exp_f32 v19, v19\n"
"v_mfma_f32_16x16x32_bf16 v[28:31], a[204:207], v[236:239], v[28:31]\n"
"s_waitcnt lgkmcnt(3)\n"
"ds_read_b64_tr_b16 v[110:111], v158 offset:28672\n"
"v_cvt_pk_bf16_f32 v37, v18, v19\n"
"v_mfma_f32_16x16x32_bf16 a[64:67], v[96:99], v[64:67], a[64:67]\n"
"v_exp_f32 v20, v20\n"
"v_mfma_f32_16x16x32_bf16 a[64:67], v[100:103], v[68:71], a[64:67]\n"
"buffer_load_dwordx4 a[180:183], v175, s[44:47], s58 offen offset:3072 sc1\n"
"v_mfma_f32_16x16x32_bf16 a[68:71], v[96:99], v[72:75], a[68:71]\n"
"v_exp_f32 v21, v21\n"
"v_mfma_f32_16x16x32_bf16 a[68:71], v[100:103], v[76:79], a[68:71]\n"
"v_cvt_pk_bf16_f32 v44, v20, v21\n"
"v_mfma_f32_16x16x32_bf16 a[72:75], v[96:99], v[80:83], a[72:75]\n"
"s_add_u32 s58, s58, 4096\n"
"v_exp_f32 v22, v22\n"
"v_mfma_f32_16x16x32_bf16 a[72:75], v[100:103], v[84:87], a[72:75]\n"
"buffer_load_dwordx4 v242, s[48:51], s60 offen offset:2048 lds\n"
"v_mfma_f32_16x16x32_bf16 a[76:79], v[96:99], v[88:91], a[76:79]\n"
"s_waitcnt lgkmcnt(0)\n"
"v_exp_f32 v23, v23\n"
"v_mfma_f32_16x16x32_bf16 a[76:79], v[100:103], v[92:95], a[76:79]\n"
"ds_read_b128 a[192:195], v149 offset:45056\n"
"v_cvt_pk_bf16_f32 v45, v22, v23\n"
"v_mfma_f32_16x16x32_bf16 a[80:83], v[104:107], v[64:67], a[80:83]\n"
"v_exp_f32 v24, v24\n"
"v_mfma_f32_16x16x32_bf16 a[80:83], v[108:111], v[68:71], a[80:83]\n"
"ds_read_b128 a[196:199], v150 offset:45056\n"
"buffer_load_dwordx4 a[184:187], v175, s[44:47], s58 offen offset:0 sc1\n"
"v_mfma_f32_16x16x32_bf16 a[84:87], v[104:107], v[72:75], a[84:87]\n"
"v_exp_f32 v25, v25\n"
"v_mfma_f32_16x16x32_bf16 a[84:87], v[108:111], v[76:79], a[84:87]\n"
"ds_read_b128 a[200:203], v151 offset:45056\n"
"v_cvt_pk_bf16_f32 v52, v24, v25\n"
"v_mfma_f32_16x16x32_bf16 a[88:91], v[104:107], v[80:83], a[88:91]\n"
"s_add_u32 s58, s58, 1024\n"
"v_exp_f32 v26, v26\n"
"v_mfma_f32_16x16x32_bf16 a[88:91], v[108:111], v[84:87], a[88:91]\n"
"ds_read_b128 a[204:207], v152 offset:45056\n"
"buffer_load_dwordx4 a[188:191], v175, s[44:47], s58 offen offset:0 sc1\n"
"v_mfma_f32_16x16x32_bf16 a[92:95], v[104:107], v[88:91], a[92:95]\n"
"v_exp_f32 v27, v27\n"
"v_mfma_f32_16x16x32_bf16 a[92:95], v[108:111], v[92:95], a[92:95]\n"
"v_cvt_pk_bf16_f32 v53, v26, v27\n"
"v_smfmac_f32_16x16x64_bf16 v[136:139], v[144:147], v[80:87], v148\n"
"s_waitcnt lgkmcnt(3)\n"
"v_mfma_f32_16x16x32_bf16 v[16:19], a[192:195], v[176:179], v[112:115]\n"
"s_waitcnt lgkmcnt(2)\n"
"v_exp_f32 v28, v28\n"
"v_mfma_f32_16x16x32_bf16 v[16:19], a[196:199], v[180:183], v[16:19]\n"
"s_waitcnt lgkmcnt(1)\n"
"ds_read_b64_tr_b16 v[96:97], v159 offset:16384\n"
"buffer_load_dwordx4 v243, s[48:51], s60 offen offset:3072 lds\n"
"v_mfma_f32_16x16x32_bf16 v[16:19], a[200:203], v[184:187], v[16:19]\n"
"s_waitcnt lgkmcnt(1)\n"
"v_exp_f32 v29, v29\n"
"v_mfma_f32_16x16x32_bf16 v[16:19], a[204:207], v[188:191], v[16:19]\n"
"ds_read_b64_tr_b16 v[98:99], v159 offset:20480\n"
"v_cvt_pk_bf16_f32 v60, v28, v29\n"
"v_mfma_f32_16x16x32_bf16 v[20:23], a[192:195], v[192:195], v[116:119]\n"
"s_add_u32 s60, s60, 110592\n"
"s_cmp_eq_u32 s60, 1769472\n"
"s_cselect_b32 s60, 0, s60\n"
"v_exp_f32 v30, v30\n"
"v_mfma_f32_16x16x32_bf16 v[20:23], a[196:199], v[196:199], v[20:23]\n"
"ds_read_b64_tr_b16 v[100:101], v159 offset:24576\n"
"buffer_load_dwordx4 a[208:211], v175, s[44:47], s58 offen offset:1024 sc1\n"
"v_mfma_f32_16x16x32_bf16 v[20:23], a[200:203], v[200:203], v[20:23]\n"
"v_exp_f32 v31, v31\n"
"v_mfma_f32_16x16x32_bf16 v[20:23], a[204:207], v[204:207], v[20:23]\n"
"ds_read_b64_tr_b16 v[102:103], v159 offset:28672\n"
"v_cvt_pk_bf16_f32 v61, v30, v31\n"
"v_mfma_f32_16x16x32_bf16 v[24:27], a[192:195], v[208:211], v[120:123]\n"
"v_exp_f32 v16, v16\n"
"v_mfma_f32_16x16x32_bf16 v[24:27], a[196:199], v[212:215], v[24:27]\n"
"ds_read_b64_tr_b16 v[104:105], v160 offset:16384\n"
"buffer_load_dwordx4 a[212:215], v175, s[44:47], s58 offen offset:2048 sc1\n"
"v_mfma_f32_16x16x32_bf16 v[24:27], a[200:203], v[216:219], v[24:27]\n"
"v_exp_f32 v17, v17\n"
"v_mfma_f32_16x16x32_bf16 v[24:27], a[204:207], v[220:223], v[24:27]\n"
"ds_read_b64_tr_b16 v[106:107], v160 offset:20480\n"
"v_cvt_pk_bf16_f32 v38, v16, v17\n"
"v_mfma_f32_16x16x32_bf16 v[28:31], a[192:195], v[224:227], v[124:127]\n"
"v_exp_f32 v18, v18\n"
"v_mfma_f32_16x16x32_bf16 v[28:31], a[196:199], v[228:231], v[28:31]\n"
"ds_read_b64_tr_b16 v[108:109], v160 offset:24576\n"
"buffer_load_dwordx4 a[216:219], v175, s[44:47], s58 offen offset:3072 sc1\n"
"v_mfma_f32_16x16x32_bf16 v[28:31], a[200:203], v[232:235], v[28:31]\n"
"v_exp_f32 v19, v19\n"
"v_mfma_f32_16x16x32_bf16 v[28:31], a[204:207], v[236:239], v[28:31]\n"
"s_waitcnt lgkmcnt(3)\n"
"ds_read_b64_tr_b16 v[110:111], v160 offset:28672\n"
"v_cvt_pk_bf16_f32 v39, v18, v19\n"
"v_mfma_f32_16x16x32_bf16 a[96:99], v[96:99], v[64:67], a[96:99]\n"
"s_add_u32 s58, s58, 4096\n"
"v_exp_f32 v20, v20\n"
"v_mfma_f32_16x16x32_bf16 a[96:99], v[100:103], v[68:71], a[96:99]\n"
"buffer_load_dwordx4 a[220:223], v175, s[44:47], s58 offen offset:0 sc1\n"
"v_mfma_f32_16x16x32_bf16 a[100:103], v[96:99], v[72:75], a[100:103]\n"
"v_exp_f32 v21, v21\n"
"v_mfma_f32_16x16x32_bf16 a[100:103], v[100:103], v[76:79], a[100:103]\n"
"v_cvt_pk_bf16_f32 v46, v20, v21\n"
"v_mfma_f32_16x16x32_bf16 a[104:107], v[96:99], v[80:83], a[104:107]\n"
"s_add_u32 s58, s58, 1024\n"
"s_cmp_eq_u32 s58, 81920\n"
"s_cselect_b32 s58, 0, s58\n"
"v_exp_f32 v22, v22\n"
"v_mfma_f32_16x16x32_bf16 a[104:107], v[100:103], v[84:87], a[104:107]\n"
"v_mfma_f32_16x16x32_bf16 a[108:111], v[96:99], v[88:91], a[108:111]\n"
"s_waitcnt vmcnt(32) lgkmcnt(0)\n"
"v_exp_f32 v23, v23\n"
"v_mfma_f32_16x16x32_bf16 a[108:111], v[100:103], v[92:95], a[108:111]\n"
"s_barrier\n"
"ds_read_b128 a[192:195], v149 offset:49152\n"
"v_cvt_pk_bf16_f32 v47, v22, v23\n"
"v_mfma_f32_16x16x32_bf16 a[112:115], v[104:107], v[64:67], a[112:115]\n"
"v_exp_f32 v24, v24\n"
"v_mfma_f32_16x16x32_bf16 a[112:115], v[108:111], v[68:71], a[112:115]\n"
"ds_read_b128 a[196:199], v150 offset:49152\n"
"v_mfma_f32_16x16x32_bf16 a[116:119], v[104:107], v[72:75], a[116:119]\n"
"v_exp_f32 v25, v25\n"
"v_mfma_f32_16x16x32_bf16 a[116:119], v[108:111], v[76:79], a[116:119]\n"
"ds_read_b128 a[200:203], v151 offset:49152\n"
"v_cvt_pk_bf16_f32 v54, v24, v25\n"
"v_mfma_f32_16x16x32_bf16 a[120:123], v[104:107], v[80:83], a[120:123]\n"
"v_exp_f32 v26, v26\n"
"v_mfma_f32_16x16x32_bf16 a[120:123], v[108:111], v[84:87], a[120:123]\n"
"ds_read_b128 a[204:207], v152 offset:49152\n"
"v_mfma_f32_16x16x32_bf16 a[124:127], v[104:107], v[88:91], a[124:127]\n"
"v_exp_f32 v27, v27\n"
"v_mfma_f32_16x16x32_bf16 a[124:127], v[108:111], v[92:95], a[124:127]\n"
"v_cvt_pk_bf16_f32 v55, v26, v27\n"
"v_smfmac_f32_16x16x64_bf16 v[140:143], v[144:147], v[88:95], v148\n"
"s_waitcnt lgkmcnt(3)\n"
"v_mfma_f32_16x16x32_bf16 v[16:19], a[192:195], v[176:179], v[112:115]\n"
"s_waitcnt lgkmcnt(2)\n"
"v_exp_f32 v28, v28\n"
"v_mfma_f32_16x16x32_bf16 v[16:19], a[196:199], v[180:183], v[16:19]\n"
"s_waitcnt lgkmcnt(1)\n"
"ds_read_b64_tr_b16 v[96:97], v153 offset:32768\n"
"v_mfma_f32_16x16x32_bf16 v[16:19], a[200:203], v[184:187], v[16:19]\n"
"s_waitcnt lgkmcnt(1)\n"
"v_exp_f32 v29, v29\n"
"v_mfma_f32_16x16x32_bf16 v[16:19], a[204:207], v[188:191], v[16:19]\n"
"ds_read_b64_tr_b16 v[98:99], v153 offset:36864\n"
"v_cvt_pk_bf16_f32 v62, v28, v29\n"
"v_mfma_f32_16x16x32_bf16 v[20:23], a[192:195], v[192:195], v[116:119]\n"
"s_add_u32 m0, s61, 0\n"
"v_exp_f32 v30, v30\n"
"v_mfma_f32_16x16x32_bf16 v[20:23], a[196:199], v[196:199], v[20:23]\n"
"ds_read_b64_tr_b16 v[100:101], v153 offset:40960\n"
"buffer_load_dwordx4 v240, s[48:51], s60 offen offset:0 lds\n"
"v_mfma_f32_16x16x32_bf16 v[20:23], a[200:203], v[200:203], v[20:23]\n"
"v_exp_f32 v31, v31\n"
"v_mfma_f32_16x16x32_bf16 v[20:23], a[204:207], v[204:207], v[20:23]\n"
"ds_read_b64_tr_b16 v[102:103], v153 offset:45056\n"
"v_cvt_pk_bf16_f32 v63, v30, v31\n"
"v_mfma_f32_16x16x32_bf16 v[24:27], a[192:195], v[208:211], v[120:123]\n"
"v_exp_f32 v16, v16\n"
"v_mfma_f32_16x16x32_bf16 v[24:27], a[196:199], v[212:215], v[24:27]\n"
"ds_read_b64_tr_b16 v[104:105], v154 offset:32768\n"
"buffer_load_dwordx4 a[224:227], v175, s[44:47], s58 offen offset:0 sc1\n"
"v_mfma_f32_16x16x32_bf16 v[24:27], a[200:203], v[216:219], v[24:27]\n"
"v_exp_f32 v17, v17\n"
"v_mfma_f32_16x16x32_bf16 v[24:27], a[204:207], v[220:223], v[24:27]\n"
"ds_read_b64_tr_b16 v[106:107], v154 offset:36864\n"
"v_cvt_pk_bf16_f32 v64, v16, v17\n"
"v_mfma_f32_16x16x32_bf16 v[28:31], a[192:195], v[224:227], v[124:127]\n"
"v_exp_f32 v18, v18\n"
"v_mfma_f32_16x16x32_bf16 v[28:31], a[196:199], v[228:231], v[28:31]\n"
"ds_read_b64_tr_b16 v[108:109], v154 offset:40960\n"
"buffer_load_dwordx4 v241, s[48:51], s60 offen offset:1024 lds\n"
"v_mfma_f32_16x16x32_bf16 v[28:31], a[200:203], v[232:235], v[28:31]\n"
"v_exp_f32 v19, v19\n"
"v_mfma_f32_16x16x32_bf16 v[28:31], a[204:207], v[236:239], v[28:31]\n"
"s_waitcnt lgkmcnt(3)\n"
"ds_read_b64_tr_b16 v[110:111], v154 offset:45056\n"
"v_cvt_pk_bf16_f32 v65, v18, v19\n"
"v_mfma_f32_16x16x32_bf16 a[0:3], v[96:99], v[32:35], a[0:3]\n"
"v_exp_f32 v20, v20\n"
"v_mfma_f32_16x16x32_bf16 a[0:3], v[100:103], v[36:39], a[0:3]\n"
"buffer_load_dwordx4 a[228:231], v175, s[44:47], s58 offen offset:1024 sc1\n"
"v_mfma_f32_16x16x32_bf16 a[4:7], v[96:99], v[40:43], a[4:7]\n"
"v_exp_f32 v21, v21\n"
"v_mfma_f32_16x16x32_bf16 a[4:7], v[100:103], v[44:47], a[4:7]\n"
"v_cvt_pk_bf16_f32 v72, v20, v21\n"
"v_mfma_f32_16x16x32_bf16 a[8:11], v[96:99], v[48:51], a[8:11]\n"
"v_exp_f32 v22, v22\n"
"v_mfma_f32_16x16x32_bf16 a[8:11], v[100:103], v[52:55], a[8:11]\n"
"buffer_load_dwordx4 v242, s[48:51], s60 offen offset:2048 lds\n"
"v_mfma_f32_16x16x32_bf16 a[12:15], v[96:99], v[56:59], a[12:15]\n"
"s_waitcnt lgkmcnt(0)\n"
"v_exp_f32 v23, v23\n"
"v_mfma_f32_16x16x32_bf16 a[12:15], v[100:103], v[60:63], a[12:15]\n"
"ds_read_b128 a[192:195], v149 offset:53248\n"
"v_cvt_pk_bf16_f32 v73, v22, v23\n"
"v_mfma_f32_16x16x32_bf16 a[16:19], v[104:107], v[32:35], a[16:19]\n"
"v_exp_f32 v24, v24\n"
"v_mfma_f32_16x16x32_bf16 a[16:19], v[108:111], v[36:39], a[16:19]\n"
"ds_read_b128 a[196:199], v150 offset:53248\n"
"buffer_load_dwordx4 a[232:235], v175, s[44:47], s58 offen offset:2048 sc1\n"
"v_mfma_f32_16x16x32_bf16 a[20:23], v[104:107], v[40:43], a[20:23]\n"
"v_exp_f32 v25, v25\n"
"v_mfma_f32_16x16x32_bf16 a[20:23], v[108:111], v[44:47], a[20:23]\n"
"ds_read_b128 a[200:203], v151 offset:53248\n"
"v_cvt_pk_bf16_f32 v80, v24, v25\n"
"v_mfma_f32_16x16x32_bf16 a[24:27], v[104:107], v[48:51], a[24:27]\n"
"v_exp_f32 v26, v26\n"
"v_mfma_f32_16x16x32_bf16 a[24:27], v[108:111], v[52:55], a[24:27]\n"
"ds_read_b128 a[204:207], v152 offset:53248\n"
"buffer_load_dwordx4 v243, s[48:51], s60 offen offset:3072 lds\n"
"v_mfma_f32_16x16x32_bf16 a[28:31], v[104:107], v[56:59], a[28:31]\n"
"v_exp_f32 v27, v27\n"
"v_mfma_f32_16x16x32_bf16 a[28:31], v[108:111], v[60:63], a[28:31]\n"
"v_cvt_pk_bf16_f32 v81, v26, v27\n"
"v_smfmac_f32_16x16x64_bf16 v[128:131], v[144:147], v[32:39], v148\n"
"s_waitcnt lgkmcnt(3)\n"
"v_mfma_f32_16x16x32_bf16 v[16:19], a[192:195], v[176:179], v[112:115]\n"
"s_waitcnt lgkmcnt(2)\n"
"v_exp_f32 v28, v28\n"
"v_mfma_f32_16x16x32_bf16 v[16:19], a[196:199], v[180:183], v[16:19]\n"
"s_waitcnt lgkmcnt(1)\n"
"ds_read_b64_tr_b16 v[96:97], v155 offset:32768\n"
"v_mfma_f32_16x16x32_bf16 v[16:19], a[200:203], v[184:187], v[16:19]\n"
"s_waitcnt lgkmcnt(1)\n"
"v_exp_f32 v29, v29\n"
"v_mfma_f32_16x16x32_bf16 v[16:19], a[204:207], v[188:191], v[16:19]\n"
"ds_read_b64_tr_b16 v[98:99], v155 offset:36864\n"
"v_cvt_pk_bf16_f32 v88, v28, v29\n"
"v_mfma_f32_16x16x32_bf16 v[20:23], a[192:195], v[192:195], v[116:119]\n"
"s_add_u32 s60, s60, 110592\n"
"v_exp_f32 v30, v30\n"
"v_mfma_f32_16x16x32_bf16 v[20:23], a[196:199], v[196:199], v[20:23]\n"
"ds_read_b64_tr_b16 v[100:101], v155 offset:40960\n"
"buffer_load_dwordx4 a[236:239], v175, s[44:47], s58 offen offset:3072 sc1\n"
"v_mfma_f32_16x16x32_bf16 v[20:23], a[200:203], v[200:203], v[20:23]\n"
"v_exp_f32 v31, v31\n"
"v_mfma_f32_16x16x32_bf16 v[20:23], a[204:207], v[204:207], v[20:23]\n"
"ds_read_b64_tr_b16 v[102:103], v155 offset:45056\n"
"v_cvt_pk_bf16_f32 v89, v30, v31\n"
"v_mfma_f32_16x16x32_bf16 v[24:27], a[192:195], v[208:211], v[120:123]\n"
"s_add_u32 s58, s58, 4096\n"
"v_exp_f32 v16, v16\n"
"v_mfma_f32_16x16x32_bf16 v[24:27], a[196:199], v[212:215], v[24:27]\n"
"ds_read_b64_tr_b16 v[104:105], v156 offset:32768\n"
"v_mfma_f32_16x16x32_bf16 v[24:27], a[200:203], v[216:219], v[24:27]\n"
"v_exp_f32 v17, v17\n"
"v_mfma_f32_16x16x32_bf16 v[24:27], a[204:207], v[220:223], v[24:27]\n"
"ds_read_b64_tr_b16 v[106:107], v156 offset:36864\n"
"v_cvt_pk_bf16_f32 v66, v16, v17\n"
"v_mfma_f32_16x16x32_bf16 v[28:31], a[192:195], v[224:227], v[124:127]\n"
"s_add_u32 m0, s61, 4096\n"
"v_exp_f32 v18, v18\n"
"v_mfma_f32_16x16x32_bf16 v[28:31], a[196:199], v[228:231], v[28:31]\n"
"ds_read_b64_tr_b16 v[108:109], v156 offset:40960\n"
"buffer_load_dwordx4 v240, s[48:51], s60 offen offset:0 lds\n"
"v_mfma_f32_16x16x32_bf16 v[28:31], a[200:203], v[232:235], v[28:31]\n"
"v_exp_f32 v19, v19\n"
"v_mfma_f32_16x16x32_bf16 v[28:31], a[204:207], v[236:239], v[28:31]\n"
"s_waitcnt lgkmcnt(3)\n"
"ds_read_b64_tr_b16 v[110:111], v156 offset:45056\n"
"v_cvt_pk_bf16_f32 v67, v18, v19\n"
"v_mfma_f32_16x16x32_bf16 a[32:35], v[96:99], v[32:35], a[32:35]\n"
"v_exp_f32 v20, v20\n"
"v_mfma_f32_16x16x32_bf16 a[32:35], v[100:103], v[36:39], a[32:35]\n"
"buffer_load_dwordx4 a[240:243], v175, s[44:47], s58 offen offset:0 sc1\n"
"v_mfma_f32_16x16x32_bf16 a[36:39], v[96:99], v[40:43], a[36:39]\n"
"v_exp_f32 v21, v21\n"
"v_mfma_f32_16x16x32_bf16 a[36:39], v[100:103], v[44:47], a[36:39]\n"
"v_cvt_pk_bf16_f32 v74, v20, v21\n"
"v_mfma_f32_16x16x32_bf16 a[40:43], v[96:99], v[48:51], a[40:43]\n"
"s_add_u32 s58, s58, 1024\n"
"v_exp_f32 v22, v22\n"
"v_mfma_f32_16x16x32_bf16 a[40:43], v[100:103], v[52:55], a[40:43]\n"
"buffer_load_dwordx4 v241, s[48:51], s60 offen offset:1024 lds\n"
"v_mfma_f32_16x16x32_bf16 a[44:47], v[96:99], v[56:59], a[44:47]\n"
"s_waitcnt lgkmcnt(0)\n"
"v_exp_f32 v23, v23\n"
"v_mfma_f32_16x16x32_bf16 a[44:47], v[100:103], v[60:63], a[44:47]\n"
"ds_read_b128 a[192:195], v149 offset:57344\n"
"v_cvt_pk_bf16_f32 v75, v22, v23\n"
"v_mfma_f32_16x16x32_bf16 a[48:51], v[104:107], v[32:35], a[48:51]\n"
"v_exp_f32 v24, v24\n"
"v_mfma_f32_16x16x32_bf16 a[48:51], v[108:111], v[36:39], a[48:51]\n"
"ds_read_b128 a[196:199], v150 offset:57344\n"
"buffer_load_dwordx4 a[244:247], v175, s[44:47], s58 offen offset:0 sc1\n"
"v_mfma_f32_16x16x32_bf16 a[52:55], v[104:107], v[40:43], a[52:55]\n"
"v_exp_f32 v25, v25\n"
"v_mfma_f32_16x16x32_bf16 a[52:55], v[108:111], v[44:47], a[52:55]\n"
"ds_read_b128 a[200:203], v151 offset:57344\n"
"v_cvt_pk_bf16_f32 v82, v24, v25\n"
"v_mfma_f32_16x16x32_bf16 a[56:59], v[104:107], v[48:51], a[56:59]\n"
"v_exp_f32 v26, v26\n"
"v_mfma_f32_16x16x32_bf16 a[56:59], v[108:111], v[52:55], a[56:59]\n"
"ds_read_b128 a[204:207], v152 offset:57344\n"
"buffer_load_dwordx4 v242, s[48:51], s60 offen offset:2048 lds\n"
"v_mfma_f32_16x16x32_bf16 a[60:63], v[104:107], v[56:59], a[60:63]\n"
"v_exp_f32 v27, v27\n"
"v_mfma_f32_16x16x32_bf16 a[60:63], v[108:111], v[60:63], a[60:63]\n"
"v_cvt_pk_bf16_f32 v83, v26, v27\n"
"v_smfmac_f32_16x16x64_bf16 v[132:135], v[144:147], v[40:47], v148\n"
"s_waitcnt lgkmcnt(3)\n"
"v_mfma_f32_16x16x32_bf16 v[16:19], a[192:195], v[176:179], v[112:115]\n"
"s_waitcnt lgkmcnt(2)\n"
"v_exp_f32 v28, v28\n"
"v_mfma_f32_16x16x32_bf16 v[16:19], a[196:199], v[180:183], v[16:19]\n"
"s_waitcnt lgkmcnt(1)\n"
"ds_read_b64_tr_b16 v[96:97], v157 offset:32768\n"
"buffer_load_dwordx4 a[248:251], v175, s[44:47], s58 offen offset:1024 sc1\n"
"v_mfma_f32_16x16x32_bf16 v[16:19], a[200:203], v[184:187], v[16:19]\n"
"s_waitcnt lgkmcnt(1)\n"
"v_exp_f32 v29, v29\n"
"v_mfma_f32_16x16x32_bf16 v[16:19], a[204:207], v[188:191], v[16:19]\n"
"ds_read_b64_tr_b16 v[98:99], v157 offset:36864\n"
"v_cvt_pk_bf16_f32 v90, v28, v29\n"
"v_mfma_f32_16x16x32_bf16 v[20:23], a[192:195], v[192:195], v[116:119]\n"
"v_exp_f32 v30, v30\n"
"v_mfma_f32_16x16x32_bf16 v[20:23], a[196:199], v[196:199], v[20:23]\n"
"ds_read_b64_tr_b16 v[100:101], v157 offset:40960\n"
"buffer_load_dwordx4 v243, s[48:51], s60 offen offset:3072 lds\n"
"v_mfma_f32_16x16x32_bf16 v[20:23], a[200:203], v[200:203], v[20:23]\n"
"v_exp_f32 v31, v31\n"
"v_mfma_f32_16x16x32_bf16 v[20:23], a[204:207], v[204:207], v[20:23]\n"
"ds_read_b64_tr_b16 v[102:103], v157 offset:45056\n"
"v_cvt_pk_bf16_f32 v91, v30, v31\n"
"v_mfma_f32_16x16x32_bf16 v[24:27], a[192:195], v[208:211], v[120:123]\n"
"s_add_u32 s60, s60, 110592\n"
"s_cmp_eq_u32 s60, 1769472\n"
"s_cselect_b32 s60, 0, s60\n"
"v_exp_f32 v16, v16\n"
"v_mfma_f32_16x16x32_bf16 v[24:27], a[196:199], v[212:215], v[24:27]\n"
"ds_read_b64_tr_b16 v[104:105], v158 offset:32768\n"
"buffer_load_dwordx4 a[252:255], v175, s[44:47], s58 offen offset:2048 sc1\n"
"v_mfma_f32_16x16x32_bf16 v[24:27], a[200:203], v[216:219], v[24:27]\n"
"v_exp_f32 v17, v17\n"
"v_mfma_f32_16x16x32_bf16 v[24:27], a[204:207], v[220:223], v[24:27]\n"
"ds_read_b64_tr_b16 v[106:107], v158 offset:36864\n"
"v_cvt_pk_bf16_f32 v68, v16, v17\n"
"v_mfma_f32_16x16x32_bf16 v[28:31], a[192:195], v[224:227], v[124:127]\n"
"s_add_u32 m0, s61, 65536\n"
"v_exp_f32 v18, v18\n"
"v_mfma_f32_16x16x32_bf16 v[28:31], a[196:199], v[228:231], v[28:31]\n"
"ds_read_b64_tr_b16 v[108:109], v158 offset:40960\n"
"buffer_load_dwordx4 v240, s[48:51], s60 offen offset:0 lds\n"
"v_mfma_f32_16x16x32_bf16 v[28:31], a[200:203], v[232:235], v[28:31]\n"
"v_exp_f32 v19, v19\n"
"v_mfma_f32_16x16x32_bf16 v[28:31], a[204:207], v[236:239], v[28:31]\n"
"s_waitcnt lgkmcnt(3)\n"
"ds_read_b64_tr_b16 v[110:111], v158 offset:45056\n"
"v_cvt_pk_bf16_f32 v69, v18, v19\n"
"v_mfma_f32_16x16x32_bf16 a[64:67], v[96:99], v[32:35], a[64:67]\n"
"v_exp_f32 v20, v20\n"
"v_mfma_f32_16x16x32_bf16 a[64:67], v[100:103], v[36:39], a[64:67]\n"
"buffer_load_dwordx4 v241, s[48:51], s60 offen offset:1024 lds\n"
"v_mfma_f32_16x16x32_bf16 a[68:71], v[96:99], v[40:43], a[68:71]\n"
"v_exp_f32 v21, v21\n"
"v_mfma_f32_16x16x32_bf16 a[68:71], v[100:103], v[44:47], a[68:71]\n"
"v_cvt_pk_bf16_f32 v76, v20, v21\n"
"v_mfma_f32_16x16x32_bf16 a[72:75], v[96:99], v[48:51], a[72:75]\n"
"v_exp_f32 v22, v22\n"
"v_mfma_f32_16x16x32_bf16 a[72:75], v[100:103], v[52:55], a[72:75]\n"
"buffer_load_dwordx4 v242, s[48:51], s60 offen offset:2048 lds\n"
"v_mfma_f32_16x16x32_bf16 a[76:79], v[96:99], v[56:59], a[76:79]\n"
"s_waitcnt lgkmcnt(0)\n"
"v_exp_f32 v23, v23\n"
"v_mfma_f32_16x16x32_bf16 a[76:79], v[100:103], v[60:63], a[76:79]\n"
"ds_read_b128 a[192:195], v149 offset:61440\n"
"v_cvt_pk_bf16_f32 v77, v22, v23\n"
"v_mfma_f32_16x16x32_bf16 a[80:83], v[104:107], v[32:35], a[80:83]\n"
"v_exp_f32 v24, v24\n"
"v_mfma_f32_16x16x32_bf16 a[80:83], v[108:111], v[36:39], a[80:83]\n"
"ds_read_b128 a[196:199], v150 offset:61440\n"
"buffer_load_dwordx4 v243, s[48:51], s60 offen offset:3072 lds\n"
"v_mfma_f32_16x16x32_bf16 a[84:87], v[104:107], v[40:43], a[84:87]\n"
"v_exp_f32 v25, v25\n"
"v_mfma_f32_16x16x32_bf16 a[84:87], v[108:111], v[44:47], a[84:87]\n"
"ds_read_b128 a[200:203], v151 offset:61440\n"
"v_cvt_pk_bf16_f32 v84, v24, v25\n"
"v_mfma_f32_16x16x32_bf16 a[88:91], v[104:107], v[48:51], a[88:91]\n"
"s_add_u32 s60, s60, 110592\n"
"v_exp_f32 v26, v26\n"
"v_mfma_f32_16x16x32_bf16 a[88:91], v[108:111], v[52:55], a[88:91]\n"
"ds_read_b128 a[204:207], v152 offset:61440\n"
"v_mfma_f32_16x16x32_bf16 a[92:95], v[104:107], v[56:59], a[92:95]\n"
"v_exp_f32 v27, v27\n"
"v_mfma_f32_16x16x32_bf16 a[92:95], v[108:111], v[60:63], a[92:95]\n"
"v_cvt_pk_bf16_f32 v85, v26, v27\n"
"v_smfmac_f32_16x16x64_bf16 v[136:139], v[144:147], v[48:55], v148\n"
"s_waitcnt lgkmcnt(3)\n"
"v_mfma_f32_16x16x32_bf16 v[16:19], a[192:195], v[176:179], v[112:115]\n"
"s_waitcnt lgkmcnt(2)\n"
"v_exp_f32 v28, v28\n"
"v_mfma_f32_16x16x32_bf16 v[16:19], a[196:199], v[180:183], v[16:19]\n"
"s_waitcnt lgkmcnt(1)\n"
"ds_read_b64_tr_b16 v[96:97], v159 offset:32768\n"
"v_mfma_f32_16x16x32_bf16 v[16:19], a[200:203], v[184:187], v[16:19]\n"
"s_waitcnt lgkmcnt(1)\n"
"v_exp_f32 v29, v29\n"
"v_mfma_f32_16x16x32_bf16 v[16:19], a[204:207], v[188:191], v[16:19]\n"
"ds_read_b64_tr_b16 v[98:99], v159 offset:36864\n"
"v_cvt_pk_bf16_f32 v92, v28, v29\n"
"v_mfma_f32_16x16x32_bf16 v[20:23], a[192:195], v[192:195], v[116:119]\n"
"s_add_u32 m0, s61, 69632\n"
"v_exp_f32 v30, v30\n"
"v_mfma_f32_16x16x32_bf16 v[20:23], a[196:199], v[196:199], v[20:23]\n"
"ds_read_b64_tr_b16 v[100:101], v159 offset:40960\n"
"buffer_load_dwordx4 v240, s[48:51], s60 offen offset:0 lds\n"
"v_mfma_f32_16x16x32_bf16 v[20:23], a[200:203], v[200:203], v[20:23]\n"
"v_exp_f32 v31, v31\n"
"v_mfma_f32_16x16x32_bf16 v[20:23], a[204:207], v[204:207], v[20:23]\n"
"ds_read_b64_tr_b16 v[102:103], v159 offset:45056\n"
"v_cvt_pk_bf16_f32 v93, v30, v31\n"
"v_mfma_f32_16x16x32_bf16 v[24:27], a[192:195], v[208:211], v[120:123]\n"
"v_exp_f32 v16, v16\n"
"v_mfma_f32_16x16x32_bf16 v[24:27], a[196:199], v[212:215], v[24:27]\n"
"ds_read_b64_tr_b16 v[104:105], v160 offset:32768\n"
"buffer_load_dwordx4 v241, s[48:51], s60 offen offset:1024 lds\n"
"v_mfma_f32_16x16x32_bf16 v[24:27], a[200:203], v[216:219], v[24:27]\n"
"v_exp_f32 v17, v17\n"
"v_mfma_f32_16x16x32_bf16 v[24:27], a[204:207], v[220:223], v[24:27]\n"
"ds_read_b64_tr_b16 v[106:107], v160 offset:36864\n"
"v_cvt_pk_bf16_f32 v70, v16, v17\n"
"v_mfma_f32_16x16x32_bf16 v[28:31], a[192:195], v[224:227], v[124:127]\n"
"v_exp_f32 v18, v18\n"
"v_mfma_f32_16x16x32_bf16 v[28:31], a[196:199], v[228:231], v[28:31]\n"
"ds_read_b64_tr_b16 v[108:109], v160 offset:40960\n"
"buffer_load_dwordx4 v242, s[48:51], s60 offen offset:2048 lds\n"
"v_mfma_f32_16x16x32_bf16 v[28:31], a[200:203], v[232:235], v[28:31]\n"
"v_exp_f32 v19, v19\n"
"v_mfma_f32_16x16x32_bf16 v[28:31], a[204:207], v[236:239], v[28:31]\n"
"s_waitcnt lgkmcnt(3)\n"
"ds_read_b64_tr_b16 v[110:111], v160 offset:45056\n"
"v_cvt_pk_bf16_f32 v71, v18, v19\n"
"v_mfma_f32_16x16x32_bf16 a[96:99], v[96:99], v[32:35], a[96:99]\n"
"v_exp_f32 v20, v20\n"
"v_mfma_f32_16x16x32_bf16 a[96:99], v[100:103], v[36:39], a[96:99]\n"
"buffer_load_dwordx4 v243, s[48:51], s60 offen offset:3072 lds\n"
"v_mfma_f32_16x16x32_bf16 a[100:103], v[96:99], v[40:43], a[100:103]\n"
"v_exp_f32 v21, v21\n"
"v_mfma_f32_16x16x32_bf16 a[100:103], v[100:103], v[44:47], a[100:103]\n"
"v_cvt_pk_bf16_f32 v78, v20, v21\n"
"v_mfma_f32_16x16x32_bf16 a[104:107], v[96:99], v[48:51], a[104:107]\n"
"s_add_u32 s60, s60, 110592\n"
"s_cmp_eq_u32 s60, 1769472\n"
"s_cselect_b32 s60, 0, s60\n"
"v_exp_f32 v22, v22\n"
"v_mfma_f32_16x16x32_bf16 a[104:107], v[100:103], v[52:55], a[104:107]\n"
"v_mfma_f32_16x16x32_bf16 a[108:111], v[96:99], v[56:59], a[108:111]\n"
"s_waitcnt vmcnt(52) lgkmcnt(0)\n"
"v_exp_f32 v23, v23\n"
"v_mfma_f32_16x16x32_bf16 a[108:111], v[100:103], v[60:63], a[108:111]\n"
"s_barrier\n"
"ds_read_b64_tr_b16 v[96:97], v153 offset:49152\n"
"v_cvt_pk_bf16_f32 v79, v22, v23\n"
"v_mfma_f32_16x16x32_bf16 a[112:115], v[104:107], v[32:35], a[112:115]\n"
"v_exp_f32 v24, v24\n"
"v_mfma_f32_16x16x32_bf16 a[112:115], v[108:111], v[36:39], a[112:115]\n"
"ds_read_b64_tr_b16 v[98:99], v153 offset:53248\n"
"v_mfma_f32_16x16x32_bf16 a[116:119], v[104:107], v[40:43], a[116:119]\n"
"v_exp_f32 v25, v25\n"
"v_mfma_f32_16x16x32_bf16 a[116:119], v[108:111], v[44:47], a[116:119]\n"
"ds_read_b64_tr_b16 v[100:101], v153 offset:57344\n"
"v_cvt_pk_bf16_f32 v86, v24, v25\n"
"v_mfma_f32_16x16x32_bf16 a[120:123], v[104:107], v[48:51], a[120:123]\n"
"v_exp_f32 v26, v26\n"
"v_mfma_f32_16x16x32_bf16 a[120:123], v[108:111], v[52:55], a[120:123]\n"
"ds_read_b64_tr_b16 v[102:103], v153 offset:61440\n"
"v_mfma_f32_16x16x32_bf16 a[124:127], v[104:107], v[56:59], a[124:127]\n"
"v_exp_f32 v27, v27\n"
"v_mfma_f32_16x16x32_bf16 a[124:127], v[108:111], v[60:63], a[124:127]\n"
"v_cvt_pk_bf16_f32 v87, v26, v27\n"
"v_smfmac_f32_16x16x64_bf16 v[140:143], v[144:147], v[56:63], v148\n"
"v_smfmac_f32_16x16x64_bf16 v[128:131], v[144:147], v[64:71], v148\n"
"ds_read_b64_tr_b16 v[112:113], v154 offset:49152\n"
"v_exp_f32 v28, v28\n"
"v_smfmac_f32_16x16x64_bf16 v[132:135], v[144:147], v[72:79], v148\n"
"ds_read_b64_tr_b16 v[114:115], v154 offset:53248\n"
"v_exp_f32 v29, v29\n"
"v_smfmac_f32_16x16x64_bf16 v[136:139], v[144:147], v[80:87], v148\n"
"ds_read_b64_tr_b16 v[116:117], v154 offset:57344\n"
"v_exp_f32 v30, v30\n"
"ds_read_b64_tr_b16 v[118:119], v154 offset:61440\n"
"v_exp_f32 v31, v31\n"
"s_mov_b32 s68, 0x5d800000\n"
"v_cvt_pk_bf16_f32 v94, v28, v29\n"
"v_cvt_pk_bf16_f32 v95, v30, v31\n"
"s_lshr_b32 s70, s64, 10\n"
"s_waitcnt lgkmcnt(4)\n"
"v_smfmac_f32_16x16x64_bf16 v[140:143], v[144:147], v[88:95], v148\n"
"v_mfma_f32_16x16x32_bf16 a[0:3], v[96:99], v[64:67], a[0:3]\n"
"s_add_u32 m0, s61, 32768\n"
"ds_read_b64_tr_b16 v[120:121], v155 offset:49152\n"
"buffer_load_dwordx4 v240, s[48:51], s60 offen offset:0 lds\n"
"v_mfma_f32_16x16x32_bf16 a[0:3], v[100:103], v[68:71], a[0:3]\n"
"ds_read_b64_tr_b16 v[122:123], v155 offset:53248\n"
"buffer_load_dwordx4 v241, s[48:51], s60 offen offset:1024 lds\n"
"v_mov_b32 v6, 98304\n"
"v_add_f32 v128, v128, v129\n"
"v_mfma_f32_16x16x32_bf16 a[4:7], v[96:99], v[72:75], a[4:7]\n"
"ds_read_b64_tr_b16 v[124:125], v155 offset:57344\n"
"buffer_load_dwordx4 v242, s[48:51], s60 offen offset:2048 lds\n"
"v_add_f32 v132, v132, v133\n"
"v_mfma_f32_16x16x32_bf16 a[4:7], v[100:103], v[76:79], a[4:7]\n"
"s_waitcnt lgkmcnt(3)\n"
"ds_read_b64_tr_b16 v[126:127], v155 offset:61440\n"
"buffer_load_dwordx4 v243, s[48:51], s60 offen offset:3072 lds\n"
"v_add_f32 v136, v136, v137\n"
"v_mfma_f32_16x16x32_bf16 a[16:19], v[112:115], v[64:67], a[16:19]\n"
"s_add_u32 s60, s60, 110592\n"
"ds_read_b64_tr_b16 v[32:33], v156 offset:49152\n"
"v_add_f32 v140, v140, v141\n"
"v_mfma_f32_16x16x32_bf16 a[16:19], v[116:119], v[68:71], a[16:19]\n"
"s_add_u32 m0, s61, 36864\n"
"ds_read_b64_tr_b16 v[34:35], v156 offset:53248\n"
"buffer_load_dwordx4 v240, s[48:51], s60 offen offset:0 lds\n"
"v_add_f32 v5, v128, v132\n"
"v_mfma_f32_16x16x32_bf16 a[20:23], v[112:115], v[72:75], a[20:23]\n"
"ds_read_b64_tr_b16 v[36:37], v156 offset:57344\n"
"buffer_load_dwordx4 v241, s[48:51], s60 offen offset:1024 lds\n"
"v_add_f32 v4, v136, v140\n"
"v_mfma_f32_16x16x32_bf16 a[20:23], v[116:119], v[76:79], a[20:23]\n"
"s_waitcnt lgkmcnt(3)\n"
"ds_read_b64_tr_b16 v[38:39], v156 offset:61440\n"
"buffer_load_dwordx4 v242, s[48:51], s60 offen offset:2048 lds\n"
"v_add_f32 v4, v4, v5\n"
"v_mfma_f32_16x16x32_bf16 a[32:35], v[120:123], v[64:67], a[32:35]\n"
"ds_read_b64_tr_b16 v[40:41], v157 offset:49152\n"
"buffer_load_dwordx4 v243, s[48:51], s60 offen offset:3072 lds\n"
"v_cmp_nge_f32 vcc, s68, v4\n"
"v_mfma_f32_16x16x32_bf16 a[32:35], v[124:127], v[68:71], a[32:35]\n"
"s_add_u32 s60, s60, 110592\n"
"s_cmp_eq_u32 s60, 1769472\n"
"s_cselect_b32 s60, 0, s60\n"
"ds_read_b64_tr_b16 v[42:43], v157 offset:53248\n"
"buffer_load_dwordx4 a[192:195], v175, s[44:47], s58 offen offset:3072 sc1\n"
"v_add_u32 v7, s70, v6\n"
"v_mfma_f32_16x16x32_bf16 a[36:39], v[120:123], v[72:75], a[36:39]\n"
"s_cmp_lg_u64 vcc, 0\n"
"s_cselect_b32 s69, 1, 0\n"
"ds_read_b64_tr_b16 v[44:45], v157 offset:57344\n"
"v_mov_b32 v5, s69\n"
"v_mfma_f32_16x16x32_bf16 a[36:39], v[124:127], v[76:79], a[36:39]\n"
"s_waitcnt lgkmcnt(3)\n"
"ds_read_b64_tr_b16 v[46:47], v157 offset:61440\n"
"v_mfma_f32_16x16x32_bf16 a[48:51], v[32:35], v[64:67], a[48:51]\n"
"s_add_u32 s58, s58, 4096\n"
"ds_write_b32 v7, v5\n"
"ds_read_b64_tr_b16 v[48:49], v158 offset:49152\n"
"buffer_load_dwordx4 a[196:199], v175, s[44:47], s58 offen offset:0 sc1\n"
"v_mfma_f32_16x16x32_bf16 a[48:51], v[36:39], v[68:71], a[48:51]\n"
"s_add_u32 s58, s58, 1024\n"
"s_cmp_eq_u32 s58, 81920\n"
"s_cselect_b32 s58, 0, s58\n"
"ds_read_b64_tr_b16 v[50:51], v158 offset:53248\n"
"v_rcp_f32 v130, v128\n"
"v_mfma_f32_16x16x32_bf16 a[52:55], v[32:35], v[72:75], a[52:55]\n"
"ds_read_b64_tr_b16 v[52:53], v158 offset:57344\n"
"v_rcp_f32 v134, v132\n"
"v_mfma_f32_16x16x32_bf16 a[52:55], v[36:39], v[76:79], a[52:55]\n"
"s_waitcnt lgkmcnt(3)\n"
"ds_read_b64_tr_b16 v[54:55], v158 offset:61440\n"
"v_mfma_f32_16x16x32_bf16 a[64:67], v[40:43], v[64:67], a[64:67]\n"
"s_barrier\n"
"ds_read_b64_tr_b16 v[56:57], v159 offset:49152\n"
"v_mfma_f32_16x16x32_bf16 a[64:67], v[44:47], v[68:71], a[64:67]\n"
"ds_read_b128 v[8:11], v6\n"
"ds_read_b64_tr_b16 v[58:59], v159 offset:53248\n"
"v_rcp_f32 v138, v136\n"
"v_mfma_f32_16x16x32_bf16 a[68:71], v[40:43], v[72:75], a[68:71]\n"
"ds_read_b64_tr_b16 v[60:61], v159 offset:57344\n"
"v_rcp_f32 v142, v140\n"
"v_mfma_f32_16x16x32_bf16 a[68:71], v[44:47], v[76:79], a[68:71]\n"
"s_waitcnt lgkmcnt(4)\n"
"ds_read_b64_tr_b16 v[62:63], v159 offset:61440\n"
"v_mfma_f32_16x16x32_bf16 a[80:83], v[48:51], v[64:67], a[80:83]\n"
"ds_read_b64_tr_b16 v[104:105], v160 offset:49152\n"
"v_mov_b32 v139, v138\n"
"v_mfma_f32_16x16x32_bf16 a[80:83], v[52:55], v[68:71], a[80:83]\n"
"ds_read_b64_tr_b16 v[106:107], v160 offset:53248\n"
"v_mov_b32 v143, v142\n"
"v_mfma_f32_16x16x32_bf16 a[84:87], v[48:51], v[72:75], a[84:87]\n"
"ds_read_b64_tr_b16 v[108:109], v160 offset:57344\n"
"v_mfma_f32_16x16x32_bf16 a[84:87], v[52:55], v[76:79], a[84:87]\n"
"s_waitcnt lgkmcnt(3)\n"
"ds_read_b64_tr_b16 v[110:111], v160 offset:61440\n"
"v_mfma_f32_16x16x32_bf16 a[96:99], v[56:59], v[64:67], a[96:99]\n"
"v_or_b32 v8, v8, v9\n"
"v_mfma_f32_16x16x32_bf16 a[96:99], v[60:63], v[68:71], a[96:99]\n"
"v_or3_b32 v8, v8, v10, v11\n"
"v_mfma_f32_16x16x32_bf16 a[100:103], v[56:59], v[72:75], a[100:103]\n"
"v_readfirstlane_b32 s68, v8\n"
"v_mfma_f32_16x16x32_bf16 a[100:103], v[60:63], v[76:79], a[100:103]\n"
"s_waitcnt lgkmcnt(0)\n"
"v_mfma_f32_16x16x32_bf16 a[112:115], v[104:107], v[64:67], a[112:115]\n"
"s_andn2_b32 s68, s68, s66\n"
"s_cmp_lg_u32 s68, 0\n"
"s_cbranch_scc1 L_fallback_%=\n"
"v_mfma_f32_16x16x32_bf16 a[112:115], v[108:111], v[68:71], a[112:115]\n"
"v_mfma_f32_16x16x32_bf16 a[116:119], v[104:107], v[72:75], a[116:119]\n"
"v_mfma_f32_16x16x32_bf16 a[116:119], v[108:111], v[76:79], a[116:119]\n"
"v_mfma_f32_16x16x32_bf16 a[8:11], v[96:99], v[80:83], a[8:11]\n"
"s_barrier\n"
"v_mfma_f32_16x16x32_bf16 a[8:11], v[100:103], v[84:87], a[8:11]\n"
"v_mfma_f32_16x16x32_bf16 a[24:27], v[112:115], v[80:83], a[24:27]\n"
"v_accvgpr_read_b32 v144, a0\n"
"v_accvgpr_read_b32 v145, a1\n"
"v_accvgpr_read_b32 v146, a2\n"
"v_accvgpr_read_b32 v147, a3\n"
"v_accvgpr_read_b32 v148, a16\n"
"v_accvgpr_read_b32 v149, a17\n"
"v_mfma_f32_16x16x32_bf16 a[24:27], v[116:119], v[84:87], a[24:27]\n"
"v_accvgpr_read_b32 v150, a18\n"
"v_accvgpr_read_b32 v151, a19\n"
"v_mul_f32 v144, v144, v130\n"
"v_mul_f32 v145, v145, v130\n"
"v_mul_f32 v146, v146, v130\n"
"v_mul_f32 v147, v147, v130\n"
"v_mfma_f32_16x16x32_bf16 a[40:43], v[120:123], v[80:83], a[40:43]\n"
"v_mul_f32 v148, v148, v130\n"
"v_mul_f32 v149, v149, v130\n"
"v_mul_f32 v150, v150, v130\n"
"v_mul_f32 v151, v151, v130\n"
"v_cvt_pk_bf16_f32 v176, v144, v145\n"
"v_cvt_pk_bf16_f32 v177, v146, v147\n"
"v_mfma_f32_16x16x32_bf16 a[40:43], v[124:127], v[84:87], a[40:43]\n"
"v_cvt_pk_bf16_f32 v178, v148, v149\n"
"v_cvt_pk_bf16_f32 v179, v150, v151\n"
"v_mfma_f32_16x16x32_bf16 a[56:59], v[32:35], v[80:83], a[56:59]\n"
"s_mov_b32 s67, 0\n"
"buffer_store_dwordx4 v[176:179], v175, s[52:55], s67 offen offset:0 sc1\n"
"v_accvgpr_read_b32 v152, a32\n"
"v_accvgpr_read_b32 v153, a33\n"
"v_accvgpr_read_b32 v154, a34\n"
"v_accvgpr_read_b32 v155, a35\n"
"v_accvgpr_read_b32 v156, a48\n"
"v_mfma_f32_16x16x32_bf16 a[56:59], v[36:39], v[84:87], a[56:59]\n"
"v_accvgpr_read_b32 v157, a49\n"
"v_accvgpr_read_b32 v158, a50\n"
"v_accvgpr_read_b32 v159, a51\n"
"v_mul_f32 v152, v152, v130\n"
"v_mul_f32 v153, v153, v130\n"
"v_mul_f32 v154, v154, v130\n"
"v_mfma_f32_16x16x32_bf16 a[72:75], v[40:43], v[80:83], a[72:75]\n"
"v_mul_f32 v155, v155, v130\n"
"v_mul_f32 v156, v156, v130\n"
"v_mul_f32 v157, v157, v130\n"
"v_mul_f32 v158, v158, v130\n"
"v_mul_f32 v159, v159, v130\n"
"v_cvt_pk_bf16_f32 v180, v152, v153\n"
"v_mfma_f32_16x16x32_bf16 a[72:75], v[44:47], v[84:87], a[72:75]\n"
"v_cvt_pk_bf16_f32 v181, v154, v155\n"
"v_cvt_pk_bf16_f32 v182, v156, v157\n"
"v_cvt_pk_bf16_f32 v183, v158, v159\n"
"v_mfma_f32_16x16x32_bf16 a[88:91], v[48:51], v[80:83], a[88:91]\n"
"s_mov_b32 s67, 0\n"
"s_waitcnt vmcnt(62)\n"
"buffer_store_dwordx4 v[180:183], v175, s[52:55], s67 offen offset:1024 sc1\n"
"v_accvgpr_read_b32 v144, a64\n"
"v_accvgpr_read_b32 v145, a65\n"
"v_accvgpr_read_b32 v146, a66\n"
"v_accvgpr_read_b32 v147, a67\n"
"v_accvgpr_read_b32 v148, a80\n"
"v_mfma_f32_16x16x32_bf16 a[88:91], v[52:55], v[84:87], a[88:91]\n"
"v_accvgpr_read_b32 v149, a81\n"
"v_accvgpr_read_b32 v150, a82\n"
"v_accvgpr_read_b32 v151, a83\n"
"v_mul_f32 v144, v144, v130\n"
"v_mul_f32 v145, v145, v130\n"
"v_mul_f32 v146, v146, v130\n"
"v_mfma_f32_16x16x32_bf16 a[104:107], v[56:59], v[80:83], a[104:107]\n"
"v_mul_f32 v147, v147, v130\n"
"v_mul_f32 v148, v148, v130\n"
"v_mul_f32 v149, v149, v130\n"
"v_mul_f32 v150, v150, v130\n"
"v_mul_f32 v151, v151, v130\n"
"v_cvt_pk_bf16_f32 v184, v144, v145\n"
"v_mfma_f32_16x16x32_bf16 a[104:107], v[60:63], v[84:87], a[104:107]\n"
"v_cvt_pk_bf16_f32 v185, v146, v147\n"
"v_cvt_pk_bf16_f32 v186, v148, v149\n"
"v_cvt_pk_bf16_f32 v187, v150, v151\n"
"v_mfma_f32_16x16x32_bf16 a[120:123], v[104:107], v[80:83], a[120:123]\n"
"s_mov_b32 s67, 0\n"
"s_waitcnt vmcnt(62)\n"
"buffer_store_dwordx4 v[184:187], v175, s[52:55], s67 offen offset:2048 sc1\n"
"v_accvgpr_read_b32 v152, a96\n"
"v_accvgpr_read_b32 v153, a97\n"
"v_accvgpr_read_b32 v154, a98\n"
"v_accvgpr_read_b32 v155, a99\n"
"v_accvgpr_read_b32 v156, a112\n"
"v_mfma_f32_16x16x32_bf16 a[120:123], v[108:111], v[84:87], a[120:123]\n"
"v_accvgpr_read_b32 v157, a113\n"
"v_accvgpr_read_b32 v158, a114\n"
"v_accvgpr_read_b32 v159, a115\n"
"v_mul_f32 v152, v152, v130\n"
"v_mul_f32 v153, v153, v130\n"
"v_mul_f32 v154, v154, v130\n"
"v_mfma_f32_16x16x32_bf16 a[12:15], v[96:99], v[88:91], a[12:15]\n"
"v_mul_f32 v155, v155, v130\n"
"v_mul_f32 v156, v156, v130\n"
"v_mul_f32 v157, v157, v130\n"
"v_mul_f32 v158, v158, v130\n"
"v_mul_f32 v159, v159, v130\n"
"v_cvt_pk_bf16_f32 v188, v152, v153\n"
"v_mfma_f32_16x16x32_bf16 a[12:15], v[100:103], v[92:95], a[12:15]\n"
"v_cvt_pk_bf16_f32 v189, v154, v155\n"
"v_cvt_pk_bf16_f32 v190, v156, v157\n"
"v_cvt_pk_bf16_f32 v191, v158, v159\n"
"v_mfma_f32_16x16x32_bf16 a[28:31], v[112:115], v[88:91], a[28:31]\n"
"s_mov_b32 s67, 0\n"
"s_waitcnt vmcnt(62)\n"
"buffer_store_dwordx4 v[188:191], v175, s[52:55], s67 offen offset:3072 sc1\n"
"v_accvgpr_read_b32 v152, a4\n"
"v_accvgpr_read_b32 v153, a5\n"
"v_accvgpr_read_b32 v154, a6\n"
"v_accvgpr_read_b32 v155, a7\n"
"v_accvgpr_read_b32 v156, a20\n"
"v_mfma_f32_16x16x32_bf16 a[28:31], v[116:119], v[92:95], a[28:31]\n"
"v_accvgpr_read_b32 v157, a21\n"
"v_accvgpr_read_b32 v158, a22\n"
"v_accvgpr_read_b32 v159, a23\n"
"v_mul_f32 v152, v152, v134\n"
"v_mul_f32 v153, v153, v134\n"
"v_mul_f32 v154, v154, v134\n"
"v_mfma_f32_16x16x32_bf16 a[44:47], v[120:123], v[88:91], a[44:47]\n"
"v_mul_f32 v155, v155, v134\n"
"v_mul_f32 v156, v156, v134\n"
"v_mul_f32 v157, v157, v134\n"
"v_mul_f32 v158, v158, v134\n"
"v_mul_f32 v159, v159, v134\n"
"v_cvt_pk_bf16_f32 v192, v152, v153\n"
"v_mfma_f32_16x16x32_bf16 a[44:47], v[124:127], v[92:95], a[44:47]\n"
"v_cvt_pk_bf16_f32 v193, v154, v155\n"
"v_cvt_pk_bf16_f32 v194, v156, v157\n"
"v_cvt_pk_bf16_f32 v195, v158, v159\n"
"v_mfma_f32_16x16x32_bf16 a[60:63], v[32:35], v[88:91], a[60:63]\n"
"s_mov_b32 s67, 131072\n"
"s_waitcnt vmcnt(62)\n"
"buffer_store_dwordx4 v[192:195], v175, s[52:55], s67 offen offset:0 sc1\n"
"v_accvgpr_read_b32 v144, a36\n"
"v_accvgpr_read_b32 v145, a37\n"
"v_accvgpr_read_b32 v146, a38\n"
"v_accvgpr_read_b32 v147, a39\n"
"v_accvgpr_read_b32 v148, a52\n"
"v_mfma_f32_16x16x32_bf16 a[60:63], v[36:39], v[92:95], a[60:63]\n"
"v_accvgpr_read_b32 v149, a53\n"
"v_accvgpr_read_b32 v150, a54\n"
"v_accvgpr_read_b32 v151, a55\n"
"v_mul_f32 v144, v144, v134\n"
"v_mul_f32 v145, v145, v134\n"
"v_mul_f32 v146, v146, v134\n"
"v_mfma_f32_16x16x32_bf16 a[76:79], v[40:43], v[88:91], a[76:79]\n"
"v_mul_f32 v147, v147, v134\n"
"v_mul_f32 v148, v148, v134\n"
"v_mul_f32 v149, v149, v134\n"
"v_mul_f32 v150, v150, v134\n"
"v_mul_f32 v151, v151, v134\n"
"v_cvt_pk_bf16_f32 v196, v144, v145\n"
"v_mfma_f32_16x16x32_bf16 a[76:79], v[44:47], v[92:95], a[76:79]\n"
"v_cvt_pk_bf16_f32 v197, v146, v147\n"
"v_cvt_pk_bf16_f32 v198, v148, v149\n"
"v_cvt_pk_bf16_f32 v199, v150, v151\n"
"v_mfma_f32_16x16x32_bf16 a[92:95], v[48:51], v[88:91], a[92:95]\n"
"s_mov_b32 s67, 131072\n"
"s_waitcnt vmcnt(62)\n"
"buffer_store_dwordx4 v[196:199], v175, s[52:55], s67 offen offset:1024 sc1\n"
"v_accvgpr_read_b32 v152, a68\n"
"v_accvgpr_read_b32 v153, a69\n"
"v_accvgpr_read_b32 v154, a70\n"
"v_accvgpr_read_b32 v155, a71\n"
"v_accvgpr_read_b32 v156, a84\n"
"v_mfma_f32_16x16x32_bf16 a[92:95], v[52:55], v[92:95], a[92:95]\n"
"v_accvgpr_read_b32 v157, a85\n"
"v_accvgpr_read_b32 v158, a86\n"
"v_accvgpr_read_b32 v159, a87\n"
"v_mul_f32 v152, v152, v134\n"
"v_mul_f32 v153, v153, v134\n"
"v_mul_f32 v154, v154, v134\n"
"v_mfma_f32_16x16x32_bf16 a[108:111], v[56:59], v[88:91], a[108:111]\n"
"v_mul_f32 v155, v155, v134\n"
"v_mul_f32 v156, v156, v134\n"
"v_mul_f32 v157, v157, v134\n"
"v_mul_f32 v158, v158, v134\n"
"v_mul_f32 v159, v159, v134\n"
"v_cvt_pk_bf16_f32 v200, v152, v153\n"
"v_mfma_f32_16x16x32_bf16 a[108:111], v[60:63], v[92:95], a[108:111]\n"
"v_cvt_pk_bf16_f32 v201, v154, v155\n"
"v_cvt_pk_bf16_f32 v202, v156, v157\n"
"v_cvt_pk_bf16_f32 v203, v158, v159\n"
"v_mfma_f32_16x16x32_bf16 a[124:127], v[104:107], v[88:91], a[124:127]\n"
"s_mov_b32 s67, 131072\n"
"s_waitcnt vmcnt(62)\n"
"buffer_store_dwordx4 v[200:203], v175, s[52:55], s67 offen offset:2048 sc1\n"
"v_accvgpr_read_b32 v144, a100\n"
"v_accvgpr_read_b32 v145, a101\n"
"v_accvgpr_read_b32 v146, a102\n"
"v_accvgpr_read_b32 v147, a103\n"
"v_accvgpr_read_b32 v148, a116\n"
"v_mfma_f32_16x16x32_bf16 a[124:127], v[108:111], v[92:95], a[124:127]\n"
"v_accvgpr_read_b32 v149, a117\n"
"v_accvgpr_read_b32 v150, a118\n"
"v_accvgpr_read_b32 v151, a119\n"
"v_mul_f32 v144, v144, v134\n"
"v_mul_f32 v145, v145, v134\n"
"v_mul_f32 v146, v146, v134\n"
"v_mul_f32 v147, v147, v134\n"
"v_mul_f32 v148, v148, v134\n"
"v_mul_f32 v149, v149, v134\n"
"v_mul_f32 v150, v150, v134\n"
"v_mul_f32 v151, v151, v134\n"
"v_cvt_pk_bf16_f32 v204, v144, v145\n"
"v_cvt_pk_bf16_f32 v205, v146, v147\n"
"v_cvt_pk_bf16_f32 v206, v148, v149\n"
"v_cvt_pk_bf16_f32 v207, v150, v151\n"
"s_mov_b32 s67, 131072\n"
"s_waitcnt vmcnt(62)\n"
"buffer_store_dwordx4 v[204:207], v175, s[52:55], s67 offen offset:3072 sc1\n"
"v_accvgpr_read_b32 v144, a8\n"
"v_accvgpr_read_b32 v145, a9\n"
"v_accvgpr_read_b32 v146, a10\n"
"v_accvgpr_read_b32 v147, a11\n"
"v_accvgpr_read_b32 v148, a24\n"
"v_accvgpr_read_b32 v149, a25\n"
"v_accvgpr_read_b32 v150, a26\n"
"v_accvgpr_read_b32 v151, a27\n"
"v_pk_mul_f32 v[144:145], v[144:145], v[138:139]\n"
"v_pk_mul_f32 v[146:147], v[146:147], v[138:139]\n"
"v_pk_mul_f32 v[148:149], v[148:149], v[138:139]\n"
"v_pk_mul_f32 v[150:151], v[150:151], v[138:139]\n"
"v_cvt_pk_bf16_f32 v208, v144, v145\n"
"v_cvt_pk_bf16_f32 v209, v146, v147\n"
"v_cvt_pk_bf16_f32 v210, v148, v149\n"
"v_cvt_pk_bf16_f32 v211, v150, v151\n"
"s_mov_b32 s67, 262144\n"
"s_waitcnt vmcnt(62)\n"
"buffer_store_dwordx4 v[208:211], v175, s[52:55], s67 offen offset:0 sc1\n"
"v_accvgpr_read_b32 v152, a40\n"
"v_accvgpr_read_b32 v153, a41\n"
"v_accvgpr_read_b32 v154, a42\n"
"v_accvgpr_read_b32 v155, a43\n"
"v_accvgpr_read_b32 v156, a56\n"
"v_accvgpr_read_b32 v157, a57\n"
"v_accvgpr_read_b32 v158, a58\n"
"v_accvgpr_read_b32 v159, a59\n"
"v_pk_mul_f32 v[152:153], v[152:153], v[138:139]\n"
"v_pk_mul_f32 v[154:155], v[154:155], v[138:139]\n"
"v_pk_mul_f32 v[156:157], v[156:157], v[138:139]\n"
"v_pk_mul_f32 v[158:159], v[158:159], v[138:139]\n"
"v_cvt_pk_bf16_f32 v212, v152, v153\n"
"v_cvt_pk_bf16_f32 v213, v154, v155\n"
"v_cvt_pk_bf16_f32 v214, v156, v157\n"
"v_cvt_pk_bf16_f32 v215, v158, v159\n"
"s_mov_b32 s67, 262144\n"
"s_waitcnt vmcnt(62)\n"
"buffer_store_dwordx4 v[212:215], v175, s[52:55], s67 offen offset:1024 sc1\n"
"v_accvgpr_read_b32 v144, a72\n"
"v_accvgpr_read_b32 v145, a73\n"
"v_accvgpr_read_b32 v146, a74\n"
"v_accvgpr_read_b32 v147, a75\n"
"v_accvgpr_read_b32 v148, a88\n"
"v_accvgpr_read_b32 v149, a89\n"
"v_accvgpr_read_b32 v150, a90\n"
"v_accvgpr_read_b32 v151, a91\n"
"v_pk_mul_f32 v[144:145], v[144:145], v[138:139]\n"
"v_pk_mul_f32 v[146:147], v[146:147], v[138:139]\n"
"v_pk_mul_f32 v[148:149], v[148:149], v[138:139]\n"
"v_pk_mul_f32 v[150:151], v[150:151], v[138:139]\n"
"v_cvt_pk_bf16_f32 v216, v144, v145\n"
"v_cvt_pk_bf16_f32 v217, v146, v147\n"
"v_cvt_pk_bf16_f32 v218, v148, v149\n"
"v_cvt_pk_bf16_f32 v219, v150, v151\n"
"s_mov_b32 s67, 262144\n"
"s_waitcnt vmcnt(62)\n"
"buffer_store_dwordx4 v[216:219], v175, s[52:55], s67 offen offset:2048 sc1\n"
"v_accvgpr_read_b32 v152, a104\n"
"v_accvgpr_read_b32 v153, a105\n"
"v_accvgpr_read_b32 v154, a106\n"
"v_accvgpr_read_b32 v155, a107\n"
"v_accvgpr_read_b32 v156, a120\n"
"v_accvgpr_read_b32 v157, a121\n"
"v_accvgpr_read_b32 v158, a122\n"
"v_accvgpr_read_b32 v159, a123\n"
"v_pk_mul_f32 v[152:153], v[152:153], v[138:139]\n"
"v_pk_mul_f32 v[154:155], v[154:155], v[138:139]\n"
"v_pk_mul_f32 v[156:157], v[156:157], v[138:139]\n"
"v_pk_mul_f32 v[158:159], v[158:159], v[138:139]\n"
"v_cvt_pk_bf16_f32 v220, v152, v153\n"
"v_cvt_pk_bf16_f32 v221, v154, v155\n"
"v_cvt_pk_bf16_f32 v222, v156, v157\n"
"v_cvt_pk_bf16_f32 v223, v158, v159\n"
"s_mov_b32 s67, 262144\n"
"s_waitcnt vmcnt(62)\n"
"buffer_store_dwordx4 v[220:223], v175, s[52:55], s67 offen offset:3072 sc1\n"
"v_accvgpr_read_b32 v152, a12\n"
"v_accvgpr_read_b32 v153, a13\n"
"v_accvgpr_read_b32 v154, a14\n"
"v_accvgpr_read_b32 v155, a15\n"
"v_accvgpr_read_b32 v156, a28\n"
"v_accvgpr_read_b32 v157, a29\n"
"v_accvgpr_read_b32 v158, a30\n"
"v_accvgpr_read_b32 v159, a31\n"
"v_pk_mul_f32 v[152:153], v[152:153], v[142:143]\n"
"v_pk_mul_f32 v[154:155], v[154:155], v[142:143]\n"
"v_pk_mul_f32 v[156:157], v[156:157], v[142:143]\n"
"v_pk_mul_f32 v[158:159], v[158:159], v[142:143]\n"
"v_cvt_pk_bf16_f32 v224, v152, v153\n"
"v_cvt_pk_bf16_f32 v225, v154, v155\n"
"v_cvt_pk_bf16_f32 v226, v156, v157\n"
"v_cvt_pk_bf16_f32 v227, v158, v159\n"
"s_mov_b32 s67, 393216\n"
"s_waitcnt vmcnt(62)\n"
"buffer_store_dwordx4 v[224:227], v175, s[52:55], s67 offen offset:0 sc1\n"
"v_accvgpr_read_b32 v144, a44\n"
"v_accvgpr_read_b32 v145, a45\n"
"v_accvgpr_read_b32 v146, a46\n"
"v_accvgpr_read_b32 v147, a47\n"
"v_accvgpr_read_b32 v148, a60\n"
"v_accvgpr_read_b32 v149, a61\n"
"v_accvgpr_read_b32 v150, a62\n"
"v_accvgpr_read_b32 v151, a63\n"
"v_pk_mul_f32 v[144:145], v[144:145], v[142:143]\n"
"v_pk_mul_f32 v[146:147], v[146:147], v[142:143]\n"
"v_pk_mul_f32 v[148:149], v[148:149], v[142:143]\n"
"v_pk_mul_f32 v[150:151], v[150:151], v[142:143]\n"
"v_cvt_pk_bf16_f32 v228, v144, v145\n"
"v_cvt_pk_bf16_f32 v229, v146, v147\n"
"v_cvt_pk_bf16_f32 v230, v148, v149\n"
"v_cvt_pk_bf16_f32 v231, v150, v151\n"
"s_mov_b32 s67, 393216\n"
"s_waitcnt vmcnt(62)\n"
"buffer_store_dwordx4 v[228:231], v175, s[52:55], s67 offen offset:1024 sc1\n"
"v_accvgpr_read_b32 v152, a76\n"
"v_accvgpr_read_b32 v153, a77\n"
"v_accvgpr_read_b32 v154, a78\n"
"v_accvgpr_read_b32 v155, a79\n"
"v_accvgpr_read_b32 v156, a92\n"
"v_accvgpr_read_b32 v157, a93\n"
"v_accvgpr_read_b32 v158, a94\n"
"v_accvgpr_read_b32 v159, a95\n"
"v_pk_mul_f32 v[152:153], v[152:153], v[142:143]\n"
"v_pk_mul_f32 v[154:155], v[154:155], v[142:143]\n"
"v_pk_mul_f32 v[156:157], v[156:157], v[142:143]\n"
"v_pk_mul_f32 v[158:159], v[158:159], v[142:143]\n"
"v_cvt_pk_bf16_f32 v232, v152, v153\n"
"v_cvt_pk_bf16_f32 v233, v154, v155\n"
"v_cvt_pk_bf16_f32 v234, v156, v157\n"
"v_cvt_pk_bf16_f32 v235, v158, v159\n"
"s_mov_b32 s67, 393216\n"
"s_waitcnt vmcnt(62)\n"
"buffer_store_dwordx4 v[232:235], v175, s[52:55], s67 offen offset:2048 sc1\n"
"v_accvgpr_read_b32 v144, a108\n"
"v_accvgpr_read_b32 v145, a109\n"
"v_accvgpr_read_b32 v146, a110\n"
"v_accvgpr_read_b32 v147, a111\n"
"v_accvgpr_read_b32 v148, a124\n"
"v_accvgpr_read_b32 v149, a125\n"
"v_accvgpr_read_b32 v150, a126\n"
"v_accvgpr_read_b32 v151, a127\n"
"v_pk_mul_f32 v[144:145], v[144:145], v[142:143]\n"
"v_pk_mul_f32 v[146:147], v[146:147], v[142:143]\n"
"v_pk_mul_f32 v[148:149], v[148:149], v[142:143]\n"
"v_pk_mul_f32 v[150:151], v[150:151], v[142:143]\n"
"v_cvt_pk_bf16_f32 v236, v144, v145\n"
"v_cvt_pk_bf16_f32 v237, v146, v147\n"
"v_cvt_pk_bf16_f32 v238, v148, v149\n"
"v_cvt_pk_bf16_f32 v239, v150, v151\n"
"s_mov_b32 s67, 393216\n"
"s_waitcnt vmcnt(62)\n"
"buffer_store_dwordx4 v[236:239], v175, s[52:55], s67 offen offset:3072 sc1\n"
"s_and_b32 s70, s33, 7\n"
"s_lshl_b32 s70, s70, 2\n"
"s_and_b32 s71, s5, 3\n"
"s_add_u32 s70, s70, s71\n"
"s_lshr_b32 s71, s33, 3\n"
"s_lshl_b32 s71, s71, 5\n"
"s_add_u32 s71, s71, s5\n"
"s_lshr_b32 s71, s71, 2\n"
"s_lshl_b32 s71, s71, 2\n"
"s_add_u32 s71, s71, s30\n"
"s_lshl_b32 s71, s71, 5\n"
"s_add_u32 s71, s71, s70\n"
"s_lshl_b32 s71, s71, 3\n"
"s_add_u32 s71, s71, 12288\n"
"v_mov_b32 v8, s71\n"
"v_mov_b32 v10, s34\n"
"v_mov_b32 v11, s35\n"
"s_lshr_b32 s64, s33, 1\n"
"s_and_b32 s65, s33, 1\n"
"s_lshl_b32 s65, s65, 3\n"
"s_lshr_b32 s69, s5, 2\n"
"s_add_u32 s65, s65, s69\n"
"s_and_b32 s68, s5, 3\n"
"s_and_b32 s62, s33, 1\n"
"s_lshl_b32 s62, s62, 2\n"
"s_and_b32 s69, s5, 3\n"
"s_add_u32 s62, s62, s69\n"
"s_lshr_b32 s63, s33, 1\n"
"s_mul_i32 s63, s63, 7\n"
"s_lshr_b32 s69, s5, 2\n"
"s_add_u32 s63, s63, s69\n"
"s_cmp_lt_u32 s69, 7\n"
"s_cselect_b32 s56, -1, 0\n"
"s_cmp_lt_u32 s63, 27\n"
"s_cselect_b32 s56, s56, 0\n"
"s_cmp_eq_u32 s56, 0\n"
"s_cbranch_scc1 L_nomlp1_%=\n"
"v_lshrrev_b32 v1, 4, v0\n"
"v_bfe_u32 v2, v0, 2, 2\n"
"v_and_b32 v3, 3, v0\n"
"v_lshl_add_u32 v4, v1, 3, v2\n"
"v_lshrrev_b32 v5, 1, v2\n"
"v_and_b32 v6, 1, v1\n"
"v_lshl_or_b32 v5, v6, 1, v5\n"
"v_lshlrev_b32 v4, 7, v4\n"
"v_lshl_add_u32 v4, v3, 3, v4\n"
"v_add_u32 v4, s61, v4\n"
"s_add_u32 s92, s30, 1\n"
"s_and_b32 s92, s92, 3\n"
"v_xor_b32 v6, s92, v5\n"
"v_lshl_add_u32 v160, v6, 5, v4\n"
"v_add_u32 v164, 0x10000, v160\n"
"v_add_u32 v168, 0x20000, v160\n"
"s_add_u32 s92, s30, 2\n"
"s_and_b32 s92, s92, 3\n"
"v_xor_b32 v6, s92, v5\n"
"v_lshl_add_u32 v161, v6, 5, v4\n"
"v_add_u32 v165, 0x10000, v161\n"
"v_add_u32 v169, 0x20000, v161\n"
"s_add_u32 s92, s30, 3\n"
"s_and_b32 s92, s92, 3\n"
"v_xor_b32 v6, s92, v5\n"
"v_lshl_add_u32 v162, v6, 5, v4\n"
"v_add_u32 v166, 0x10000, v162\n"
"v_add_u32 v170, 0x20000, v162\n"
"s_add_u32 s92, s30, 4\n"
"s_and_b32 s92, s92, 3\n"
"v_xor_b32 v6, s92, v5\n"
"v_lshl_add_u32 v163, v6, 5, v4\n"
"v_add_u32 v167, 0x10000, v163\n"
"v_add_u32 v171, 0x20000, v163\n"
"s_lshl_b32 s71, s63, 1\n"
"s_lshr_b32 s69, s30, 1\n"
"s_add_u32 s71, s71, s69\n"
"s_lshl_b32 s69, s63, 2\n"
"s_add_u32 s69, s69, s30\n"
"s_lshl_b32 s69, s69, 5\n"
"s_mul_i32 s70, s71, 5120\n"
"s_mul_i32 s56, s62, 276480\n"
"s_add_u32 s70, s70, s56\n"
"s_add_u32 s70, s70, 8388608\n"
"s_and_b32 s56, s30, 1\n"
"s_lshl_b32 s57, s56, 9\n"
"s_add_u32 s70, s70, s57\n"
"s_sub_u32 s57, 5120, s57\n"
"s_lshl_b32 s71, s71, 1\n"
"s_add_u32 s71, s71, s56\n"
"s_lshl_b32 s56, s62, 7\n"
"s_add_u32 s71, s71, s56\n"
"s_lshl_b32 s71, s71, 3\n"
"s_add_u32 s66, s71, 20480\n"
"s_add_u32 s36, s20, s69\n"
"s_addc_u32 s37, s21, 0\n"
"s_mov_b32 s38, 32\n"
"s_mov_b32 s39, 0x20000\n"
"s_add_u32 s40, s28, s70\n"
"s_addc_u32 s41, s29, 0\n"
"s_mov_b32 s42, s57\n"
"s_mov_b32 s43, 0x20000\n"
"v_lshrrev_b32 v1, 4, v0\n"
"v_lshlrev_b32 v1, 3, v1\n"
"s_waitcnt vmcnt(54)\n"
"ds_read_b64_tr_b16 v[96:97], v168 offset:0\n"
"ds_read_b64_tr_b16 v[98:99], v168 offset:512\n"
"ds_read_b64_tr_b16 v[100:101], v168 offset:4096\n"
"ds_read_b64_tr_b16 v[102:103], v168 offset:4608\n"
"s_waitcnt lgkmcnt(0)\n"
"v_mfma_f32_16x16x32_bf16 v[16:19], v[96:99], a[128:131], 0\n"
"ds_read_b64_tr_b16 v[104:105], v169 offset:0\n"
"buffer_load_dwordx4 a[0:3], v175, s[44:47], s58 offen offset:0 sc1\n"
"v_mfma_f32_16x16x32_bf16 v[16:19], v[100:103], a[148:151], v[16:19]\n"
"ds_read_b64_tr_b16 v[106:107], v169 offset:512\n"
"v_mfma_f32_16x16x32_bf16 v[20:23], v[96:99], a[132:135], 0\n"
"ds_read_b64_tr_b16 v[108:109], v169 offset:4096\n"
"v_mfma_f32_16x16x32_bf16 v[20:23], v[100:103], a[152:155], v[20:23]\n"
"ds_read_b64_tr_b16 v[110:111], v169 offset:4608\n"
"v_mfma_f32_16x16x32_bf16 v[24:27], v[96:99], a[136:139], 0\n"
"buffer_load_dwordx4 a[4:7], v175, s[44:47], s58 offen offset:1024 sc1\n"
"v_mfma_f32_16x16x32_bf16 v[24:27], v[100:103], a[156:159], v[24:27]\n"
"v_mfma_f32_16x16x32_bf16 v[28:31], v[96:99], a[140:143], 0\n"
"v_mfma_f32_16x16x32_bf16 v[28:31], v[100:103], a[160:163], v[28:31]\n"
"v_mfma_f32_16x16x32_bf16 v[32:35], v[96:99], a[144:147], 0\n"
"v_mfma_f32_16x16x32_bf16 v[32:35], v[100:103], a[164:167], v[32:35]\n"
"s_waitcnt lgkmcnt(0)\n"
"buffer_load_dwordx4 a[8:11], v175, s[44:47], s58 offen offset:2048 sc1\n"
"v_mfma_f32_16x16x32_bf16 v[36:39], v[104:107], a[128:131], 0\n"
"ds_read_b64_tr_b16 v[96:97], v170 offset:0\n"
"v_mfma_f32_16x16x32_bf16 v[36:39], v[108:111], a[148:151], v[36:39]\n"
"ds_read_b64_tr_b16 v[98:99], v170 offset:512\n"
"v_mfma_f32_16x16x32_bf16 v[40:43], v[104:107], a[132:135], 0\n"
"ds_read_b64_tr_b16 v[100:101], v170 offset:4096\n"
"v_mfma_f32_16x16x32_bf16 v[40:43], v[108:111], a[152:155], v[40:43]\n"
"ds_read_b64_tr_b16 v[102:103], v170 offset:4608\n"
"v_mfma_f32_16x16x32_bf16 v[44:47], v[104:107], a[136:139], 0\n"
"buffer_load_dwordx4 a[12:15], v175, s[44:47], s58 offen offset:3072 sc1\n"
"v_mfma_f32_16x16x32_bf16 v[44:47], v[108:111], a[156:159], v[44:47]\n"
"s_add_u32 s58, s58, 4096\n"
"v_mfma_f32_16x16x32_bf16 v[48:51], v[104:107], a[140:143], 0\n"
"v_mfma_f32_16x16x32_bf16 v[48:51], v[108:111], a[160:163], v[48:51]\n"
"v_mfma_f32_16x16x32_bf16 v[52:55], v[104:107], a[144:147], 0\n"
"v_mfma_f32_16x16x32_bf16 v[52:55], v[108:111], a[164:167], v[52:55]\n"
"s_waitcnt lgkmcnt(0)\n"
"buffer_load_dwordx4 a[16:19], v175, s[44:47], s58 offen offset:0 sc1\n"
"v_mfma_f32_16x16x32_bf16 v[56:59], v[96:99], a[128:131], 0\n"
"s_add_u32 s58, s58, 1024\n"
"ds_read_b64_tr_b16 v[104:105], v171 offset:0\n"
"v_mfma_f32_16x16x32_bf16 v[56:59], v[100:103], a[148:151], v[56:59]\n"
"ds_read_b64_tr_b16 v[106:107], v171 offset:512\n"
"v_mfma_f32_16x16x32_bf16 v[60:63], v[96:99], a[132:135], 0\n"
"ds_read_b64_tr_b16 v[108:109], v171 offset:4096\n"
"v_mfma_f32_16x16x32_bf16 v[60:63], v[100:103], a[152:155], v[60:63]\n"
"ds_read_b64_tr_b16 v[110:111], v171 offset:4608\n"
"buffer_load_dwordx4 a[20:23], v175, s[44:47], s58 offen offset:0 sc1\n"
"v_mfma_f32_16x16x32_bf16 v[64:67], v[96:99], a[136:139], 0\n"
"v_mfma_f32_16x16x32_bf16 v[64:67], v[100:103], a[156:159], v[64:67]\n"
"v_mfma_f32_16x16x32_bf16 v[68:71], v[96:99], a[140:143], 0\n"
"v_mfma_f32_16x16x32_bf16 v[68:71], v[100:103], a[160:163], v[68:71]\n"
"v_mfma_f32_16x16x32_bf16 v[72:75], v[96:99], a[144:147], 0\n"
"buffer_load_dwordx4 a[24:27], v175, s[44:47], s58 offen offset:1024 sc1\n"
"v_mfma_f32_16x16x32_bf16 v[72:75], v[100:103], a[164:167], v[72:75]\n"
"s_waitcnt lgkmcnt(0)\n"
"v_mfma_f32_16x16x32_bf16 v[76:79], v[104:107], a[128:131], 0\n"
"s_waitcnt vmcnt(42)\n"
"s_waitcnt vmcnt(7)\n"
"s_mov_b64 exec, 1\n"
"global_store_dwordx2 v8, v[10:11], s[6:7] sc1\n"
"s_mov_b64 exec, -1\n"
"ds_read_b64_tr_b16 v[96:97], v160 offset:0\n"
"v_mfma_f32_16x16x32_bf16 v[76:79], v[108:111], a[148:151], v[76:79]\n"
"ds_read_b64_tr_b16 v[98:99], v160 offset:512\n"
"v_mfma_f32_16x16x32_bf16 v[80:83], v[104:107], a[132:135], 0\n"
"ds_read_b64_tr_b16 v[100:101], v160 offset:4096\n"
"v_mfma_f32_16x16x32_bf16 v[80:83], v[108:111], a[152:155], v[80:83]\n"
"ds_read_b64_tr_b16 v[102:103], v160 offset:4608\n"
"buffer_load_dwordx4 a[28:31], v175, s[44:47], s58 offen offset:2048 sc1\n"
"v_mfma_f32_16x16x32_bf16 v[84:87], v[104:107], a[136:139], 0\n"
"v_mfma_f32_16x16x32_bf16 v[84:87], v[108:111], a[156:159], v[84:87]\n"
"v_mfma_f32_16x16x32_bf16 v[88:91], v[104:107], a[140:143], 0\n"
"v_mfma_f32_16x16x32_bf16 v[88:91], v[108:111], a[160:163], v[88:91]\n"
"v_mfma_f32_16x16x32_bf16 v[92:95], v[104:107], a[144:147], 0\n"
"v_mfma_f32_16x16x32_bf16 v[92:95], v[108:111], a[164:167], v[92:95]\n"
"s_waitcnt lgkmcnt(0)\n"
"v_mfma_f32_16x16x32_bf16 v[16:19], v[96:99], a[168:171], v[16:19]\n"
"ds_read_b64_tr_b16 v[104:105], v161 offset:0\n"
"buffer_load_dwordx4 a[32:35], v175, s[44:47], s58 offen offset:3072 sc1\n"
"v_mfma_f32_16x16x32_bf16 v[16:19], v[100:103], a[188:191], v[16:19]\n"
"s_add_u32 s58, s58, 4096\n"
"ds_read_b64_tr_b16 v[106:107], v161 offset:512\n"
"v_mfma_f32_16x16x32_bf16 v[20:23], v[96:99], a[172:175], v[20:23]\n"
"ds_read_b64_tr_b16 v[108:109], v161 offset:4096\n"
"buffer_load_dwordx4 a[36:39], v175, s[44:47], s58 offen offset:0 sc1\n"
"v_mfma_f32_16x16x32_bf16 v[20:23], v[100:103], a[208:211], v[20:23]\n"
"s_add_u32 s58, s58, 1024\n"
"s_cmp_eq_u32 s58, 81920\n"
"s_cselect_b32 s58, 0, s58\n"
"ds_read_b64_tr_b16 v[110:111], v161 offset:4608\n"
"v_mfma_f32_16x16x32_bf16 v[24:27], v[96:99], a[176:179], v[24:27]\n"
"s_add_u32 m0, s61, 131072\n"
"v_mfma_f32_16x16x32_bf16 v[24:27], v[100:103], a[212:215], v[24:27]\n"
"buffer_load_dwordx4 v240, s[48:51], s60 offen offset:0 lds\n"
"v_mfma_f32_16x16x32_bf16 v[28:31], v[96:99], a[180:183], v[28:31]\n"
"v_mfma_f32_16x16x32_bf16 v[28:31], v[100:103], a[216:219], v[28:31]\n"
"v_mfma_f32_16x16x32_bf16 v[32:35], v[96:99], a[184:187], v[32:35]\n"
"buffer_load_dwordx4 v241, s[48:51], s60 offen offset:1024 lds\n"
"v_mfma_f32_16x16x32_bf16 v[32:35], v[100:103], a[220:223], v[32:35]\n"
"s_waitcnt lgkmcnt(0)\n"
"v_mfma_f32_16x16x32_bf16 v[36:39], v[104:107], a[168:171], v[36:39]\n"
"ds_read_b64_tr_b16 v[96:97], v162 offset:0\n"
"v_mfma_f32_16x16x32_bf16 v[36:39], v[108:111], a[188:191], v[36:39]\n"
"ds_read_b64_tr_b16 v[98:99], v162 offset:512\n"
"buffer_load_dwordx4 v242, s[48:51], s60 offen offset:2048 lds\n"
"v_mfma_f32_16x16x32_bf16 v[40:43], v[104:107], a[172:175], v[40:43]\n"
"ds_read_b64_tr_b16 v[100:101], v162 offset:4096\n"
"v_mfma_f32_16x16x32_bf16 v[40:43], v[108:111], a[208:211], v[40:43]\n"
"ds_read_b64_tr_b16 v[102:103], v162 offset:4608\n"
"v_mfma_f32_16x16x32_bf16 v[44:47], v[104:107], a[176:179], v[44:47]\n"
"buffer_load_dwordx4 v243, s[48:51], s60 offen offset:3072 lds\n"
"v_mfma_f32_16x16x32_bf16 v[44:47], v[108:111], a[212:215], v[44:47]\n"
"s_add_u32 s60, s60, 110592\n"
"v_mfma_f32_16x16x32_bf16 v[48:51], v[104:107], a[180:183], v[48:51]\n"
"v_mfma_f32_16x16x32_bf16 v[48:51], v[108:111], a[216:219], v[48:51]\n"
"buffer_load_dwordx4 a[128:131], v175, s[44:47], s58 offen offset:0 sc1\n"
"v_mfma_f32_16x16x32_bf16 v[52:55], v[104:107], a[184:187], v[52:55]\n"
"v_mfma_f32_16x16x32_bf16 v[52:55], v[108:111], a[220:223], v[52:55]\n"
"s_waitcnt lgkmcnt(0)\n"
"v_mfma_f32_16x16x32_bf16 v[56:59], v[96:99], a[168:171], v[56:59]\n"
"ds_read_b64_tr_b16 v[104:105], v163 offset:0\n"
"buffer_load_dwordx4 a[132:135], v175, s[44:47], s58 offen offset:1024 sc1\n"
"v_mfma_f32_16x16x32_bf16 v[56:59], v[100:103], a[188:191], v[56:59]\n"
"ds_read_b64_tr_b16 v[106:107], v163 offset:512\n"
"v_mfma_f32_16x16x32_bf16 v[60:63], v[96:99], a[172:175], v[60:63]\n"
"ds_read_b64_tr_b16 v[108:109], v163 offset:4096\n"
"v_mfma_f32_16x16x32_bf16 v[60:63], v[100:103], a[208:211], v[60:63]\n"
"ds_read_b64_tr_b16 v[110:111], v163 offset:4608\n"
"buffer_load_dwordx4 a[136:139], v175, s[44:47], s58 offen offset:2048 sc1\n"
"v_mfma_f32_16x16x32_bf16 v[64:67], v[96:99], a[176:179], v[64:67]\n"
"v_mfma_f32_16x16x32_bf16 v[64:67], v[100:103], a[212:215], v[64:67]\n"
"v_mfma_f32_16x16x32_bf16 v[68:71], v[96:99], a[180:183], v[68:71]\n"
"buffer_load_dwordx4 a[140:143], v175, s[44:47], s58 offen offset:3072 sc1\n"
"v_mfma_f32_16x16x32_bf16 v[68:71], v[100:103], a[216:219], v[68:71]\n"
"s_add_u32 s58, s58, 4096\n"
"v_mfma_f32_16x16x32_bf16 v[72:75], v[96:99], a[184:187], v[72:75]\n"
"v_mfma_f32_16x16x32_bf16 v[72:75], v[100:103], a[220:223], v[72:75]\n"
"s_waitcnt lgkmcnt(0)\n"
"buffer_load_dwordx4 a[144:147], v175, s[44:47], s58 offen offset:0 sc1\n"
"v_mfma_f32_16x16x32_bf16 v[76:79], v[104:107], a[168:171], v[76:79]\n"
"s_waitcnt vmcnt(36)\n"
"ds_read_b64_tr_b16 v[96:97], v164 offset:0\n"
"v_mfma_f32_16x16x32_bf16 v[76:79], v[108:111], a[188:191], v[76:79]\n"
"s_add_u32 s58, s58, 1024\n"
"ds_read_b64_tr_b16 v[98:99], v164 offset:512\n"
"v_mfma_f32_16x16x32_bf16 v[80:83], v[104:107], a[172:175], v[80:83]\n"
"s_add_u32 m0, s61, 135168\n"
"ds_read_b64_tr_b16 v[100:101], v164 offset:4096\n"
"buffer_load_dwordx4 v240, s[48:51], s60 offen offset:0 lds\n"
"v_mfma_f32_16x16x32_bf16 v[80:83], v[108:111], a[208:211], v[80:83]\n"
"ds_read_b64_tr_b16 v[102:103], v164 offset:4608\n"
"v_mfma_f32_16x16x32_bf16 v[84:87], v[104:107], a[176:179], v[84:87]\n"
"v_mfma_f32_16x16x32_bf16 v[84:87], v[108:111], a[212:215], v[84:87]\n"
"buffer_load_dwordx2 v[12:13], v1, s[36:39], 0 offen\n"
"v_mfma_f32_16x16x32_bf16 v[88:91], v[104:107], a[180:183], v[88:91]\n"
"v_mfma_f32_16x16x32_bf16 v[88:91], v[108:111], a[216:219], v[88:91]\n"
"v_mfma_f32_16x16x32_bf16 v[92:95], v[104:107], a[184:187], v[92:95]\n"
"v_mfma_f32_16x16x32_bf16 v[92:95], v[108:111], a[220:223], v[92:95]\n"
"s_waitcnt lgkmcnt(0)\n"
"v_mfma_f32_16x16x32_bf16 v[16:19], v[96:99], a[224:227], v[16:19]\n"
"s_lshl_b32 s69, s65, 2\n"
"s_add_u32 s69, s69, s30\n"
"ds_read_b64_tr_b16 v[104:105], v165 offset:0\n"
"buffer_load_dwordx4 v241, s[48:51], s60 offen offset:1024 lds\n"
"v_mfma_f32_16x16x32_bf16 v[16:19], v[100:103], a[244:247], v[16:19]\n"
"s_lshl_b32 s69, s69, 18\n"
"s_lshl_b32 s70, s64, 15\n"
"ds_read_b64_tr_b16 v[106:107], v165 offset:512\n"
"v_mfma_f32_16x16x32_bf16 v[20:23], v[96:99], a[228:231], v[20:23]\n"
"s_add_u32 s69, s69, s70\n"
"s_add_u32 s69, s69, 16777216\n"
"ds_read_b64_tr_b16 v[108:109], v165 offset:4096\n"
"buffer_load_dwordx4 v242, s[48:51], s60 offen offset:2048 lds\n"
"v_mfma_f32_16x16x32_bf16 v[20:23], v[100:103], a[248:251], v[20:23]\n"
"s_mul_i32 s70, s64, 1310720\n"
"s_mul_i32 s71, s68, 320\n"
"ds_read_b64_tr_b16 v[110:111], v165 offset:4608\n"
"v_mfma_f32_16x16x32_bf16 v[24:27], v[96:99], a[232:235], v[24:27]\n"
"s_add_u32 s70, s70, s71\n"
"s_sub_u32 s71, 5242880, s70\n"
"v_mfma_f32_16x16x32_bf16 v[24:27], v[100:103], a[252:255], v[24:27]\n"
"s_add_u32 s52, s28, s69\n"
"s_addc_u32 s53, s29, 0\n"
"s_mov_b32 s54, 262144\n"
"buffer_load_dwordx4 v243, s[48:51], s60 offen offset:3072 lds\n"
"v_mfma_f32_16x16x32_bf16 v[28:31], v[96:99], a[236:239], v[28:31]\n"
"s_add_u32 s60, s60, 110592\n"
"s_cmp_eq_u32 s60, 1769472\n"
"s_cselect_b32 s60, 0, s60\n"
"v_mfma_f32_16x16x32_bf16 v[28:31], v[100:103], a[192:195], v[28:31]\n"
"s_mov_b32 s55, 0x20000\n"
"s_add_u32 s96, s16, s70\n"
"s_addc_u32 s97, s17, 0\n"
"v_mfma_f32_16x16x32_bf16 v[32:35], v[96:99], a[240:243], v[32:35]\n"
"s_mov_b32 s98, s71\n"
"s_mov_b32 s99, 0x20000\n"
"buffer_load_dwordx4 a[148:151], v175, s[44:47], s58 offen offset:0 sc1\n"
"v_mfma_f32_16x16x32_bf16 v[32:35], v[100:103], a[196:199], v[32:35]\n"
"s_waitcnt lgkmcnt(0)\n"
"v_mfma_f32_16x16x32_bf16 v[36:39], v[104:107], a[224:227], v[36:39]\n"
"s_mov_b32 s56, 0\n"
"s_mov_b32 s57, 131072\n"
"ds_read_b64_tr_b16 v[96:97], v166 offset:0\n"
"buffer_load_dwordx4 a[152:155], v175, s[44:47], s58 offen offset:1024 sc1\n"
"v_mfma_f32_16x16x32_bf16 v[36:39], v[108:111], a[244:247], v[36:39]\n"
"s_mov_b32 s62, 0\n"
"s_mul_i32 s63, s30, 5120\n"
"ds_read_b64_tr_b16 v[98:99], v166 offset:512\n"
"v_mfma_f32_16x16x32_bf16 v[40:43], v[104:107], a[228:231], v[40:43]\n"
"s_lshl_b32 s92, s30, 4\n"
"s_movk_i32 s93, 3277\n"
"ds_read_b64_tr_b16 v[100:101], v166 offset:4096\n"
"v_mfma_f32_16x16x32_bf16 v[40:43], v[108:111], a[248:251], v[40:43]\n"
"s_movk_i32 s94, 1280\n"
"ds_read_b64_tr_b16 v[102:103], v166 offset:4608\n"
"buffer_load_dwordx4 a[156:159], v175, s[44:47], s58 offen offset:2048 sc1\n"
"v_add_u32 v1, 0, v0\n"
"v_mfma_f32_16x16x32_bf16 v[44:47], v[104:107], a[232:235], v[44:47]\n"
"v_mul_u32_u24 v2, s93, v1\n"
"v_lshrrev_b32 v2, 16, v2\n"
"v_mfma_f32_16x16x32_bf16 v[44:47], v[108:111], a[252:255], v[44:47]\n"
"v_mul_u32_u24 v3, 20, v2\n"
"v_sub_u32 v3, v1, v3\n"
"v_mfma_f32_16x16x32_bf16 v[48:51], v[104:107], a[236:239], v[48:51]\n"
"buffer_load_dwordx4 a[160:163], v175, s[44:47], s58 offen offset:3072 sc1\n"
"v_bfe_u32 v4, v2, 2, 1\n"
"v_lshlrev_b32 v4, 1, v4\n"
"v_mfma_f32_16x16x32_bf16 v[48:51], v[108:111], a[192:195], v[48:51]\n"
"s_add_u32 s58, s58, 4096\n"
"v_xor_b32 v3, v3, v4\n"
"v_add_u32 v2, s92, v2\n"
"v_mfma_f32_16x16x32_bf16 v[52:55], v[104:107], a[240:243], v[52:55]\n"
"v_mul_u32_u24 v2, s94, v2\n"
"v_lshl_add_u32 v2, v3, 4, v2\n"
"v_mfma_f32_16x16x32_bf16 v[52:55], v[108:111], a[196:199], v[52:55]\n"
"s_waitcnt lgkmcnt(0)\n"
"buffer_load_dwordx4 a[164:167], v175, s[44:47], s58 offen offset:0 sc1\n"
"v_mov_b32 v114, v2\n"
"v_add_u32 v1, 64, v0\n"
"v_mfma_f32_16x16x32_bf16 v[56:59], v[96:99], a[224:227], v[56:59]\n"
"s_add_u32 s58, s58, 1024\n"
"s_cmp_eq_u32 s58, 81920\n"
"s_cselect_b32 s58, 0, s58\n"
"ds_read_b64_tr_b16 v[104:105], v167 offset:0\n"
"v_mul_u32_u24 v2, s93, v1\n"
"v_mfma_f32_16x16x32_bf16 v[56:59], v[100:103], a[244:247], v[56:59]\n"
"s_add_u32 m0, s61, 0\n"
"ds_read_b64_tr_b16 v[106:107], v167 offset:512\n"
"buffer_load_dwordx4 v240, s[48:51], s60 offen offset:0 lds\n"
"v_lshrrev_b32 v2, 16, v2\n"
"v_mfma_f32_16x16x32_bf16 v[60:63], v[96:99], a[228:231], v[60:63]\n"
"ds_read_b64_tr_b16 v[108:109], v167 offset:4096\n"
"v_mul_u32_u24 v3, 20, v2\n"
"v_mfma_f32_16x16x32_bf16 v[60:63], v[100:103], a[248:251], v[60:63]\n"
"ds_read_b64_tr_b16 v[110:111], v167 offset:4608\n"
"v_sub_u32 v3, v1, v3\n"
"v_mfma_f32_16x16x32_bf16 v[64:67], v[96:99], a[232:235], v[64:67]\n"
"buffer_load_dwordx4 v241, s[48:51], s60 offen offset:1024 lds\n"
"v_bfe_u32 v4, v2, 2, 1\n"
"v_lshlrev_b32 v4, 1, v4\n"
"v_mfma_f32_16x16x32_bf16 v[64:67], v[100:103], a[252:255], v[64:67]\n"
"v_xor_b32 v3, v3, v4\n"
"v_add_u32 v2, s92, v2\n"
"v_mfma_f32_16x16x32_bf16 v[68:71], v[96:99], a[236:239], v[68:71]\n"
"v_mul_u32_u24 v2, s94, v2\n"
"v_lshl_add_u32 v2, v3, 4, v2\n"
"v_mfma_f32_16x16x32_bf16 v[68:71], v[100:103], a[192:195], v[68:71]\n"
"buffer_load_dwordx4 v242, s[48:51], s60 offen offset:2048 lds\n"
"v_subrev_u32 v115, 1024, v2\n"
"v_add_u32 v1, 128, v0\n"
"v_mfma_f32_16x16x32_bf16 v[72:75], v[96:99], a[240:243], v[72:75]\n"
"v_mul_u32_u24 v2, s93, v1\n"
"v_lshrrev_b32 v2, 16, v2\n"
"v_mfma_f32_16x16x32_bf16 v[72:75], v[100:103], a[196:199], v[72:75]\n"
"s_waitcnt lgkmcnt(0)\n"
"buffer_load_dwordx4 v243, s[48:51], s60 offen offset:3072 lds\n"
"v_mul_u32_u24 v3, 20, v2\n"
"v_sub_u32 v3, v1, v3\n"
"v_mfma_f32_16x16x32_bf16 v[76:79], v[104:107], a[224:227], v[76:79]\n"
"s_waitcnt vmcnt(23)\n"
"ds_read_b64_tr_b16 v[96:97], v160 offset:32768\n"
"v_bfe_u32 v4, v2, 2, 1\n"
"v_mfma_f32_16x16x32_bf16 v[76:79], v[108:111], a[244:247], v[76:79]\n"
"s_add_u32 s60, s60, 110592\n"
"ds_read_b64_tr_b16 v[98:99], v160 offset:33280\n"
"v_lshlrev_b32 v4, 1, v4\n"
"v_mfma_f32_16x16x32_bf16 v[80:83], v[104:107], a[228:231], v[80:83]\n"
"ds_read_b64_tr_b16 v[100:101], v160 offset:36864\n"
"buffer_load_dwordx4 a[168:171], v175, s[44:47], s58 offen offset:0 sc1\n"
"v_xor_b32 v3, v3, v4\n"
"v_mfma_f32_16x16x32_bf16 v[80:83], v[108:111], a[248:251], v[80:83]\n"
"ds_read_b64_tr_b16 v[102:103], v160 offset:37376\n"
"v_add_u32 v2, s92, v2\n"
"v_mfma_f32_16x16x32_bf16 v[84:87], v[104:107], a[232:235], v[84:87]\n"
"v_mul_u32_u24 v2, s94, v2\n"
"v_lshl_add_u32 v2, v3, 4, v2\n"
"v_mfma_f32_16x16x32_bf16 v[84:87], v[108:111], a[252:255], v[84:87]\n"
"buffer_load_dwordx4 a[172:175], v175, s[44:47], s58 offen offset:1024 sc1\n"
"v_subrev_u32 v116, 2048, v2\n"
"v_add_u32 v1, 192, v0\n"
"v_mfma_f32_16x16x32_bf16 v[88:91], v[104:107], a[236:239], v[88:91]\n"
"v_mul_u32_u24 v2, s93, v1\n"
"v_lshrrev_b32 v2, 16, v2\n"
"v_mfma_f32_16x16x32_bf16 v[88:91], v[108:111], a[192:195], v[88:91]\n"
"v_mul_u32_u24 v3, 20, v2\n"
"v_sub_u32 v3, v1, v3\n"
"v_mfma_f32_16x16x32_bf16 v[92:95], v[104:107], a[240:243], v[92:95]\n"
"v_bfe_u32 v4, v2, 2, 1\n"
"v_lshlrev_b32 v4, 1, v4\n"
"v_mfma_f32_16x16x32_bf16 v[92:95], v[108:111], a[196:199], v[92:95]\n"
"s_waitcnt lgkmcnt(0)\n"
"v_xor_b32 v3, v3, v4\n"
"v_add_u32 v2, s92, v2\n"
"v_mfma_f32_16x16x32_bf16 v[16:19], v[96:99], a[0:3], v[16:19]\n"
"ds_read_b64_tr_b16 v[104:105], v161 offset:32768\n"
"buffer_load_dwordx4 a[176:179], v175, s[44:47], s58 offen offset:2048 sc1\n"
"v_mul_u32_u24 v2, s94, v2\n"
"v_mfma_f32_16x16x32_bf16 v[16:19], v[100:103], a[20:23], v[16:19]\n"
"ds_read_b64_tr_b16 v[106:107], v161 offset:33280\n"
"v_lshl_add_u32 v2, v3, 4, v2\n"
"v_mfma_f32_16x16x32_bf16 v[20:23], v[96:99], a[4:7], v[20:23]\n"
"ds_read_b64_tr_b16 v[108:109], v161 offset:36864\n"
"buffer_load_dwordx4 a[180:183], v175, s[44:47], s58 offen offset:3072 sc1\n"
"v_subrev_u32 v117, 3072, v2\n"
"v_mfma_f32_16x16x32_bf16 v[20:23], v[100:103], a[24:27], v[20:23]\n"
"s_add_u32 s58, s58, 4096\n"
"ds_read_b64_tr_b16 v[110:111], v161 offset:37376\n"
"v_add_u32 v1, 256, v0\n"
"v_mfma_f32_16x16x32_bf16 v[24:27], v[96:99], a[8:11], v[24:27]\n"
"buffer_load_dwordx4 a[184:187], v175, s[44:47], s58 offen offset:0 sc1\n"
"v_mul_u32_u24 v2, s93, v1\n"
"v_lshrrev_b32 v2, 16, v2\n"
"v_mfma_f32_16x16x32_bf16 v[24:27], v[100:103], a[28:31], v[24:27]\n"
"s_add_u32 s58, s58, 1024\n"
"v_mul_u32_u24 v3, 20, v2\n"
"v_sub_u32 v3, v1, v3\n"
"v_mfma_f32_16x16x32_bf16 v[28:31], v[96:99], a[12:15], v[28:31]\n"
"s_add_u32 m0, s61, 4096\n"
"v_bfe_u32 v4, v2, 2, 1\n"
"v_lshlrev_b32 v4, 1, v4\n"
"v_mfma_f32_16x16x32_bf16 v[28:31], v[100:103], a[32:35], v[28:31]\n"
"buffer_load_dwordx4 v240, s[48:51], s60 offen offset:0 lds\n"
"v_xor_b32 v3, v3, v4\n"
"v_add_u32 v2, s92, v2\n"
"v_mfma_f32_16x16x32_bf16 v[32:35], v[96:99], a[16:19], v[32:35]\n"
"v_mul_u32_u24 v2, s94, v2\n"
"v_lshl_add_u32 v2, v3, 4, v2\n"
"v_mfma_f32_16x16x32_bf16 v[32:35], v[100:103], a[36:39], v[32:35]\n"
"s_waitcnt lgkmcnt(0)\n"
"buffer_load_dwordx4 v241, s[48:51], s60 offen offset:1024 lds\n"
"v_mov_b32 v118, v2\n"
"v_lshrrev_b32 v1, 2, v0\n"
"v_mfma_f32_16x16x32_bf16 v[36:39], v[104:107], a[0:3], v[36:39]\n"
"ds_read_b64_tr_b16 v[96:97], v162 offset:32768\n"
"v_and_b32 v3, 3, v0\n"
"v_mfma_f32_16x16x32_bf16 v[36:39], v[108:111], a[20:23], v[36:39]\n"
"ds_read_b64_tr_b16 v[98:99], v162 offset:33280\n"
"buffer_load_dwordx4 v242, s[48:51], s60 offen offset:2048 lds\n"
"v_bfe_u32 v2, v0, 4, 1\n"
"v_mfma_f32_16x16x32_bf16 v[40:43], v[104:107], a[4:7], v[40:43]\n"
"ds_read_b64_tr_b16 v[100:101], v162 offset:36864\n"
"v_mul_u32_u24 v1, 320, v1\n"
"v_mfma_f32_16x16x32_bf16 v[40:43], v[108:111], a[24:27], v[40:43]\n"
"ds_read_b64_tr_b16 v[102:103], v162 offset:37376\n"
"v_lshl_add_u32 v1, v3, 3, v1\n"
"v_mfma_f32_16x16x32_bf16 v[44:47], v[104:107], a[8:11], v[44:47]\n"
"buffer_load_dwordx4 v243, s[48:51], s60 offen offset:3072 lds\n"
"v_xor_b32 v4, 0, v2\n"
"v_lshl_add_u32 v112, v4, 5, v1\n"
"v_mfma_f32_16x16x32_bf16 v[44:47], v[108:111], a[28:31], v[44:47]\n"
"s_add_u32 s60, s60, 110592\n"
"s_cmp_eq_u32 s60, 1769472\n"
"s_cselect_b32 s60, 0, s60\n"
"v_add_u32 v119, 53248, v112\n"
"v_add_u32 v122, 131072, v112\n"
"v_mfma_f32_16x16x32_bf16 v[48:51], v[104:107], a[12:15], v[48:51]\n"
"buffer_load_dwordx4 a[188:191], v175, s[44:47], s58 offen offset:0 sc1\n"
"v_xor_b32 v4, 1, v2\n"
"v_lshl_add_u32 v113, v4, 5, v1\n"
"v_mfma_f32_16x16x32_bf16 v[48:51], v[108:111], a[32:35], v[48:51]\n"
"v_add_u32 v120, 53248, v113\n"
"v_add_u32 v123, 131072, v113\n"
"v_mfma_f32_16x16x32_bf16 v[52:55], v[104:107], a[16:19], v[52:55]\n"
"s_lshl_b32 s70, s65, 1\n"
"s_lshr_b32 s71, s30, 1\n"
"v_mfma_f32_16x16x32_bf16 v[52:55], v[108:111], a[36:39], v[52:55]\n"
"s_waitcnt lgkmcnt(0)\n"
"buffer_load_dwordx4 a[208:211], v175, s[44:47], s58 offen offset:1024 sc1\n"
"v_mfma_f32_16x16x32_bf16 v[56:59], v[96:99], a[0:3], v[56:59]\n"
"s_add_u32 s70, s70, s71\n"
"s_lshl_b32 s70, s70, 2\n"
"ds_read_b64_tr_b16 v[104:105], v163 offset:32768\n"
"v_mfma_f32_16x16x32_bf16 v[56:59], v[100:103], a[20:23], v[56:59]\n"
"s_add_u32 s70, s70, s64\n"
"s_lshl_b32 s70, s70, 6\n"
"ds_read_b64_tr_b16 v[106:107], v163 offset:33280\n"
"buffer_load_dwordx4 a[212:215], v175, s[44:47], s58 offen offset:2048 sc1\n"
"v_mfma_f32_16x16x32_bf16 v[60:63], v[96:99], a[4:7], v[60:63]\n"
"s_add_u32 s70, s70, 12288\n"
"ds_read_b64_tr_b16 v[108:109], v163 offset:36864\n"
"v_and_b32 v4, 7, v0\n"
"v_mfma_f32_16x16x32_bf16 v[60:63], v[100:103], a[24:27], v[60:63]\n"
"ds_read_b64_tr_b16 v[110:111], v163 offset:37376\n"
"buffer_load_dwordx4 a[216:219], v175, s[44:47], s58 offen offset:3072 sc1\n"
"v_lshlrev_b32 v4, 3, v4\n"
"v_mfma_f32_16x16x32_bf16 v[64:67], v[96:99], a[8:11], v[64:67]\n"
"s_add_u32 s58, s58, 4096\n"
"v_add_u32 v4, s70, v4\n"
"v_mfma_f32_16x16x32_bf16 v[64:67], v[100:103], a[28:31], v[64:67]\n"
"s_add_u32 s95, s30, 1\n"
"s_and_b32 s95, s95, 3\n"
"v_mfma_f32_16x16x32_bf16 v[68:71], v[96:99], a[12:15], v[68:71]\n"
"s_cmp_ge_u32 s95, 2\n"
"s_addc_u32 s95, s95, 0\n"
"s_cmp_ge_u32 s95, 2\n"
"s_addc_u32 s95, s95, 0\n"
"buffer_load_dwordx4 a[220:223], v175, s[44:47], s58 offen offset:0 sc1\n"
"v_mfma_f32_16x16x32_bf16 v[68:71], v[100:103], a[32:35], v[68:71]\n"
"s_add_u32 s58, s58, 1024\n"
"s_cmp_eq_u32 s58, 81920\n"
"s_cselect_b32 s58, 0, s58\n"
"v_mfma_f32_16x16x32_bf16 v[72:75], v[96:99], a[16:19], v[72:75]\n"
"s_add_u32 m0, s61, 65536\n"
"v_mfma_f32_16x16x32_bf16 v[72:75], v[100:103], a[36:39], v[72:75]\n"
"s_waitcnt lgkmcnt(0)\n"
"buffer_load_dwordx4 v240, s[48:51], s60 offen offset:0 lds\n"
"v_mfma_f32_16x16x32_bf16 v[76:79], v[104:107], a[0:3], v[76:79]\n"
"s_waitcnt vmcnt(19)\n"
"ds_read_b64_tr_b16 v[96:97], v168 offset:0\n"
"buffer_load_dwordx4 v241, s[48:51], s60 offen offset:1024 lds\n"
"v_mfma_f32_16x16x32_bf16 v[76:79], v[108:111], a[20:23], v[76:79]\n"
"s_lshl_b32 s95, s95, 14\n"
"s_add_u32 s95, s95, 10240\n"
"ds_read_b64_tr_b16 v[98:99], v168 offset:512\n"
"v_add_u32 v152, s95, v175\n"
"v_mfma_f32_16x16x32_bf16 v[80:83], v[104:107], a[4:7], v[80:83]\n"
"s_add_u32 s95, s30, 2\n"
"s_and_b32 s95, s95, 3\n"
"ds_read_b64_tr_b16 v[100:101], v168 offset:4096\n"
"v_mfma_f32_16x16x32_bf16 v[80:83], v[108:111], a[24:27], v[80:83]\n"
"s_cmp_ge_u32 s95, 2\n"
"s_addc_u32 s95, s95, 0\n"
"s_cmp_ge_u32 s95, 2\n"
"s_addc_u32 s95, s95, 0\n"
"ds_read_b64_tr_b16 v[102:103], v168 offset:4608\n"
"buffer_load_dwordx4 v242, s[48:51], s60 offen offset:2048 lds\n"
"v_mfma_f32_16x16x32_bf16 v[84:87], v[104:107], a[8:11], v[84:87]\n"
"s_lshl_b32 s95, s95, 14\n"
"s_add_u32 s95, s95, 5120\n"
"v_add_u32 v153, s95, v175\n"
"v_mfma_f32_16x16x32_bf16 v[84:87], v[108:111], a[28:31], v[84:87]\n"
"s_add_u32 s95, s30, 3\n"
"s_and_b32 s95, s95, 3\n"
"buffer_load_dwordx4 v243, s[48:51], s60 offen offset:3072 lds\n"
"v_mfma_f32_16x16x32_bf16 v[88:91], v[104:107], a[12:15], v[88:91]\n"
"s_add_u32 s60, s60, 110592\n"
"v_mfma_f32_16x16x32_bf16 v[88:91], v[108:111], a[32:35], v[88:91]\n"
"s_cmp_ge_u32 s95, 2\n"
"s_addc_u32 s95, s95, 0\n"
"s_cmp_ge_u32 s95, 2\n"
"s_addc_u32 s95, s95, 0\n"
"v_mfma_f32_16x16x32_bf16 v[92:95], v[104:107], a[16:19], v[92:95]\n"
"s_lshl_b32 s95, s95, 14\n"
"s_add_u32 s95, s95, 0\n"
"v_add_u32 v154, s95, v175\n"
"v_mfma_f32_16x16x32_bf16 v[92:95], v[108:111], a[36:39], v[92:95]\n"
"s_waitcnt lgkmcnt(0)\n"
"v_mfma_f32_16x16x32_bf16 v[16:19], v[96:99], a[128:131], v[16:19]\n"
"ds_read_b64_tr_b16 v[104:105], v169 offset:0\n"
"buffer_load_dwordx4 a[224:227], v175, s[44:47], s58 offen offset:0 sc1\n"
"v_mfma_f32_16x16x32_bf16 v[16:19], v[100:103], a[148:151], v[16:19]\n"
"ds_read_b64_tr_b16 v[106:107], v169 offset:512\n"
"v_mfma_f32_16x16x32_bf16 v[20:23], v[96:99], a[132:135], v[20:23]\n"
"ds_read_b64_tr_b16 v[108:109], v169 offset:4096\n"
"buffer_load_dwordx4 a[228:231], v175, s[44:47], s58 offen offset:1024 sc1\n"
"v_mfma_f32_16x16x32_bf16 v[20:23], v[100:103], a[152:155], v[20:23]\n"
"ds_read_b64_tr_b16 v[110:111], v169 offset:4608\n"
"v_mfma_f32_16x16x32_bf16 v[24:27], v[96:99], a[136:139], v[24:27]\n"
"buffer_load_dwordx4 a[232:235], v175, s[44:47], s58 offen offset:2048 sc1\n"
"v_mfma_f32_16x16x32_bf16 v[24:27], v[100:103], a[156:159], v[24:27]\n"
"v_mfma_f32_16x16x32_bf16 v[28:31], v[96:99], a[140:143], v[28:31]\n"
"buffer_load_dwordx4 a[236:239], v175, s[44:47], s58 offen offset:3072 sc1\n"
"v_mfma_f32_16x16x32_bf16 v[28:31], v[100:103], a[160:163], v[28:31]\n"
"s_add_u32 s58, s58, 4096\n"
"v_mfma_f32_16x16x32_bf16 v[32:35], v[96:99], a[144:147], v[32:35]\n"
"buffer_load_dwordx4 a[240:243], v175, s[44:47], s58 offen offset:0 sc1\n"
"v_mfma_f32_16x16x32_bf16 v[32:35], v[100:103], a[164:167], v[32:35]\n"
"s_waitcnt lgkmcnt(0)\n"
"v_mfma_f32_16x16x32_bf16 v[36:39], v[104:107], a[128:131], v[36:39]\n"
"s_add_u32 s58, s58, 1024\n"
"ds_read_b64_tr_b16 v[96:97], v170 offset:0\n"
"v_mfma_f32_16x16x32_bf16 v[36:39], v[108:111], a[148:151], v[36:39]\n"
"s_add_u32 m0, s61, 69632\n"
"ds_read_b64_tr_b16 v[98:99], v170 offset:512\n"
"buffer_load_dwordx4 v240, s[48:51], s60 offen offset:0 lds\n"
"v_mfma_f32_16x16x32_bf16 v[40:43], v[104:107], a[132:135], v[40:43]\n"
"ds_read_b64_tr_b16 v[100:101], v170 offset:4096\n"
"buffer_load_dwordx4 v241, s[48:51], s60 offen offset:1024 lds\n"
"v_mfma_f32_16x16x32_bf16 v[40:43], v[108:111], a[152:155], v[40:43]\n"
"ds_read_b64_tr_b16 v[102:103], v170 offset:4608\n"
"v_mfma_f32_16x16x32_bf16 v[44:47], v[104:107], a[136:139], v[44:47]\n"
"buffer_load_dwordx4 v242, s[48:51], s60 offen offset:2048 lds\n"
"v_mfma_f32_16x16x32_bf16 v[44:47], v[108:111], a[156:159], v[44:47]\n"
"v_mfma_f32_16x16x32_bf16 v[48:51], v[104:107], a[140:143], v[48:51]\n"
"buffer_load_dwordx4 v243, s[48:51], s60 offen offset:3072 lds\n"
"v_mfma_f32_16x16x32_bf16 v[48:51], v[108:111], a[160:163], v[48:51]\n"
"s_add_u32 s60, s60, 110592\n"
"s_cmp_eq_u32 s60, 1769472\n"
"s_cselect_b32 s60, 0, s60\n"
"v_mfma_f32_16x16x32_bf16 v[52:55], v[104:107], a[144:147], v[52:55]\n"
"v_mfma_f32_16x16x32_bf16 v[52:55], v[108:111], a[164:167], v[52:55]\n"
"s_waitcnt lgkmcnt(0)\n"
"buffer_load_dwordx4 a[244:247], v175, s[44:47], s58 offen offset:0 sc1\n"
"v_mfma_f32_16x16x32_bf16 v[56:59], v[96:99], a[128:131], v[56:59]\n"
"ds_read_b64_tr_b16 v[104:105], v171 offset:0\n"
"v_mfma_f32_16x16x32_bf16 v[56:59], v[100:103], a[148:151], v[56:59]\n"
"ds_read_b64_tr_b16 v[106:107], v171 offset:512\n"
"buffer_load_dwordx4 a[248:251], v175, s[44:47], s58 offen offset:1024 sc1\n"
"v_mfma_f32_16x16x32_bf16 v[60:63], v[96:99], a[132:135], v[60:63]\n"
"ds_read_b64_tr_b16 v[108:109], v171 offset:4096\n"
"v_mfma_f32_16x16x32_bf16 v[60:63], v[100:103], a[152:155], v[60:63]\n"
"ds_read_b64_tr_b16 v[110:111], v171 offset:4608\n"
"buffer_load_dwordx4 a[252:255], v175, s[44:47], s58 offen offset:2048 sc1\n"
"v_mfma_f32_16x16x32_bf16 v[64:67], v[96:99], a[136:139], v[64:67]\n"
"v_mfma_f32_16x16x32_bf16 v[64:67], v[100:103], a[156:159], v[64:67]\n"
"buffer_load_dwordx4 a[192:195], v175, s[44:47], s58 offen offset:3072 sc1\n"
"v_mfma_f32_16x16x32_bf16 v[68:71], v[96:99], a[140:143], v[68:71]\n"
"s_add_u32 s58, s58, 4096\n"
"v_mfma_f32_16x16x32_bf16 v[68:71], v[100:103], a[160:163], v[68:71]\n"
"buffer_load_dwordx4 a[196:199], v175, s[44:47], s58 offen offset:0 sc1\n"
"v_mfma_f32_16x16x32_bf16 v[72:75], v[96:99], a[144:147], v[72:75]\n"
"s_add_u32 s58, s58, 1024\n"
"s_cmp_eq_u32 s58, 81920\n"
"s_cselect_b32 s58, 0, s58\n"
"v_mfma_f32_16x16x32_bf16 v[72:75], v[100:103], a[164:167], v[72:75]\n"
"s_waitcnt lgkmcnt(0)\n"
"v_mfma_f32_16x16x32_bf16 v[76:79], v[104:107], a[128:131], v[76:79]\n"
"s_waitcnt vmcnt(18)\n"
"ds_read_b64_tr_b16 v[96:97], v160 offset:0\n"
"v_mfma_f32_16x16x32_bf16 v[76:79], v[108:111], a[148:151], v[76:79]\n"
"s_add_u32 m0, s61, 32768\n"
"ds_read_b64_tr_b16 v[98:99], v160 offset:512\n"
"buffer_load_dwordx4 v240, s[48:51], s60 offen offset:0 lds\n"
"v_mfma_f32_16x16x32_bf16 v[80:83], v[104:107], a[132:135], v[80:83]\n"
"ds_read_b64_tr_b16 v[100:101], v160 offset:4096\n"
"buffer_load_dwordx4 v241, s[48:51], s60 offen offset:1024 lds\n"
"v_mfma_f32_16x16x32_bf16 v[80:83], v[108:111], a[152:155], v[80:83]\n"
"ds_read_b64_tr_b16 v[102:103], v160 offset:4608\n"
"buffer_load_dwordx4 v242, s[48:51], s60 offen offset:2048 lds\n"
"v_mfma_f32_16x16x32_bf16 v[84:87], v[104:107], a[136:139], v[84:87]\n"
"v_mfma_f32_16x16x32_bf16 v[84:87], v[108:111], a[156:159], v[84:87]\n"
"buffer_load_dwordx4 v243, s[48:51], s60 offen offset:3072 lds\n"
"v_mfma_f32_16x16x32_bf16 v[88:91], v[104:107], a[140:143], v[88:91]\n"
"s_add_u32 s60, s60, 110592\n"
"v_mfma_f32_16x16x32_bf16 v[88:91], v[108:111], a[160:163], v[88:91]\n"
"v_mfma_f32_16x16x32_bf16 v[92:95], v[104:107], a[144:147], v[92:95]\n"
"v_mfma_f32_16x16x32_bf16 v[92:95], v[108:111], a[164:167], v[92:95]\n"
"s_waitcnt lgkmcnt(0)\n"
"v_mfma_f32_16x16x32_bf16 v[16:19], v[96:99], a[168:171], v[16:19]\n"
"ds_read_b64_tr_b16 v[104:105], v161 offset:0\n"
"buffer_load_dwordx4 a[0:3], v175, s[44:47], s58 offen offset:0 sc1\n"
"v_mfma_f32_16x16x32_bf16 v[16:19], v[100:103], a[188:191], v[16:19]\n"
"ds_read_b64_tr_b16 v[106:107], v161 offset:512\n"
"buffer_load_dwordx4 a[4:7], v175, s[44:47], s58 offen offset:1024 sc1\n"
"v_mfma_f32_16x16x32_bf16 v[20:23], v[96:99], a[172:175], v[20:23]\n"
"ds_read_b64_tr_b16 v[108:109], v161 offset:4096\n"
"v_mfma_f32_16x16x32_bf16 v[20:23], v[100:103], a[208:211], v[20:23]\n"
"ds_read_b64_tr_b16 v[110:111], v161 offset:4608\n"
"buffer_load_dwordx4 a[8:11], v175, s[44:47], s58 offen offset:2048 sc1\n"
"v_mfma_f32_16x16x32_bf16 v[24:27], v[96:99], a[176:179], v[24:27]\n"
"buffer_load_dwordx4 a[12:15], v175, s[44:47], s58 offen offset:3072 sc1\n"
"v_mfma_f32_16x16x32_bf16 v[24:27], v[100:103], a[212:215], v[24:27]\n"
"s_add_u32 s58, s58, 4096\n"
"v_mfma_f32_16x16x32_bf16 v[28:31], v[96:99], a[180:183], v[28:31]\n"
"buffer_load_dwordx4 a[16:19], v175, s[44:47], s58 offen offset:0 sc1\n"
"v_mfma_f32_16x16x32_bf16 v[28:31], v[100:103], a[216:219], v[28:31]\n"
"s_add_u32 s58, s58, 1024\n"
"v_mfma_f32_16x16x32_bf16 v[32:35], v[96:99], a[184:187], v[32:35]\n"
"s_add_u32 m0, s61, 36864\n"
"v_mfma_f32_16x16x32_bf16 v[32:35], v[100:103], a[220:223], v[32:35]\n"
"s_waitcnt lgkmcnt(0)\n"
"buffer_load_dwordx4 v240, s[48:51], s60 offen offset:0 lds\n"
"v_mfma_f32_16x16x32_bf16 v[36:39], v[104:107], a[168:171], v[36:39]\n"
"ds_read_b64_tr_b16 v[96:97], v162 offset:0\n"
"buffer_load_dwordx4 v241, s[48:51], s60 offen offset:1024 lds\n"
"v_mfma_f32_16x16x32_bf16 v[36:39], v[108:111], a[188:191], v[36:39]\n"
"ds_read_b64_tr_b16 v[98:99], v162 offset:512\n"
"buffer_load_dwordx4 v242, s[48:51], s60 offen offset:2048 lds\n"
"v_mfma_f32_16x16x32_bf16 v[40:43], v[104:107], a[172:175], v[40:43]\n"
"ds_read_b64_tr_b16 v[100:101], v162 offset:4096\n"
"buffer_load_dwordx4 v243, s[48:51], s60 offen offset:3072 lds\n"
"v_mfma_f32_16x16x32_bf16 v[40:43], v[108:111], a[208:211], v[40:43]\n"
"s_add_u32 s60, s60, 110592\n"
"s_cmp_eq_u32 s60, 1769472\n"
"s_cselect_b32 s60, 0, s60\n"
"ds_read_b64_tr_b16 v[102:103], v162 offset:4608\n"
"v_mfma_f32_16x16x32_bf16 v[44:47], v[104:107], a[176:179], v[44:47]\n"
"buffer_load_dwordx4 a[20:23], v175, s[44:47], s58 offen offset:0 sc1\n"
"v_mfma_f32_16x16x32_bf16 v[44:47], v[108:111], a[212:215], v[44:47]\n"
"buffer_load_dwordx4 a[24:27], v175, s[44:47], s58 offen offset:1024 sc1\n"
"v_mfma_f32_16x16x32_bf16 v[48:51], v[104:107], a[180:183], v[48:51]\n"
"v_mfma_f32_16x16x32_bf16 v[48:51], v[108:111], a[216:219], v[48:51]\n"
"buffer_load_dwordx4 a[28:31], v175, s[44:47], s58 offen offset:2048 sc1\n"
"v_mfma_f32_16x16x32_bf16 v[52:55], v[104:107], a[184:187], v[52:55]\n"
"buffer_load_dwordx4 a[32:35], v175, s[44:47], s58 offen offset:3072 sc1\n"
"v_mfma_f32_16x16x32_bf16 v[52:55], v[108:111], a[220:223], v[52:55]\n"
"s_waitcnt lgkmcnt(0)\n"
"v_mfma_f32_16x16x32_bf16 v[56:59], v[96:99], a[168:171], v[56:59]\n"
"s_add_u32 s58, s58, 4096\n"
"ds_read_b64_tr_b16 v[104:105], v163 offset:0\n"
"buffer_load_dwordx4 a[36:39], v175, s[44:47], s58 offen offset:0 sc1\n"
"v_mfma_f32_16x16x32_bf16 v[56:59], v[100:103], a[188:191], v[56:59]\n"
"s_add_u32 s58, s58, 1024\n"
"s_cmp_eq_u32 s58, 81920\n"
"s_cselect_b32 s58, 0, s58\n"
"ds_read_b64_tr_b16 v[106:107], v163 offset:512\n"
"v_mfma_f32_16x16x32_bf16 v[60:63], v[96:99], a[172:175], v[60:63]\n"
"ds_read_b64_tr_b16 v[108:109], v163 offset:4096\n"
"v_mfma_f32_16x16x32_bf16 v[60:63], v[100:103], a[208:211], v[60:63]\n"
"ds_read_b64_tr_b16 v[110:111], v163 offset:4608\n"
"v_mfma_f32_16x16x32_bf16 v[64:67], v[96:99], a[176:179], v[64:67]\n"
"v_mfma_f32_16x16x32_bf16 v[64:67], v[100:103], a[212:215], v[64:67]\n"
"v_mfma_f32_16x16x32_bf16 v[68:71], v[96:99], a[180:183], v[68:71]\n"
"v_mfma_f32_16x16x32_bf16 v[68:71], v[100:103], a[216:219], v[68:71]\n"
"v_mfma_f32_16x16x32_bf16 v[72:75], v[96:99], a[184:187], v[72:75]\n"
"v_mfma_f32_16x16x32_bf16 v[72:75], v[100:103], a[220:223], v[72:75]\n"
"s_waitcnt lgkmcnt(0)\n"
"v_mfma_f32_16x16x32_bf16 v[76:79], v[104:107], a[168:171], v[76:79]\n"
"s_waitcnt vmcnt(18)\n"
"ds_read_b64_tr_b16 v[96:97], v164 offset:0\n"
"v_mfma_f32_16x16x32_bf16 v[76:79], v[108:111], a[188:191], v[76:79]\n"
"ds_read_b64_tr_b16 v[98:99], v164 offset:512\n"
"v_mfma_f32_16x16x32_bf16 v[80:83], v[104:107], a[172:175], v[80:83]\n"
"ds_read_b64_tr_b16 v[100:101], v164 offset:4096\n"
"v_mfma_f32_16x16x32_bf16 v[80:83], v[108:111], a[208:211], v[80:83]\n"
"ds_read_b64_tr_b16 v[102:103], v164 offset:4608\n"
"v_mfma_f32_16x16x32_bf16 v[84:87], v[104:107], a[176:179], v[84:87]\n"
"v_mfma_f32_16x16x32_bf16 v[84:87], v[108:111], a[212:215], v[84:87]\n"
"v_mfma_f32_16x16x32_bf16 v[88:91], v[104:107], a[180:183], v[88:91]\n"
"v_mfma_f32_16x16x32_bf16 v[88:91], v[108:111], a[216:219], v[88:91]\n"
"v_mfma_f32_16x16x32_bf16 v[92:95], v[104:107], a[184:187], v[92:95]\n"
"v_mfma_f32_16x16x32_bf16 v[92:95], v[108:111], a[220:223], v[92:95]\n"
"s_waitcnt lgkmcnt(0)\n"
"v_mfma_f32_16x16x32_bf16 v[16:19], v[96:99], a[224:227], v[16:19]\n"
"ds_read_b64_tr_b16 v[104:105], v165 offset:0\n"
"global_load_dwordx2 v[2:3], v4, s[6:7] sc1\n"
"v_mfma_f32_16x16x32_bf16 v[16:19], v[100:103], a[244:247], v[16:19]\n"
"s_add_u32 m0, s63, 102400\n"
"ds_read_b64_tr_b16 v[106:107], v165 offset:512\n"
"v_mfma_f32_16x16x32_bf16 v[20:23], v[96:99], a[228:231], v[20:23]\n"
"ds_read_b64_tr_b16 v[108:109], v165 offset:4096\n"
"v_mfma_f32_16x16x32_bf16 v[20:23], v[100:103], a[248:251], v[20:23]\n"
"ds_read_b64_tr_b16 v[110:111], v165 offset:4608\n"
"buffer_load_dwordx4 v114, s[96:99], s62 offen offset:0 lds\n"
"v_mfma_f32_16x16x32_bf16 v[24:27], v[96:99], a[232:235], v[24:27]\n"
"v_mfma_f32_16x16x32_bf16 v[24:27], v[100:103], a[252:255], v[24:27]\n"
"v_mfma_f32_16x16x32_bf16 v[28:31], v[96:99], a[236:239], v[28:31]\n"
"v_mfma_f32_16x16x32_bf16 v[28:31], v[100:103], a[192:195], v[28:31]\n"
"buffer_load_dwordx4 v115, s[96:99], s62 offen offset:1024 lds\n"
"v_mfma_f32_16x16x32_bf16 v[32:35], v[96:99], a[240:243], v[32:35]\n"
"v_mfma_f32_16x16x32_bf16 v[32:35], v[100:103], a[196:199], v[32:35]\n"
"s_waitcnt lgkmcnt(0)\n"
"v_mfma_f32_16x16x32_bf16 v[36:39], v[104:107], a[224:227], v[36:39]\n"
"ds_read_b64_tr_b16 v[96:97], v166 offset:0\n"
"v_mfma_f32_16x16x32_bf16 v[36:39], v[108:111], a[244:247], v[36:39]\n"
"ds_read_b64_tr_b16 v[98:99], v166 offset:512\n"
"buffer_load_dwordx4 v116, s[96:99], s62 offen offset:2048 lds\n"
"v_mfma_f32_16x16x32_bf16 v[40:43], v[104:107], a[228:231], v[40:43]\n"
"ds_read_b64_tr_b16 v[100:101], v166 offset:4096\n"
"v_mfma_f32_16x16x32_bf16 v[40:43], v[108:111], a[248:251], v[40:43]\n"
"ds_read_b64_tr_b16 v[102:103], v166 offset:4608\n"
"v_mfma_f32_16x16x32_bf16 v[44:47], v[104:107], a[232:235], v[44:47]\n"
"buffer_load_dwordx4 v117, s[96:99], s62 offen offset:3072 lds\n"
"v_mfma_f32_16x16x32_bf16 v[44:47], v[108:111], a[252:255], v[44:47]\n"
"s_add_u32 m0, s63, 106496\n"
"v_mfma_f32_16x16x32_bf16 v[48:51], v[104:107], a[236:239], v[48:51]\n"
"v_mfma_f32_16x16x32_bf16 v[48:51], v[108:111], a[192:195], v[48:51]\n"
"v_mfma_f32_16x16x32_bf16 v[52:55], v[104:107], a[240:243], v[52:55]\n"
"buffer_load_dwordx4 v118, s[96:99], s62 offen offset:0 lds\n"
"v_mfma_f32_16x16x32_bf16 v[52:55], v[108:111], a[196:199], v[52:55]\n"
"s_waitcnt lgkmcnt(0)\n"
"v_mfma_f32_16x16x32_bf16 v[56:59], v[96:99], a[224:227], v[56:59]\n"
"s_add_u32 s62, s62, 81920\n"
"ds_read_b64_tr_b16 v[104:105], v167 offset:0\n"
"v_mfma_f32_16x16x32_bf16 v[56:59], v[100:103], a[244:247], v[56:59]\n"
"ds_read_b64_tr_b16 v[106:107], v167 offset:512\n"
"v_mfma_f32_16x16x32_bf16 v[60:63], v[96:99], a[228:231], v[60:63]\n"
"ds_read_b64_tr_b16 v[108:109], v167 offset:4096\n"
"v_mfma_f32_16x16x32_bf16 v[60:63], v[100:103], a[248:251], v[60:63]\n"
"ds_read_b64_tr_b16 v[110:111], v167 offset:4608\n"
"v_mfma_f32_16x16x32_bf16 v[64:67], v[96:99], a[232:235], v[64:67]\n"
"v_mfma_f32_16x16x32_bf16 v[64:67], v[100:103], a[252:255], v[64:67]\n"
"v_mfma_f32_16x16x32_bf16 v[68:71], v[96:99], a[236:239], v[68:71]\n"
"v_mfma_f32_16x16x32_bf16 v[68:71], v[100:103], a[192:195], v[68:71]\n"
"v_mfma_f32_16x16x32_bf16 v[72:75], v[96:99], a[240:243], v[72:75]\n"
"v_mfma_f32_16x16x32_bf16 v[72:75], v[100:103], a[196:199], v[72:75]\n"
"s_waitcnt lgkmcnt(0)\n"
"v_mfma_f32_16x16x32_bf16 v[76:79], v[104:107], a[224:227], v[76:79]\n"
"s_waitcnt vmcnt(6)\n"
"ds_read_b64_tr_b16 v[96:97], v160 offset:32768\n"
"v_mfma_f32_16x16x32_bf16 v[76:79], v[108:111], a[244:247], v[76:79]\n"
"ds_read_b64_tr_b16 v[98:99], v160 offset:33280\n"
"v_mfma_f32_16x16x32_bf16 v[80:83], v[104:107], a[228:231], v[80:83]\n"
"ds_read_b64_tr_b16 v[100:101], v160 offset:36864\n"
"v_mfma_f32_16x16x32_bf16 v[80:83], v[108:111], a[248:251], v[80:83]\n"
"ds_read_b64_tr_b16 v[102:103], v160 offset:37376\n"
"v_mfma_f32_16x16x32_bf16 v[84:87], v[104:107], a[232:235], v[84:87]\n"
"v_mfma_f32_16x16x32_bf16 v[84:87], v[108:111], a[252:255], v[84:87]\n"
"v_mfma_f32_16x16x32_bf16 v[88:91], v[104:107], a[236:239], v[88:91]\n"
"v_mfma_f32_16x16x32_bf16 v[88:91], v[108:111], a[192:195], v[88:91]\n"
"v_mfma_f32_16x16x32_bf16 v[92:95], v[104:107], a[240:243], v[92:95]\n"
"v_mfma_f32_16x16x32_bf16 v[92:95], v[108:111], a[196:199], v[92:95]\n"
"s_waitcnt lgkmcnt(0)\n"
"s_barrier\n"
"s_and_b32 s95, s61, 16384\n"
"s_add_u32 s95, s95, s61\n"
"s_lshl_b32 s95, s95, 1\n"
"v_add_u32 v7, s95, v175\n"
"v_mfma_f32_16x16x32_bf16 v[16:19], v[96:99], a[0:3], v[16:19]\n"
"ds_read_b64_tr_b16 v[104:105], v161 offset:32768\n"
"s_waitcnt vmcnt(5)\n"
"v_cmp_ne_u64 vcc, s[34:35], v[2:3]\n"
"s_nop 0\n"
"s_cmp_eq_u64 vcc, 0\n"
"s_cbranch_scc0 L_slow_pollO_%=\n"
"L_back_pollO_%=:\n"
"v_mfma_f32_16x16x32_bf16 v[16:19], v[100:103], a[20:23], v[16:19]\n"
"ds_read_b64_tr_b16 v[106:107], v161 offset:33280\n"
"buffer_load_dwordx4 a[88:91], v175, s[52:55], s56 offen offset:0 sc1\n"
"v_mfma_f32_16x16x32_bf16 v[20:23], v[96:99], a[4:7], v[20:23]\n"
"ds_read_b64_tr_b16 v[108:109], v161 offset:36864\n"
"buffer_load_dwordx4 a[92:95], v175, s[52:55], s56 offen offset:1024 sc1\n"
"v_mfma_f32_16x16x32_bf16 v[20:23], v[100:103], a[24:27], v[20:23]\n"
"s_add_u32 s56, s56, 2048\n"
"ds_read_b64_tr_b16 v[110:111], v161 offset:37376\n"
"buffer_load_dwordx4 a[96:99], v175, s[52:55], s57 offen offset:0 sc1\n"
"v_mfma_f32_16x16x32_bf16 v[24:27], v[96:99], a[8:11], v[24:27]\n"
"buffer_load_dwordx4 a[100:103], v175, s[52:55], s57 offen offset:1024 sc1\n"
"v_mfma_f32_16x16x32_bf16 v[24:27], v[100:103], a[28:31], v[24:27]\n"
"s_add_u32 s57, s57, 2048\n"
"buffer_load_dwordx4 a[104:107], v175, s[52:55], s56 offen offset:0 sc1\n"
"v_mfma_f32_16x16x32_bf16 v[28:31], v[96:99], a[12:15], v[28:31]\n"
"buffer_load_dwordx4 a[108:111], v175, s[52:55], s56 offen offset:1024 sc1\n"
"v_mfma_f32_16x16x32_bf16 v[28:31], v[100:103], a[32:35], v[28:31]\n"
"s_add_u32 s56, s56, 2048\n"
"buffer_load_dwordx4 a[112:115], v175, s[52:55], s57 offen offset:0 sc1\n"
"v_mfma_f32_16x16x32_bf16 v[32:35], v[96:99], a[16:19], v[32:35]\n"
"buffer_load_dwordx4 a[116:119], v175, s[52:55], s57 offen offset:1024 sc1\n"
"v_mfma_f32_16x16x32_bf16 v[32:35], v[100:103], a[36:39], v[32:35]\n"
"s_waitcnt lgkmcnt(0)\n"
"v_mfma_f32_16x16x32_bf16 v[36:39], v[104:107], a[0:3], v[36:39]\n"
"s_add_u32 s57, s57, 2048\n"
"ds_read_b64_tr_b16 v[96:97], v162 offset:32768\n"
"v_mfma_f32_16x16x32_bf16 v[36:39], v[108:111], a[20:23], v[36:39]\n"
"s_add_u32 m0, s63, 139264\n"
"ds_read_b64_tr_b16 v[98:99], v162 offset:33280\n"
"buffer_load_dwordx4 v114, s[96:99], s62 offen offset:0 lds\n"
"v_mfma_f32_16x16x32_bf16 v[40:43], v[104:107], a[4:7], v[40:43]\n"
"ds_read_b64_tr_b16 v[100:101], v162 offset:36864\n"
"buffer_load_dwordx4 v115, s[96:99], s62 offen offset:1024 lds\n"
"v_mfma_f32_16x16x32_bf16 v[40:43], v[108:111], a[24:27], v[40:43]\n"
"ds_read_b64_tr_b16 v[102:103], v162 offset:37376\n"
"buffer_load_dwordx4 v116, s[96:99], s62 offen offset:2048 lds\n"
"v_mfma_f32_16x16x32_bf16 v[44:47], v[104:107], a[8:11], v[44:47]\n"
"ds_write_b128 v7, v[16:19] offset:0\n"
"buffer_load_dwordx4 v117, s[96:99], s62 offen offset:3072 lds\n"
"v_mfma_f32_16x16x32_bf16 v[44:47], v[108:111], a[28:31], v[44:47]\n"
"s_add_u32 m0, s63, 143360\n"
"ds_write_b128 v7, v[20:23] offset:1024\n"
"v_mfma_f32_16x16x32_bf16 v[48:51], v[104:107], a[12:15], v[48:51]\n"
"ds_write_b128 v7, v[24:27] offset:2048\n"
"buffer_load_dwordx4 v118, s[96:99], s62 offen offset:0 lds\n"
"v_mfma_f32_16x16x32_bf16 v[48:51], v[108:111], a[32:35], v[48:51]\n"
"s_add_u32 s62, s62, 81920\n"
"buffer_load_dwordx4 a[120:123], v175, s[52:55], s56 offen offset:0 sc1\n"
"v_mfma_f32_16x16x32_bf16 v[52:55], v[104:107], a[16:19], v[52:55]\n"
"ds_write_b128 v7, v[28:31] offset:3072\n"
"buffer_load_dwordx4 a[124:127], v175, s[52:55], s56 offen offset:1024 sc1\n"
"v_mfma_f32_16x16x32_bf16 v[52:55], v[108:111], a[36:39], v[52:55]\n"
"s_waitcnt lgkmcnt(4)\n"
"ds_write_b128 v7, v[32:35] offset:4096\n"
"v_mfma_f32_16x16x32_bf16 v[56:59], v[96:99], a[0:3], v[56:59]\n"
"s_add_u32 s56, s56, 2048\n"
"ds_read_b64_tr_b16 v[104:105], v163 offset:32768\n"
"buffer_load_dwordx4 a[128:131], v175, s[52:55], s57 offen offset:0 sc1\n"
"v_mfma_f32_16x16x32_bf16 v[56:59], v[100:103], a[20:23], v[56:59]\n"
"ds_read_b64_tr_b16 v[106:107], v163 offset:33280\n"
"buffer_load_dwordx4 a[132:135], v175, s[52:55], s57 offen offset:1024 sc1\n"
"v_mfma_f32_16x16x32_bf16 v[60:63], v[96:99], a[4:7], v[60:63]\n"
"s_add_u32 s57, s57, 2048\n"
"ds_read_b64_tr_b16 v[108:109], v163 offset:36864\n"
"buffer_load_dwordx4 a[136:139], v175, s[52:55], s56 offen offset:0 sc1\n"
"v_mfma_f32_16x16x32_bf16 v[60:63], v[100:103], a[24:27], v[60:63]\n"
"ds_read_b64_tr_b16 v[110:111], v163 offset:37376\n"
"buffer_load_dwordx4 a[140:143], v175, s[52:55], s56 offen offset:1024 sc1\n"
"v_mfma_f32_16x16x32_bf16 v[64:67], v[96:99], a[8:11], v[64:67]\n"
"s_add_u32 s56, s56, 2048\n"
"ds_write_b128 v7, v[36:39] offset:5120\n"
"buffer_load_dwordx4 a[144:147], v175, s[52:55], s57 offen offset:0 sc1\n"
"v_mfma_f32_16x16x32_bf16 v[64:67], v[100:103], a[28:31], v[64:67]\n"
"ds_write_b128 v7, v[40:43] offset:6144\n"
"buffer_load_dwordx4 a[148:151], v175, s[52:55], s57 offen offset:1024 sc1\n"
"v_mfma_f32_16x16x32_bf16 v[68:71], v[96:99], a[12:15], v[68:71]\n"
"s_add_u32 s57, s57, 2048\n"
"ds_write_b128 v7, v[44:47] offset:7168\n"
"buffer_load_dwordx4 a[152:155], v175, s[52:55], s56 offen offset:0 sc1\n"
"v_mfma_f32_16x16x32_bf16 v[68:71], v[100:103], a[32:35], v[68:71]\n"
"buffer_load_dwordx4 a[156:159], v175, s[52:55], s56 offen offset:1024 sc1\n"
"v_mfma_f32_16x16x32_bf16 v[72:75], v[96:99], a[16:19], v[72:75]\n"
"s_add_u32 s56, s56, 2048\n"
"ds_write_b128 v7, v[48:51] offset:8192\n"
"buffer_load_dwordx4 a[160:163], v175, s[52:55], s57 offen offset:0 sc1\n"
"v_mfma_f32_16x16x32_bf16 v[72:75], v[100:103], a[36:39], v[72:75]\n"
"s_waitcnt lgkmcnt(4)\n"
"ds_write_b128 v7, v[52:55] offset:9216\n"
"buffer_load_dwordx4 a[164:167], v175, s[52:55], s57 offen offset:1024 sc1\n"
"v_mfma_f32_16x16x32_bf16 v[76:79], v[104:107], a[0:3], v[76:79]\n"
"s_add_u32 s57, s57, 2048\n"
"v_mfma_f32_16x16x32_bf16 v[76:79], v[108:111], a[20:23], v[76:79]\n"
"ds_write_b128 v7, v[56:59] offset:10240\n"
"v_mfma_f32_16x16x32_bf16 v[80:83], v[104:107], a[4:7], v[80:83]\n"
"v_mfma_f32_16x16x32_bf16 v[80:83], v[108:111], a[24:27], v[80:83]\n"
"ds_write_b128 v7, v[60:63] offset:11264\n"
"v_mfma_f32_16x16x32_bf16 v[84:87], v[104:107], a[8:11], v[84:87]\n"
"ds_write_b128 v7, v[64:67] offset:12288\n"
"v_mfma_f32_16x16x32_bf16 v[84:87], v[108:111], a[28:31], v[84:87]\n"
"v_mfma_f32_16x16x32_bf16 v[88:91], v[104:107], a[12:15], v[88:91]\n"
"ds_write_b128 v7, v[68:71] offset:13312\n"
"v_mfma_f32_16x16x32_bf16 v[88:91], v[108:111], a[32:35], v[88:91]\n"
"ds_write_b128 v7, v[72:75] offset:14336\n"
"v_mfma_f32_16x16x32_bf16 v[92:95], v[104:107], a[16:19], v[92:95]\n"
"v_mfma_f32_16x16x32_bf16 v[92:95], v[108:111], a[36:39], v[92:95]\n"
"s_waitcnt lgkmcnt(0)\n"
"s_barrier\n"
"ds_read_b128 v[16:19], v152 offset:0\n"
"ds_read_b128 v[20:23], v152 offset:1024\n"
"ds_read_b128 v[24:27], v152 offset:2048\n"
"ds_read_b128 v[28:31], v152 offset:3072\n"
"ds_read_b128 v[32:35], v152 offset:4096\n"
"ds_read_b128 v[36:39], v153 offset:0\n"
"ds_read_b128 v[40:43], v153 offset:1024\n"
"ds_read_b128 v[44:47], v153 offset:2048\n"
"ds_read_b128 v[48:51], v153 offset:3072\n"
"ds_read_b128 v[52:55], v153 offset:4096\n"
"ds_read_b128 v[56:59], v154 offset:0\n"
"ds_read_b128 v[60:63], v154 offset:1024\n"
"ds_read_b128 v[64:67], v154 offset:2048\n"
"ds_read_b128 v[68:71], v154 offset:3072\n"
"ds_read_b128 v[72:75], v154 offset:4096\n"
"buffer_load_dwordx4 a[40:43], v175, s[52:55], s56 offen offset:0 sc1\n"
"buffer_load_dwordx4 a[44:47], v175, s[52:55], s56 offen offset:1024 sc1\n"
"s_add_u32 s56, s56, 2048\n"
"buffer_load_dwordx4 a[48:51], v175, s[52:55], s57 offen offset:0 sc1\n"
"buffer_load_dwordx4 a[52:55], v175, s[52:55], s57 offen offset:1024 sc1\n"
"s_add_u32 s57, s57, 2048\n"
"buffer_load_dwordx4 a[56:59], v175, s[52:55], s56 offen offset:0 sc1\n"
"buffer_load_dwordx4 a[60:63], v175, s[52:55], s56 offen offset:1024 sc1\n"
"s_add_u32 s56, s56, 2048\n"
"buffer_load_dwordx4 a[64:67], v175, s[52:55], s57 offen offset:0 sc1\n"
"buffer_load_dwordx4 a[68:71], v175, s[52:55], s57 offen offset:1024 sc1\n"
"s_add_u32 s57, s57, 2048\n"
"s_or_b32 s69, s31, s30\n"
"s_cmp_lg_u32 s69, 0\n"
"s_cbranch_scc1 L_noE_%=\n"
"v_mov_b32 v8, 0\n"
"v_mov_b32 v10, s34\n"
"v_mov_b32 v11, s35\n"
"s_mov_b64 exec, 1\n"
"global_store_dwordx2 v8, v[10:11], s[6:7] sc1\n"
"s_mov_b64 exec, -1\n"
"L_noE_%=:\n"
"v_mov_b32 v8, s66\n"
"v_mov_b32 v10, s34\n"
"v_mov_b32 v11, s35\n"
"s_mov_b32 s66, 1\n"
"s_waitcnt lgkmcnt(0)\n"
"v_pk_add_f32 v[76:77], v[76:77], v[16:17]\n"
"v_pk_add_f32 v[78:79], v[78:79], v[18:19]\n"
"v_pk_add_f32 v[36:37], v[36:37], v[56:57]\n"
"v_pk_add_f32 v[38:39], v[38:39], v[58:59]\n"
"v_pk_add_f32 v[76:77], v[76:77], v[36:37]\n"
"v_pk_add_f32 v[78:79], v[78:79], v[38:39]\n"
"v_pk_add_f32 v[80:81], v[80:81], v[20:21]\n"
"v_pk_add_f32 v[82:83], v[82:83], v[22:23]\n"
"v_pk_add_f32 v[40:41], v[40:41], v[60:61]\n"
"v_pk_add_f32 v[42:43], v[42:43], v[62:63]\n"
"v_pk_add_f32 v[80:81], v[80:81], v[40:41]\n"
"v_pk_add_f32 v[82:83], v[82:83], v[42:43]\n"
"v_pk_add_f32 v[84:85], v[84:85], v[24:25]\n"
"v_pk_add_f32 v[86:87], v[86:87], v[26:27]\n"
"v_pk_add_f32 v[44:45], v[44:45], v[64:65]\n"
"v_pk_add_f32 v[46:47], v[46:47], v[66:67]\n"
"v_pk_add_f32 v[84:85], v[84:85], v[44:45]\n"
"v_pk_add_f32 v[86:87], v[86:87], v[46:47]\n"
"v_pk_add_f32 v[88:89], v[88:89], v[28:29]\n"
"v_pk_add_f32 v[90:91], v[90:91], v[30:31]\n"
"v_pk_add_f32 v[48:49], v[48:49], v[68:69]\n"
"v_pk_add_f32 v[50:51], v[50:51], v[70:71]\n"
"v_pk_add_f32 v[88:89], v[88:89], v[48:49]\n"
"v_pk_add_f32 v[90:91], v[90:91], v[50:51]\n"
"v_pk_add_f32 v[92:93], v[92:93], v[32:33]\n"
"v_pk_add_f32 v[94:95], v[94:95], v[34:35]\n"
"v_pk_add_f32 v[52:53], v[52:53], v[72:73]\n"
"v_pk_add_f32 v[54:55], v[54:55], v[74:75]\n"
"v_pk_add_f32 v[92:93], v[92:93], v[52:53]\n"
"v_pk_add_f32 v[94:95], v[94:95], v[54:55]\n"
"s_waitcnt vmcnt(29)\n"
"s_branch L_oproj_%=\n"
"L_nomlp1_%=:\n"
"s_waitcnt vmcnt(40)\n"
"s_waitcnt vmcnt(0)\n"
"s_mov_b64 exec, 1\n"
"global_store_dwordx2 v8, v[10:11], s[6:7] sc1\n"
"s_mov_b64 exec, -1\n"
"s_lshl_b32 s69, s65, 2\n"
"s_add_u32 s69, s69, s30\n"
"s_lshl_b32 s69, s69, 18\n"
"s_lshl_b32 s70, s64, 15\n"
"s_add_u32 s69, s69, s70\n"
"s_add_u32 s69, s69, 16777216\n"
"s_mul_i32 s70, s64, 1310720\n"
"s_mul_i32 s71, s68, 320\n"
"s_add_u32 s70, s70, s71\n"
"s_sub_u32 s71, 5242880, s70\n"
"s_add_u32 s52, s28, s69\n"
"s_addc_u32 s53, s29, 0\n"
"s_mov_b32 s54, 262144\n"
"s_mov_b32 s55, 0x20000\n"
"s_add_u32 s96, s16, s70\n"
"s_addc_u32 s97, s17, 0\n"
"s_mov_b32 s98, s71\n"
"s_mov_b32 s99, 0x20000\n"
"s_mov_b32 s56, 0\n"
"s_mov_b32 s57, 131072\n"
"s_mov_b32 s62, 0\n"
"s_mul_i32 s63, s30, 5120\n"
"s_lshl_b32 s92, s30, 4\n"
"s_movk_i32 s93, 3277\n"
"s_movk_i32 s94, 1280\n"
"v_add_u32 v1, 0, v0\n"
"v_mul_u32_u24 v2, s93, v1\n"
"v_lshrrev_b32 v2, 16, v2\n"
"v_mul_u32_u24 v3, 20, v2\n"
"v_sub_u32 v3, v1, v3\n"
"v_bfe_u32 v4, v2, 2, 1\n"
"v_lshlrev_b32 v4, 1, v4\n"
"v_xor_b32 v3, v3, v4\n"
"v_add_u32 v2, s92, v2\n"
"v_mul_u32_u24 v2, s94, v2\n"
"v_lshl_add_u32 v2, v3, 4, v2\n"
"v_mov_b32 v114, v2\n"
"v_add_u32 v1, 64, v0\n"
"v_mul_u32_u24 v2, s93, v1\n"
"v_lshrrev_b32 v2, 16, v2\n"
"v_mul_u32_u24 v3, 20, v2\n"
"v_sub_u32 v3, v1, v3\n"
"v_bfe_u32 v4, v2, 2, 1\n"
"v_lshlrev_b32 v4, 1, v4\n"
"v_xor_b32 v3, v3, v4\n"
"v_add_u32 v2, s92, v2\n"
"v_mul_u32_u24 v2, s94, v2\n"
"v_lshl_add_u32 v2, v3, 4, v2\n"
"v_subrev_u32 v115, 1024, v2\n"
"v_add_u32 v1, 128, v0\n"
"v_mul_u32_u24 v2, s93, v1\n"
"v_lshrrev_b32 v2, 16, v2\n"
"v_mul_u32_u24 v3, 20, v2\n"
"v_sub_u32 v3, v1, v3\n"
"v_bfe_u32 v4, v2, 2, 1\n"
"v_lshlrev_b32 v4, 1, v4\n"
"v_xor_b32 v3, v3, v4\n"
"v_add_u32 v2, s92, v2\n"
"v_mul_u32_u24 v2, s94, v2\n"
"v_lshl_add_u32 v2, v3, 4, v2\n"
"v_subrev_u32 v116, 2048, v2\n"
"v_add_u32 v1, 192, v0\n"
"v_mul_u32_u24 v2, s93, v1\n"
"v_lshrrev_b32 v2, 16, v2\n"
"v_mul_u32_u24 v3, 20, v2\n"
"v_sub_u32 v3, v1, v3\n"
"v_bfe_u32 v4, v2, 2, 1\n"
"v_lshlrev_b32 v4, 1, v4\n"
"v_xor_b32 v3, v3, v4\n"
"v_add_u32 v2, s92, v2\n"
"v_mul_u32_u24 v2, s94, v2\n"
"v_lshl_add_u32 v2, v3, 4, v2\n"
"v_subrev_u32 v117, 3072, v2\n"
"v_add_u32 v1, 256, v0\n"
"v_mul_u32_u24 v2, s93, v1\n"
"v_lshrrev_b32 v2, 16, v2\n"
"v_mul_u32_u24 v3, 20, v2\n"
"v_sub_u32 v3, v1, v3\n"
"v_bfe_u32 v4, v2, 2, 1\n"
"v_lshlrev_b32 v4, 1, v4\n"
"v_xor_b32 v3, v3, v4\n"
"v_add_u32 v2, s92, v2\n"
"v_mul_u32_u24 v2, s94, v2\n"
"v_lshl_add_u32 v2, v3, 4, v2\n"
"v_mov_b32 v118, v2\n"
"v_lshrrev_b32 v1, 2, v0\n"
"v_and_b32 v3, 3, v0\n"
"v_bfe_u32 v2, v0, 4, 1\n"
"v_mul_u32_u24 v1, 320, v1\n"
"v_lshl_add_u32 v1, v3, 3, v1\n"
"v_xor_b32 v4, 0, v2\n"
"v_lshl_add_u32 v112, v4, 5, v1\n"
"v_add_u32 v119, 53248, v112\n"
"v_add_u32 v122, 131072, v112\n"
"v_xor_b32 v4, 1, v2\n"
"v_lshl_add_u32 v113, v4, 5, v1\n"
"v_add_u32 v120, 53248, v113\n"
"v_add_u32 v123, 131072, v113\n"
"s_lshl_b32 s70, s65, 1\n"
"s_lshr_b32 s71, s30, 1\n"
"s_add_u32 s70, s70, s71\n"
"s_lshl_b32 s70, s70, 2\n"
"s_add_u32 s70, s70, s64\n"
"s_lshl_b32 s70, s70, 6\n"
"s_add_u32 s70, s70, 12288\n"
"v_and_b32 v4, 7, v0\n"
"v_lshlrev_b32 v4, 3, v4\n"
"v_add_u32 v4, s70, v4\n"
"s_barrier\n"
"s_add_u32 m0, s63, 102400\n"
"s_nop 0\n"
"buffer_load_dwordx4 v114, s[96:99], s62 offen offset:0 lds\n"
"buffer_load_dwordx4 v115, s[96:99], s62 offen offset:1024 lds\n"
"buffer_load_dwordx4 v116, s[96:99], s62 offen offset:2048 lds\n"
"buffer_load_dwordx4 v117, s[96:99], s62 offen offset:3072 lds\n"
"s_add_u32 m0, s63, 106496\n"
"s_nop 0\n"
"buffer_load_dwordx4 v118, s[96:99], s62 offen offset:0 lds\n"
"s_add_u32 s62, s62, 81920\n"
"s_add_u32 m0, s63, 139264\n"
"s_nop 0\n"
"buffer_load_dwordx4 v114, s[96:99], s62 offen offset:0 lds\n"
"buffer_load_dwordx4 v115, s[96:99], s62 offen offset:1024 lds\n"
"buffer_load_dwordx4 v116, s[96:99], s62 offen offset:2048 lds\n"
"buffer_load_dwordx4 v117, s[96:99], s62 offen offset:3072 lds\n"
"s_add_u32 m0, s63, 143360\n"
"s_nop 0\n"
"buffer_load_dwordx4 v118, s[96:99], s62 offen offset:0 lds\n"
"s_add_u32 s62, s62, 81920\n"
"s_mov_b32 s69, 0x20000\n"
"L_poll0_%=:\n"
"global_load_dwordx2 v[2:3], v4, s[6:7] sc1\n"
"s_waitcnt vmcnt(0)\n"
"v_cmp_ne_u64 vcc, s[34:35], v[2:3]\n"
"s_nop 0\n"
"s_cmp_eq_u64 vcc, 0\n"
"s_cbranch_scc1 L_polled0_%=\n"
"s_sleep 4\n"
"s_sub_u32 s69, s69, 1\n"
"s_cmp_lg_u32 s69, 0\n"
"s_cbranch_scc1 L_poll0_%=\n"
"L_polled0_%=:\n"
"buffer_load_dwordx4 a[88:91], v175, s[52:55], s56 offen offset:0 sc1\n"
"buffer_load_dwordx4 a[92:95], v175, s[52:55], s56 offen offset:1024 sc1\n"
"s_add_u32 s56, s56, 2048\n"
"buffer_load_dwordx4 a[96:99], v175, s[52:55], s57 offen offset:0 sc1\n"
"buffer_load_dwordx4 a[100:103], v175, s[52:55], s57 offen offset:1024 sc1\n"
"s_add_u32 s57, s57, 2048\n"
"buffer_load_dwordx4 a[104:107], v175, s[52:55], s56 offen offset:0 sc1\n"
"buffer_load_dwordx4 a[108:111], v175, s[52:55], s56 offen offset:1024 sc1\n"
"s_add_u32 s56, s56, 2048\n"
"buffer_load_dwordx4 a[112:115], v175, s[52:55], s57 offen offset:0 sc1\n"
"buffer_load_dwordx4 a[116:119], v175, s[52:55], s57 offen offset:1024 sc1\n"
"s_add_u32 s57, s57, 2048\n"
"buffer_load_dwordx4 a[120:123], v175, s[52:55], s56 offen offset:0 sc1\n"
"buffer_load_dwordx4 a[124:127], v175, s[52:55], s56 offen offset:1024 sc1\n"
"s_add_u32 s56, s56, 2048\n"
"buffer_load_dwordx4 a[128:131], v175, s[52:55], s57 offen offset:0 sc1\n"
"buffer_load_dwordx4 a[132:135], v175, s[52:55], s57 offen offset:1024 sc1\n"
"s_add_u32 s57, s57, 2048\n"
"buffer_load_dwordx4 a[136:139], v175, s[52:55], s56 offen offset:0 sc1\n"
"buffer_load_dwordx4 a[140:143], v175, s[52:55], s56 offen offset:1024 sc1\n"
"s_add_u32 s56, s56, 2048\n"
"buffer_load_dwordx4 a[144:147], v175, s[52:55], s57 offen offset:0 sc1\n"
"buffer_load_dwordx4 a[148:151], v175, s[52:55], s57 offen offset:1024 sc1\n"
"s_add_u32 s57, s57, 2048\n"
"buffer_load_dwordx4 a[152:155], v175, s[52:55], s56 offen offset:0 sc1\n"
"buffer_load_dwordx4 a[156:159], v175, s[52:55], s56 offen offset:1024 sc1\n"
"s_add_u32 s56, s56, 2048\n"
"buffer_load_dwordx4 a[160:163], v175, s[52:55], s57 offen offset:0 sc1\n"
"buffer_load_dwordx4 a[164:167], v175, s[52:55], s57 offen offset:1024 sc1\n"
"s_add_u32 s57, s57, 2048\n"
"buffer_load_dwordx4 a[40:43], v175, s[52:55], s56 offen offset:0 sc1\n"
"buffer_load_dwordx4 a[44:47], v175, s[52:55], s56 offen offset:1024 sc1\n"
"s_add_u32 s56, s56, 2048\n"
"buffer_load_dwordx4 a[48:51], v175, s[52:55], s57 offen offset:0 sc1\n"
"buffer_load_dwordx4 a[52:55], v175, s[52:55], s57 offen offset:1024 sc1\n"
"s_add_u32 s57, s57, 2048\n"
"buffer_load_dwordx4 a[56:59], v175, s[52:55], s56 offen offset:0 sc1\n"
"buffer_load_dwordx4 a[60:63], v175, s[52:55], s56 offen offset:1024 sc1\n"
"s_add_u32 s56, s56, 2048\n"
"buffer_load_dwordx4 a[64:67], v175, s[52:55], s57 offen offset:0 sc1\n"
"buffer_load_dwordx4 a[68:71], v175, s[52:55], s57 offen offset:1024 sc1\n"
"s_add_u32 s57, s57, 2048\n"
"s_mov_b32 s66, 0\n"
"s_add_u32 s40, s28, 0\n"
"s_addc_u32 s41, s29, 0\n"
"s_mov_b32 s42, 0\n"
"s_mov_b32 s43, 0x20000\n"
"s_waitcnt vmcnt(0)\n"
"L_oproj_%=:\n"
"s_barrier\n"
"s_add_u32 m0, s63, 0\n"
"s_nop 0\n"
"buffer_load_dwordx4 v114, s[96:99], s62 offen offset:0 lds\n"
"buffer_load_dwordx4 v115, s[96:99], s62 offen offset:1024 lds\n"
"buffer_load_dwordx4 v116, s[96:99], s62 offen offset:2048 lds\n"
"buffer_load_dwordx4 v117, s[96:99], s62 offen offset:3072 lds\n"
"s_add_u32 m0, s63, 4096\n"
"s_nop 0\n"
"buffer_load_dwordx4 v118, s[96:99], s62 offen offset:0 lds\n"
"s_add_u32 s62, s62, 81920\n"
"ds_read_b64_tr_b16 a[0:1], v119 offset:49152\n"
"ds_read_b64_tr_b16 a[2:3], v119 offset:54272\n"
"ds_read_b64_tr_b16 a[4:5], v119 offset:59392\n"
"ds_read_b64_tr_b16 a[6:7], v119 offset:64512\n"
"ds_read_b64_tr_b16 a[8:9], v120 offset:49152\n"
"ds_read_b64_tr_b16 a[10:11], v120 offset:54272\n"
"ds_read_b64_tr_b16 a[12:13], v120 offset:59392\n"
"ds_read_b64_tr_b16 a[14:15], v120 offset:64512\n"
"s_waitcnt lgkmcnt(4)\n"
"v_mfma_f32_16x16x32_bf16 v[176:179], a[0:3], a[88:91], 0\n"
"s_add_u32 m0, s63, 32768\n"
"ds_read_b64_tr_b16 a[16:17], v119 offset:49216\n"
"v_lshlrev_b32 v96, 16, v12\n"
"v_mfma_f32_16x16x32_bf16 v[176:179], a[4:7], a[92:95], v[176:179]\n"
"ds_read_b64_tr_b16 a[18:19], v119 offset:54336\n"
"v_and_b32 v97, 0xffff0000, v12\n"
"v_lshlrev_b32 v98, 16, v13\n"
"v_mfma_f32_16x16x32_bf16 v[216:219], a[0:3], a[96:99], 0\n"
"ds_read_b64_tr_b16 a[20:21], v119 offset:59456\n"
"buffer_load_dwordx4 v114, s[96:99], s62 offen offset:0 lds\n"
"v_mfma_f32_16x16x32_bf16 v[216:219], a[4:7], a[100:103], v[216:219]\n"
"s_waitcnt lgkmcnt(3)\n"
"ds_read_b64_tr_b16 a[22:23], v119 offset:64576\n"
"v_and_b32 v99, 0xffff0000, v13\n"
"v_mfma_f32_16x16x32_bf16 v[180:183], a[8:11], a[88:91], 0\n"
"s_mov_b32 s93, 0xbdd2d3e8\n"
"ds_read_b64_tr_b16 a[24:25], v120 offset:49216\n"
"v_mov_b32 v1, 0xc0135761\n"
"v_mfma_f32_16x16x32_bf16 v[180:183], a[12:15], a[92:95], v[180:183]\n"
"s_movk_i32 s94, 4096\n"
"ds_read_b64_tr_b16 a[26:27], v120 offset:54336\n"
"buffer_load_dwordx4 v115, s[96:99], s62 offen offset:1024 lds\n"
"v_mfma_f32_16x16x32_bf16 v[220:223], a[8:11], a[96:99], 0\n"
"ds_read_b64_tr_b16 a[28:29], v120 offset:59456\n"
"v_bfe_u32 v2, v0, 4, 1\n"
"v_mfma_f32_16x16x32_bf16 v[220:223], a[12:15], a[100:103], v[220:223]\n"
"s_waitcnt lgkmcnt(3)\n"
"ds_read_b64_tr_b16 a[30:31], v120 offset:64576\n"
"v_lshlrev_b32 v2, 10, v2\n"
"v_lshrrev_b32 v3, 5, v0\n"
"v_mfma_f32_16x16x32_bf16 v[184:187], a[16:19], a[88:91], 0\n"
"ds_read_b64_tr_b16 a[0:1], v119 offset:49280\n"
"buffer_load_dwordx4 v116, s[96:99], s62 offen offset:2048 lds\n"
"v_mfma_f32_16x16x32_bf16 v[184:187], a[20:23], a[92:95], v[184:187]\n"
"ds_read_b64_tr_b16 a[2:3], v119 offset:54400\n"
"v_lshl_add_u32 v2, v3, 8, v2\n"
"v_and_b32 v3, 15, v0\n"
"v_mfma_f32_16x16x32_bf16 v[224:227], a[16:19], a[96:99], 0\n"
"ds_read_b64_tr_b16 a[4:5], v119 offset:59520\n"
"v_lshl_add_u32 v2, v3, 4, v2\n"
"v_mfma_f32_16x16x32_bf16 v[224:227], a[20:23], a[100:103], v[224:227]\n"
"s_waitcnt lgkmcnt(3)\n"
"ds_read_b64_tr_b16 a[6:7], v119 offset:64640\n"
"v_add_f32 v76, v76, v96\n"
"v_add_f32 v77, v77, v97\n"
"v_mfma_f32_16x16x32_bf16 v[188:191], a[24:27], a[88:91], 0\n"
"ds_read_b64_tr_b16 a[8:9], v120 offset:49280\n"
"v_add_f32 v78, v78, v98\n"
"v_mfma_f32_16x16x32_bf16 v[188:191], a[28:31], a[92:95], v[188:191]\n"
"ds_read_b64_tr_b16 a[10:11], v120 offset:54400\n"
"v_add_f32 v79, v79, v99\n"
"v_mul_f32 v104, v76, v76\n"
"v_mfma_f32_16x16x32_bf16 v[228:231], a[24:27], a[96:99], 0\n"
"ds_read_b64_tr_b16 a[12:13], v120 offset:59520\n"
"buffer_load_dwordx4 v117, s[96:99], s62 offen offset:3072 lds\n"
"v_mfma_f32_16x16x32_bf16 v[228:231], a[28:31], a[100:103], v[228:231]\n"
"s_waitcnt lgkmcnt(3)\n"
"ds_read_b64_tr_b16 a[14:15], v120 offset:64640\n"
"v_mul_f32 v105, v77, v77\n"
"v_mul_f32 v106, v78, v78\n"
"v_mfma_f32_16x16x32_bf16 v[192:195], a[0:3], a[88:91], 0\n"
"s_add_u32 m0, s63, 36864\n"
"ds_read_b64_tr_b16 a[16:17], v119 offset:49344\n"
"v_mul_f32 v107, v79, v79\n"
"v_mfma_f32_16x16x32_bf16 v[192:195], a[4:7], a[92:95], v[192:195]\n"
"ds_read_b64_tr_b16 a[18:19], v119 offset:54464\n"
"buffer_load_dwordx4 v118, s[96:99], s62 offen offset:0 lds\n"
"v_mfma_f32_16x16x32_bf16 v[232:235], a[0:3], a[96:99], 0\n"
"s_add_u32 s62, s62, 81920\n"
"ds_read_b64_tr_b16 a[20:21], v119 offset:59584\n"
"v_fma_f32 v104, v104, s93, v1\n"
"v_mfma_f32_16x16x32_bf16 v[232:235], a[4:7], a[100:103], v[232:235]\n"
"s_waitcnt lgkmcnt(3)\n"
"ds_read_b64_tr_b16 a[22:23], v119 offset:64704\n"
"v_fma_f32 v105, v105, s93, v1\n"
"v_fma_f32 v106, v106, s93, v1\n"
"v_mfma_f32_16x16x32_bf16 v[196:199], a[8:11], a[88:91], 0\n"
"ds_read_b64_tr_b16 a[24:25], v120 offset:49344\n"
"buffer_load_dwordx4 a[72:75], v175, s[52:55], s56 offen offset:0 sc1\n"
"v_mfma_f32_16x16x32_bf16 v[196:199], a[12:15], a[92:95], v[196:199]\n"
"ds_read_b64_tr_b16 a[26:27], v120 offset:54464\n"
"v_fma_f32 v107, v107, s93, v1\n"
"v_mul_f32 v104, v104, v76\n"
"v_mfma_f32_16x16x32_bf16 v[236:239], a[8:11], a[96:99], 0\n"
"ds_read_b64_tr_b16 a[28:29], v120 offset:59584\n"
"v_mul_f32 v105, v105, v77\n"
"v_mfma_f32_16x16x32_bf16 v[236:239], a[12:15], a[100:103], v[236:239]\n"
"s_waitcnt lgkmcnt(3)\n"
"ds_read_b64_tr_b16 a[30:31], v120 offset:64704\n"
"v_mul_f32 v106, v106, v78\n"
"v_mul_f32 v107, v107, v79\n"
"v_mfma_f32_16x16x32_bf16 v[200:203], a[16:19], a[88:91], 0\n"
"ds_read_b64_tr_b16 a[0:1], v119 offset:49408\n"
"v_exp_f32 v104, v104\n"
"v_mfma_f32_16x16x32_bf16 v[200:203], a[20:23], a[92:95], v[200:203]\n"
"ds_read_b64_tr_b16 a[2:3], v119 offset:54528\n"
"v_exp_f32 v105, v105\n"
"v_exp_f32 v106, v106\n"
"v_mfma_f32_16x16x32_bf16 v[240:243], a[16:19], a[96:99], 0\n"
"ds_read_b64_tr_b16 a[4:5], v119 offset:59648\n"
"buffer_load_dwordx4 a[76:79], v175, s[52:55], s56 offen offset:1024 sc1\n"
"v_mfma_f32_16x16x32_bf16 v[240:243], a[20:23], a[100:103], v[240:243]\n"
"s_waitcnt lgkmcnt(3)\n"
"ds_read_b64_tr_b16 a[6:7], v119 offset:64768\n"
"v_exp_f32 v107, v107\n"
"v_add_f32 v104, 1.0, v104\n"
"v_mfma_f32_16x16x32_bf16 v[204:207], a[24:27], a[88:91], 0\n"
"s_add_u32 s56, s56, 2048\n"
"ds_read_b64_tr_b16 a[8:9], v120 offset:49408\n"
"v_add_f32 v105, 1.0, v105\n"
"v_mfma_f32_16x16x32_bf16 v[204:207], a[28:31], a[92:95], v[204:207]\n"
"ds_read_b64_tr_b16 a[10:11], v120 offset:54528\n"
"buffer_load_dwordx4 a[80:83], v175, s[52:55], s57 offen offset:0 sc1\n"
"v_mfma_f32_16x16x32_bf16 v[244:247], a[24:27], a[96:99], 0\n"
"ds_read_b64_tr_b16 a[12:13], v120 offset:59648\n"
"v_add_f32 v106, 1.0, v106\n"
"v_mfma_f32_16x16x32_bf16 v[244:247], a[28:31], a[100:103], v[244:247]\n"
"s_waitcnt vmcnt(33) lgkmcnt(3)\n"
"ds_read_b64_tr_b16 a[14:15], v120 offset:64768\n"
"v_add_f32 v107, 1.0, v107\n"
"v_rcp_f32 v104, v104\n"
"v_mfma_f32_16x16x32_bf16 v[208:211], a[0:3], a[88:91], 0\n"
"s_barrier\n"
"ds_read_b64_tr_b16 a[16:17], v122 offset:8192\n"
"v_rcp_f32 v105, v105\n"
"v_mfma_f32_16x16x32_bf16 v[208:211], a[4:7], a[92:95], v[208:211]\n"
"ds_read_b64_tr_b16 a[18:19], v122 offset:13312\n"
"v_rcp_f32 v106, v106\n"
"v_rcp_f32 v107, v107\n"
"v_mfma_f32_16x16x32_bf16 v[248:251], a[0:3], a[96:99], 0\n"
"ds_read_b64_tr_b16 a[20:21], v122 offset:18432\n"
"v_mul_f32 v76, v76, v104\n"
"v_mfma_f32_16x16x32_bf16 v[248:251], a[4:7], a[100:103], v[248:251]\n"
"s_waitcnt lgkmcnt(3)\n"
"ds_read_b64_tr_b16 a[22:23], v122 offset:23552\n"
"v_mul_f32 v77, v77, v105\n"
"v_mul_f32 v78, v78, v106\n"
"v_mfma_f32_16x16x32_bf16 v[212:215], a[8:11], a[88:91], 0\n"
"ds_read_b64_tr_b16 a[24:25], v123 offset:8192\n"
"v_mul_f32 v79, v79, v107\n"
"v_mfma_f32_16x16x32_bf16 v[212:215], a[12:15], a[92:95], v[212:215]\n"
"ds_read_b64_tr_b16 a[26:27], v123 offset:13312\n"
"v_cvt_pk_bf16_f32 v16, v76, v77\n"
"v_cvt_pk_bf16_f32 v17, v78, v79\n"
"v_mfma_f32_16x16x32_bf16 v[252:255], a[8:11], a[96:99], 0\n"
"ds_read_b64_tr_b16 a[28:29], v123 offset:18432\n"
"buffer_load_dwordx4 a[84:87], v175, s[52:55], s57 offen offset:1024 sc1\n"
"v_mfma_f32_16x16x32_bf16 v[252:255], a[12:15], a[100:103], v[252:255]\n"
"s_waitcnt lgkmcnt(3)\n"
"ds_read_b64_tr_b16 a[30:31], v123 offset:23552\n"
"v_add_f32 v80, v80, v96\n"
"v_add_f32 v81, v81, v97\n"
"s_add_u32 s57, s57, 2048\n"
"v_mfma_f32_16x16x32_bf16 v[176:179], a[16:19], a[104:107], v[176:179]\n"
"s_add_u32 m0, s63, 53248\n"
"ds_read_b64_tr_b16 a[0:1], v122 offset:8256\n"
"v_add_f32 v82, v82, v98\n"
"v_mfma_f32_16x16x32_bf16 v[176:179], a[20:23], a[108:111], v[176:179]\n"
"ds_read_b64_tr_b16 a[2:3], v122 offset:13376\n"
"v_add_f32 v83, v83, v99\n"
"v_mul_f32 v104, v80, v80\n"
"v_mfma_f32_16x16x32_bf16 v[216:219], a[16:19], a[112:115], v[216:219]\n"
"ds_read_b64_tr_b16 a[4:5], v122 offset:18496\n"
"buffer_load_dwordx4 v114, s[96:99], s62 offen offset:0 lds\n"
"v_mfma_f32_16x16x32_bf16 v[216:219], a[20:23], a[116:119], v[216:219]\n"
"s_waitcnt lgkmcnt(3)\n"
"ds_read_b64_tr_b16 a[6:7], v122 offset:23616\n"
"v_mul_f32 v105, v81, v81\n"
"v_mul_f32 v106, v82, v82\n"
"v_mfma_f32_16x16x32_bf16 v[180:183], a[24:27], a[104:107], v[180:183]\n"
"ds_read_b64_tr_b16 a[8:9], v123 offset:8256\n"
"v_mul_f32 v107, v83, v83\n"
"v_mfma_f32_16x16x32_bf16 v[180:183], a[28:31], a[108:111], v[180:183]\n"
"ds_read_b64_tr_b16 a[10:11], v123 offset:13376\n"
"buffer_load_dwordx4 v115, s[96:99], s62 offen offset:1024 lds\n"
"v_mfma_f32_16x16x32_bf16 v[220:223], a[24:27], a[112:115], v[220:223]\n"
"ds_read_b64_tr_b16 a[12:13], v123 offset:18496\n"
"v_fma_f32 v104, v104, s93, v1\n"
"v_mfma_f32_16x16x32_bf16 v[220:223], a[28:31], a[116:119], v[220:223]\n"
"s_waitcnt lgkmcnt(3)\n"
"ds_read_b64_tr_b16 a[14:15], v123 offset:23616\n"
"v_fma_f32 v105, v105, s93, v1\n"
"v_fma_f32 v106, v106, s93, v1\n"
"v_mfma_f32_16x16x32_bf16 v[184:187], a[0:3], a[104:107], v[184:187]\n"
"ds_read_b64_tr_b16 a[16:17], v122 offset:8320\n"
"buffer_load_dwordx4 v116, s[96:99], s62 offen offset:2048 lds\n"
"v_mfma_f32_16x16x32_bf16 v[184:187], a[4:7], a[108:111], v[184:187]\n"
"ds_read_b64_tr_b16 a[18:19], v122 offset:13440\n"
"v_fma_f32 v107, v107, s93, v1\n"
"v_mul_f32 v104, v104, v80\n"
"v_mfma_f32_16x16x32_bf16 v[224:227], a[0:3], a[112:115], v[224:227]\n"
"ds_read_b64_tr_b16 a[20:21], v122 offset:18560\n"
"v_mul_f32 v105, v105, v81\n"
"v_mfma_f32_16x16x32_bf16 v[224:227], a[4:7], a[116:119], v[224:227]\n"
"s_waitcnt lgkmcnt(3)\n"
"ds_read_b64_tr_b16 a[22:23], v122 offset:23680\n"
"v_mul_f32 v106, v106, v82\n"
"v_mul_f32 v107, v107, v83\n"
"v_mfma_f32_16x16x32_bf16 v[188:191], a[8:11], a[104:107], v[188:191]\n"
"ds_read_b64_tr_b16 a[24:25], v123 offset:8320\n"
"v_exp_f32 v104, v104\n"
"v_mfma_f32_16x16x32_bf16 v[188:191], a[12:15], a[108:111], v[188:191]\n"
"ds_read_b64_tr_b16 a[26:27], v123 offset:13440\n"
"v_exp_f32 v105, v105\n"
"v_exp_f32 v106, v106\n"
"v_mfma_f32_16x16x32_bf16 v[228:231], a[8:11], a[112:115], v[228:231]\n"
"ds_read_b64_tr_b16 a[28:29], v123 offset:18560\n"
"buffer_load_dwordx4 v117, s[96:99], s62 offen offset:3072 lds\n"
"v_mfma_f32_16x16x32_bf16 v[228:231], a[12:15], a[116:119], v[228:231]\n"
"s_waitcnt lgkmcnt(3)\n"
"ds_read_b64_tr_b16 a[30:31], v123 offset:23680\n"
"v_exp_f32 v107, v107\n"
"v_add_f32 v104, 1.0, v104\n"
"v_mfma_f32_16x16x32_bf16 v[192:195], a[16:19], a[104:107], v[192:195]\n"
"s_add_u32 m0, s63, 57344\n"
"ds_read_b64_tr_b16 a[0:1], v122 offset:8384\n"
"v_add_f32 v105, 1.0, v105\n"
"v_mfma_f32_16x16x32_bf16 v[192:195], a[20:23], a[108:111], v[192:195]\n"
"ds_read_b64_tr_b16 a[2:3], v122 offset:13504\n"
"buffer_load_dwordx4 v118, s[96:99], s62 offen offset:0 lds\n"
"v_mfma_f32_16x16x32_bf16 v[232:235], a[16:19], a[112:115], v[232:235]\n"
"s_add_u32 s62, s62, 81920\n"
"ds_read_b64_tr_b16 a[4:5], v122 offset:18624\n"
"v_add_f32 v106, 1.0, v106\n"
"v_mfma_f32_16x16x32_bf16 v[232:235], a[20:23], a[116:119], v[232:235]\n"
"s_waitcnt lgkmcnt(3)\n"
"ds_read_b64_tr_b16 a[6:7], v122 offset:23744\n"
"v_add_f32 v107, 1.0, v107\n"
"v_rcp_f32 v104, v104\n"
"v_mfma_f32_16x16x32_bf16 v[196:199], a[24:27], a[104:107], v[196:199]\n"
"ds_read_b64_tr_b16 a[8:9], v123 offset:8384\n"
"buffer_load_dwordx4 a[88:91], v175, s[52:55], s56 offen offset:0 sc1\n"
"v_mfma_f32_16x16x32_bf16 v[196:199], a[28:31], a[108:111], v[196:199]\n"
"ds_read_b64_tr_b16 a[10:11], v123 offset:13504\n"
"v_rcp_f32 v105, v105\n"
"v_rcp_f32 v106, v106\n"
"v_mfma_f32_16x16x32_bf16 v[236:239], a[24:27], a[112:115], v[236:239]\n"
"ds_read_b64_tr_b16 a[12:13], v123 offset:18624\n"
"v_rcp_f32 v107, v107\n"
"v_mfma_f32_16x16x32_bf16 v[236:239], a[28:31], a[116:119], v[236:239]\n"
"s_waitcnt lgkmcnt(3)\n"
"ds_read_b64_tr_b16 a[14:15], v123 offset:23744\n"
"v_mul_f32 v80, v80, v104\n"
"v_mul_f32 v81, v81, v105\n"
"v_mfma_f32_16x16x32_bf16 v[200:203], a[0:3], a[104:107], v[200:203]\n"
"ds_read_b64_tr_b16 a[16:17], v122 offset:8448\n"
"v_mul_f32 v82, v82, v106\n"
"v_mfma_f32_16x16x32_bf16 v[200:203], a[4:7], a[108:111], v[200:203]\n"
"ds_read_b64_tr_b16 a[18:19], v122 offset:13568\n"
"v_mul_f32 v83, v83, v107\n"
"v_cvt_pk_bf16_f32 v18, v80, v81\n"
"v_mfma_f32_16x16x32_bf16 v[240:243], a[0:3], a[112:115], v[240:243]\n"
"ds_read_b64_tr_b16 a[20:21], v122 offset:18688\n"
"buffer_load_dwordx4 a[92:95], v175, s[52:55], s56 offen offset:1024 sc1\n"
"v_mfma_f32_16x16x32_bf16 v[240:243], a[4:7], a[116:119], v[240:243]\n"
"s_waitcnt lgkmcnt(3)\n"
"ds_read_b64_tr_b16 a[22:23], v122 offset:23808\n"
"v_cvt_pk_bf16_f32 v19, v82, v83\n"
"v_mfma_f32_16x16x32_bf16 v[204:207], a[8:11], a[104:107], v[204:207]\n"
"s_add_u32 s56, s56, 2048\n"
"ds_read_b64_tr_b16 a[24:25], v123 offset:8448\n"
"v_mfma_f32_16x16x32_bf16 v[204:207], a[12:15], a[108:111], v[204:207]\n"
"s_nop 1\n"
"ds_read_b64_tr_b16 a[26:27], v123 offset:13568\n"
"buffer_load_dwordx4 a[96:99], v175, s[52:55], s57 offen offset:0 sc1\n"
"v_mfma_f32_16x16x32_bf16 v[244:247], a[8:11], a[112:115], v[244:247]\n"
"ds_read_b64_tr_b16 a[28:29], v123 offset:18688\n"
"v_permlane16_swap_b32 v16, v18\n"
"v_mfma_f32_16x16x32_bf16 v[244:247], a[12:15], a[116:119], v[244:247]\n"
"s_waitcnt vmcnt(17) lgkmcnt(3)\n"
"ds_read_b64_tr_b16 a[30:31], v123 offset:23808\n"
"v_permlane16_swap_b32 v17, v19\n"
"buffer_store_dwordx4 v[16:19], v2, s[40:43], 0 offen offset:0 sc1\n"
"v_mfma_f32_16x16x32_bf16 v[208:211], a[16:19], a[104:107], v[208:211]\n"
"s_barrier\n"
"ds_read_b64_tr_b16 a[0:1], v112 offset:0\n"
"v_add_f32 v84, v84, v96\n"
"v_mfma_f32_16x16x32_bf16 v[208:211], a[20:23], a[108:111], v[208:211]\n"
"ds_read_b64_tr_b16 a[2:3], v112 offset:5120\n"
"v_add_f32 v85, v85, v97\n"
"v_add_f32 v86, v86, v98\n"
"v_mfma_f32_16x16x32_bf16 v[248:251], a[16:19], a[112:115], v[248:251]\n"
"ds_read_b64_tr_b16 a[4:5], v112 offset:10240\n"
"v_add_f32 v87, v87, v99\n"
"v_mfma_f32_16x16x32_bf16 v[248:251], a[20:23], a[116:119], v[248:251]\n"
"s_waitcnt lgkmcnt(3)\n"
"ds_read_b64_tr_b16 a[6:7], v112 offset:15360\n"
"v_mul_f32 v104, v84, v84\n"
"v_mul_f32 v105, v85, v85\n"
"v_mfma_f32_16x16x32_bf16 v[212:215], a[24:27], a[104:107], v[212:215]\n"
"ds_read_b64_tr_b16 a[8:9], v113 offset:0\n"
"v_mul_f32 v106, v86, v86\n"
"v_mfma_f32_16x16x32_bf16 v[212:215], a[28:31], a[108:111], v[212:215]\n"
"ds_read_b64_tr_b16 a[10:11], v113 offset:5120\n"
"v_mul_f32 v107, v87, v87\n"
"v_fma_f32 v104, v104, s93, v1\n"
"v_mfma_f32_16x16x32_bf16 v[252:255], a[24:27], a[112:115], v[252:255]\n"
"ds_read_b64_tr_b16 a[12:13], v113 offset:10240\n"
"buffer_load_dwordx4 a[100:103], v175, s[52:55], s57 offen offset:1024 sc1\n"
"v_mfma_f32_16x16x32_bf16 v[252:255], a[28:31], a[116:119], v[252:255]\n"
"s_waitcnt lgkmcnt(3)\n"
"ds_read_b64_tr_b16 a[14:15], v113 offset:15360\n"
"v_fma_f32 v105, v105, s93, v1\n"
"v_fma_f32 v106, v106, s93, v1\n"
"s_add_u32 s57, s57, 2048\n"
"v_mfma_f32_16x16x32_bf16 v[176:179], a[0:3], a[120:123], v[176:179]\n"
"s_add_u32 m0, s63, 102400\n"
"ds_read_b64_tr_b16 a[16:17], v112 offset:64\n"
"v_fma_f32 v107, v107, s93, v1\n"
"v_mfma_f32_16x16x32_bf16 v[176:179], a[4:7], a[124:127], v[176:179]\n"
"ds_read_b64_tr_b16 a[18:19], v112 offset:5184\n"
"v_mul_f32 v104, v104, v84\n"
"v_mul_f32 v105, v105, v85\n"
"v_mfma_f32_16x16x32_bf16 v[216:219], a[0:3], a[128:131], v[216:219]\n"
"ds_read_b64_tr_b16 a[20:21], v112 offset:10304\n"
"buffer_load_dwordx4 v114, s[96:99], s62 offen offset:0 lds\n"
"v_mfma_f32_16x16x32_bf16 v[216:219], a[4:7], a[132:135], v[216:219]\n"
"s_waitcnt lgkmcnt(3)\n"
"ds_read_b64_tr_b16 a[22:23], v112 offset:15424\n"
"v_mul_f32 v106, v106, v86\n"
"v_mul_f32 v107, v107, v87\n"
"v_mfma_f32_16x16x32_bf16 v[180:183], a[8:11], a[120:123], v[180:183]\n"
"ds_read_b64_tr_b16 a[24:25], v113 offset:64\n"
"v_exp_f32 v104, v104\n"
"v_mfma_f32_16x16x32_bf16 v[180:183], a[12:15], a[124:127], v[180:183]\n"
"ds_read_b64_tr_b16 a[26:27], v113 offset:5184\n"
"buffer_load_dwordx4 v115, s[96:99], s62 offen offset:1024 lds\n"
"v_mfma_f32_16x16x32_bf16 v[220:223], a[8:11], a[128:131], v[220:223]\n"
"ds_read_b64_tr_b16 a[28:29], v113 offset:10304\n"
"v_exp_f32 v105, v105\n"
"v_mfma_f32_16x16x32_bf16 v[220:223], a[12:15], a[132:135], v[220:223]\n"
"s_waitcnt lgkmcnt(3)\n"
"ds_read_b64_tr_b16 a[30:31], v113 offset:15424\n"
"v_exp_f32 v106, v106\n"
"v_exp_f32 v107, v107\n"
"v_mfma_f32_16x16x32_bf16 v[184:187], a[16:19], a[120:123], v[184:187]\n"
"ds_read_b64_tr_b16 a[0:1], v112 offset:128\n"
"buffer_load_dwordx4 v116, s[96:99], s62 offen offset:2048 lds\n"
"v_mfma_f32_16x16x32_bf16 v[184:187], a[20:23], a[124:127], v[184:187]\n"
"ds_read_b64_tr_b16 a[2:3], v112 offset:5248\n"
"v_add_f32 v104, 1.0, v104\n"
"v_add_f32 v105, 1.0, v105\n"
"v_mfma_f32_16x16x32_bf16 v[224:227], a[16:19], a[128:131], v[224:227]\n"
"ds_read_b64_tr_b16 a[4:5], v112 offset:10368\n"
"v_add_f32 v106, 1.0, v106\n"
"v_mfma_f32_16x16x32_bf16 v[224:227], a[20:23], a[132:135], v[224:227]\n"
"s_waitcnt lgkmcnt(3)\n"
"ds_read_b64_tr_b16 a[6:7], v112 offset:15488\n"
"v_add_f32 v107, 1.0, v107\n"
"v_rcp_f32 v104, v104\n"
"v_mfma_f32_16x16x32_bf16 v[188:191], a[24:27], a[120:123], v[188:191]\n"
"ds_read_b64_tr_b16 a[8:9], v113 offset:128\n"
"v_rcp_f32 v105, v105\n"
"v_mfma_f32_16x16x32_bf16 v[188:191], a[28:31], a[124:127], v[188:191]\n"
"ds_read_b64_tr_b16 a[10:11], v113 offset:5248\n"
"v_rcp_f32 v106, v106\n"
"v_rcp_f32 v107, v107\n"
"v_mfma_f32_16x16x32_bf16 v[228:231], a[24:27], a[128:131], v[228:231]\n"
"ds_read_b64_tr_b16 a[12:13], v113 offset:10368\n"
"buffer_load_dwordx4 v117, s[96:99], s62 offen offset:3072 lds\n"
"v_mfma_f32_16x16x32_bf16 v[228:231], a[28:31], a[132:135], v[228:231]\n"
"s_waitcnt lgkmcnt(3)\n"
"ds_read_b64_tr_b16 a[14:15], v113 offset:15488\n"
"v_mul_f32 v84, v84, v104\n"
"v_mul_f32 v85, v85, v105\n"
"v_mfma_f32_16x16x32_bf16 v[192:195], a[0:3], a[120:123], v[192:195]\n"
"s_add_u32 m0, s63, 106496\n"
"ds_read_b64_tr_b16 a[16:17], v112 offset:192\n"
"v_mul_f32 v86, v86, v106\n"
"v_mfma_f32_16x16x32_bf16 v[192:195], a[4:7], a[124:127], v[192:195]\n"
"ds_read_b64_tr_b16 a[18:19], v112 offset:5312\n"
"buffer_load_dwordx4 v118, s[96:99], s62 offen offset:0 lds\n"
"v_mfma_f32_16x16x32_bf16 v[232:235], a[0:3], a[128:131], v[232:235]\n"
"s_add_u32 s62, s62, 81920\n"
"ds_read_b64_tr_b16 a[20:21], v112 offset:10432\n"
"v_mul_f32 v87, v87, v107\n"
"v_mfma_f32_16x16x32_bf16 v[232:235], a[4:7], a[132:135], v[232:235]\n"
"s_waitcnt lgkmcnt(3)\n"
"ds_read_b64_tr_b16 a[22:23], v112 offset:15552\n"
"v_cvt_pk_bf16_f32 v20, v84, v85\n"
"v_cvt_pk_bf16_f32 v21, v86, v87\n"
"v_mfma_f32_16x16x32_bf16 v[196:199], a[8:11], a[120:123], v[196:199]\n"
"ds_read_b64_tr_b16 a[24:25], v113 offset:192\n"
"buffer_load_dwordx4 a[104:107], v175, s[52:55], s56 offen offset:0 sc1\n"
"v_mfma_f32_16x16x32_bf16 v[196:199], a[12:15], a[124:127], v[196:199]\n"
"ds_read_b64_tr_b16 a[26:27], v113 offset:5312\n"
"v_add_f32 v88, v88, v96\n"
"v_add_f32 v89, v89, v97\n"
"v_mfma_f32_16x16x32_bf16 v[236:239], a[8:11], a[128:131], v[236:239]\n"
"ds_read_b64_tr_b16 a[28:29], v113 offset:10432\n"
"v_add_f32 v90, v90, v98\n"
"v_mfma_f32_16x16x32_bf16 v[236:239], a[12:15], a[132:135], v[236:239]\n"
"s_waitcnt lgkmcnt(3)\n"
"ds_read_b64_tr_b16 a[30:31], v113 offset:15552\n"
"v_add_f32 v91, v91, v99\n"
"v_mul_f32 v104, v88, v88\n"
"v_mfma_f32_16x16x32_bf16 v[200:203], a[16:19], a[120:123], v[200:203]\n"
"ds_read_b64_tr_b16 a[0:1], v112 offset:256\n"
"v_mul_f32 v105, v89, v89\n"
"v_mfma_f32_16x16x32_bf16 v[200:203], a[20:23], a[124:127], v[200:203]\n"
"ds_read_b64_tr_b16 a[2:3], v112 offset:5376\n"
"v_mul_f32 v106, v90, v90\n"
"v_mul_f32 v107, v91, v91\n"
"v_mfma_f32_16x16x32_bf16 v[240:243], a[16:19], a[128:131], v[240:243]\n"
"ds_read_b64_tr_b16 a[4:5], v112 offset:10496\n"
"buffer_load_dwordx4 a[108:111], v175, s[52:55], s56 offen offset:1024 sc1\n"
"v_mfma_f32_16x16x32_bf16 v[240:243], a[20:23], a[132:135], v[240:243]\n"
"s_waitcnt lgkmcnt(3)\n"
"ds_read_b64_tr_b16 a[6:7], v112 offset:15616\n"
"v_fma_f32 v104, v104, s93, v1\n"
"v_fma_f32 v105, v105, s93, v1\n"
"v_mfma_f32_16x16x32_bf16 v[204:207], a[24:27], a[120:123], v[204:207]\n"
"s_add_u32 s56, s56, 2048\n"
"ds_read_b64_tr_b16 a[8:9], v113 offset:256\n"
"v_fma_f32 v106, v106, s93, v1\n"
"v_mfma_f32_16x16x32_bf16 v[204:207], a[28:31], a[124:127], v[204:207]\n"
"ds_read_b64_tr_b16 a[10:11], v113 offset:5376\n"
"buffer_load_dwordx4 a[112:115], v175, s[52:55], s57 offen offset:0 sc1\n"
"v_mfma_f32_16x16x32_bf16 v[244:247], a[24:27], a[128:131], v[244:247]\n"
"ds_read_b64_tr_b16 a[12:13], v113 offset:10496\n"
"v_fma_f32 v107, v107, s93, v1\n"
"v_mfma_f32_16x16x32_bf16 v[244:247], a[28:31], a[132:135], v[244:247]\n"
"s_waitcnt vmcnt(22) lgkmcnt(3)\n"
"ds_read_b64_tr_b16 a[14:15], v113 offset:15616\n"
"v_mul_f32 v104, v104, v88\n"
"v_mul_f32 v105, v105, v89\n"
"v_mfma_f32_16x16x32_bf16 v[208:211], a[0:3], a[120:123], v[208:211]\n"
"s_barrier\n"
"ds_read_b64_tr_b16 a[16:17], v112 offset:32768\n"
"v_mul_f32 v106, v106, v90\n"
"v_mfma_f32_16x16x32_bf16 v[208:211], a[4:7], a[124:127], v[208:211]\n"
"ds_read_b64_tr_b16 a[18:19], v112 offset:37888\n"
"v_mul_f32 v107, v107, v91\n"
"v_exp_f32 v104, v104\n"
"v_mfma_f32_16x16x32_bf16 v[248:251], a[0:3], a[128:131], v[248:251]\n"
"ds_read_b64_tr_b16 a[20:21], v112 offset:43008\n"
"v_exp_f32 v105, v105\n"
"v_mfma_f32_16x16x32_bf16 v[248:251], a[4:7], a[132:135], v[248:251]\n"
"s_waitcnt lgkmcnt(3)\n"
"ds_read_b64_tr_b16 a[22:23], v112 offset:48128\n"
"v_exp_f32 v106, v106\n"
"v_exp_f32 v107, v107\n"
"v_mfma_f32_16x16x32_bf16 v[212:215], a[8:11], a[120:123], v[212:215]\n"
"ds_read_b64_tr_b16 a[24:25], v113 offset:32768\n"
"v_add_f32 v104, 1.0, v104\n"
"v_mfma_f32_16x16x32_bf16 v[212:215], a[12:15], a[124:127], v[212:215]\n"
"ds_read_b64_tr_b16 a[26:27], v113 offset:37888\n"
"v_add_f32 v105, 1.0, v105\n"
"v_add_f32 v106, 1.0, v106\n"
"v_mfma_f32_16x16x32_bf16 v[252:255], a[8:11], a[128:131], v[252:255]\n"
"ds_read_b64_tr_b16 a[28:29], v113 offset:43008\n"
"buffer_load_dwordx4 a[116:119], v175, s[52:55], s57 offen offset:1024 sc1\n"
"v_mfma_f32_16x16x32_bf16 v[252:255], a[12:15], a[132:135], v[252:255]\n"
"s_waitcnt lgkmcnt(3)\n"
"ds_read_b64_tr_b16 a[30:31], v113 offset:48128\n"
"v_add_f32 v107, 1.0, v107\n"
"v_rcp_f32 v104, v104\n"
"s_add_u32 s57, s57, 2048\n"
"v_mfma_f32_16x16x32_bf16 v[176:179], a[16:19], a[136:139], v[176:179]\n"
"s_add_u32 m0, s63, 139264\n"
"ds_read_b64_tr_b16 a[0:1], v112 offset:32832\n"
"v_rcp_f32 v105, v105\n"
"v_mfma_f32_16x16x32_bf16 v[176:179], a[20:23], a[140:143], v[176:179]\n"
"ds_read_b64_tr_b16 a[2:3], v112 offset:37952\n"
"v_rcp_f32 v106, v106\n"
"v_rcp_f32 v107, v107\n"
"v_mfma_f32_16x16x32_bf16 v[216:219], a[16:19], a[144:147], v[216:219]\n"
"ds_read_b64_tr_b16 a[4:5], v112 offset:43072\n"
"buffer_load_dwordx4 v114, s[96:99], s62 offen offset:0 lds\n"
"v_mfma_f32_16x16x32_bf16 v[216:219], a[20:23], a[148:151], v[216:219]\n"
"s_waitcnt lgkmcnt(3)\n"
"ds_read_b64_tr_b16 a[6:7], v112 offset:48192\n"
"v_mul_f32 v88, v88, v104\n"
"v_mul_f32 v89, v89, v105\n"
"v_mfma_f32_16x16x32_bf16 v[180:183], a[24:27], a[136:139], v[180:183]\n"
"ds_read_b64_tr_b16 a[8:9], v113 offset:32832\n"
"v_mul_f32 v90, v90, v106\n"
"v_mfma_f32_16x16x32_bf16 v[180:183], a[28:31], a[140:143], v[180:183]\n"
"ds_read_b64_tr_b16 a[10:11], v113 offset:37952\n"
"buffer_load_dwordx4 v115, s[96:99], s62 offen offset:1024 lds\n"
"v_mfma_f32_16x16x32_bf16 v[220:223], a[24:27], a[144:147], v[220:223]\n"
"ds_read_b64_tr_b16 a[12:13], v113 offset:43072\n"
"v_mul_f32 v91, v91, v107\n"
"v_mfma_f32_16x16x32_bf16 v[220:223], a[28:31], a[148:151], v[220:223]\n"
"s_waitcnt lgkmcnt(3)\n"
"ds_read_b64_tr_b16 a[14:15], v113 offset:48192\n"
"v_cvt_pk_bf16_f32 v22, v88, v89\n"
"v_cvt_pk_bf16_f32 v23, v90, v91\n"
"v_mfma_f32_16x16x32_bf16 v[184:187], a[0:3], a[136:139], v[184:187]\n"
"s_nop 1\n"
"ds_read_b64_tr_b16 a[16:17], v112 offset:32896\n"
"buffer_load_dwordx4 v116, s[96:99], s62 offen offset:2048 lds\n"
"v_mfma_f32_16x16x32_bf16 v[184:187], a[4:7], a[140:143], v[184:187]\n"
"ds_read_b64_tr_b16 a[18:19], v112 offset:38016\n"
"v_permlane16_swap_b32 v20, v22\n"
"v_permlane16_swap_b32 v21, v23\n"
"v_mfma_f32_16x16x32_bf16 v[224:227], a[0:3], a[144:147], v[224:227]\n"
"ds_read_b64_tr_b16 a[20:21], v112 offset:43136\n"
"buffer_store_dwordx4 v[20:23], v2, s[40:43], 0 offen offset:2048 sc1\n"
"v_mfma_f32_16x16x32_bf16 v[224:227], a[4:7], a[148:151], v[224:227]\n"
"s_waitcnt lgkmcnt(3)\n"
"ds_read_b64_tr_b16 a[22:23], v112 offset:48256\n"
"v_add_f32 v92, v92, v96\n"
"v_add_f32 v93, v93, v97\n"
"v_mfma_f32_16x16x32_bf16 v[188:191], a[8:11], a[136:139], v[188:191]\n"
"ds_read_b64_tr_b16 a[24:25], v113 offset:32896\n"
"v_add_f32 v94, v94, v98\n"
"v_mfma_f32_16x16x32_bf16 v[188:191], a[12:15], a[140:143], v[188:191]\n"
"ds_read_b64_tr_b16 a[26:27], v113 offset:38016\n"
"v_add_f32 v95, v95, v99\n"
"v_mul_f32 v104, v92, v92\n"
"v_mfma_f32_16x16x32_bf16 v[228:231], a[8:11], a[144:147], v[228:231]\n"
"ds_read_b64_tr_b16 a[28:29], v113 offset:43136\n"
"buffer_load_dwordx4 v117, s[96:99], s62 offen offset:3072 lds\n"
"v_mfma_f32_16x16x32_bf16 v[228:231], a[12:15], a[148:151], v[228:231]\n"
"s_waitcnt lgkmcnt(3)\n"
"ds_read_b64_tr_b16 a[30:31], v113 offset:48256\n"
"v_mul_f32 v105, v93, v93\n"
"v_mul_f32 v106, v94, v94\n"
"v_mfma_f32_16x16x32_bf16 v[192:195], a[16:19], a[136:139], v[192:195]\n"
"s_add_u32 m0, s63, 143360\n"
"ds_read_b64_tr_b16 a[0:1], v112 offset:32960\n"
"v_mul_f32 v107, v95, v95\n"
"v_mfma_f32_16x16x32_bf16 v[192:195], a[20:23], a[140:143], v[192:195]\n"
"ds_read_b64_tr_b16 a[2:3], v112 offset:38080\n"
"buffer_load_dwordx4 v118, s[96:99], s62 offen offset:0 lds\n"
"v_mfma_f32_16x16x32_bf16 v[232:235], a[16:19], a[144:147], v[232:235]\n"
"s_add_u32 s62, s62, 81920\n"
"ds_read_b64_tr_b16 a[4:5], v112 offset:43200\n"
"v_fma_f32 v104, v104, s93, v1\n"
"v_mfma_f32_16x16x32_bf16 v[232:235], a[20:23], a[148:151], v[232:235]\n"
"s_waitcnt lgkmcnt(3)\n"
"ds_read_b64_tr_b16 a[6:7], v112 offset:48320\n"
"v_fma_f32 v105, v105, s93, v1\n"
"v_fma_f32 v106, v106, s93, v1\n"
"v_mfma_f32_16x16x32_bf16 v[196:199], a[24:27], a[136:139], v[196:199]\n"
"ds_read_b64_tr_b16 a[8:9], v113 offset:32960\n"
"buffer_load_dwordx4 a[120:123], v175, s[52:55], s56 offen offset:0 sc1\n"
"v_mfma_f32_16x16x32_bf16 v[196:199], a[28:31], a[140:143], v[196:199]\n"
"ds_read_b64_tr_b16 a[10:11], v113 offset:38080\n"
"v_fma_f32 v107, v107, s93, v1\n"
"v_mul_f32 v104, v104, v92\n"
"v_mfma_f32_16x16x32_bf16 v[236:239], a[24:27], a[144:147], v[236:239]\n"
"ds_read_b64_tr_b16 a[12:13], v113 offset:43200\n"
"v_mul_f32 v105, v105, v93\n"
"v_mfma_f32_16x16x32_bf16 v[236:239], a[28:31], a[148:151], v[236:239]\n"
"s_waitcnt lgkmcnt(3)\n"
"ds_read_b64_tr_b16 a[14:15], v113 offset:48320\n"
"v_mul_f32 v106, v106, v94\n"
"v_mul_f32 v107, v107, v95\n"
"v_mfma_f32_16x16x32_bf16 v[200:203], a[0:3], a[136:139], v[200:203]\n"
"ds_read_b64_tr_b16 a[16:17], v112 offset:33024\n"
"v_exp_f32 v104, v104\n"
"v_mfma_f32_16x16x32_bf16 v[200:203], a[4:7], a[140:143], v[200:203]\n"
"ds_read_b64_tr_b16 a[18:19], v112 offset:38144\n"
"v_exp_f32 v105, v105\n"
"v_exp_f32 v106, v106\n"
"v_mfma_f32_16x16x32_bf16 v[240:243], a[0:3], a[144:147], v[240:243]\n"
"ds_read_b64_tr_b16 a[20:21], v112 offset:43264\n"
"buffer_load_dwordx4 a[124:127], v175, s[52:55], s56 offen offset:1024 sc1\n"
"v_mfma_f32_16x16x32_bf16 v[240:243], a[4:7], a[148:151], v[240:243]\n"
"s_waitcnt lgkmcnt(3)\n"
"ds_read_b64_tr_b16 a[22:23], v112 offset:48384\n"
"v_exp_f32 v107, v107\n"
"v_add_f32 v104, 1.0, v104\n"
"v_mfma_f32_16x16x32_bf16 v[204:207], a[8:11], a[136:139], v[204:207]\n"
"s_add_u32 s56, s56, 2048\n"
"ds_read_b64_tr_b16 a[24:25], v113 offset:33024\n"
"v_add_f32 v105, 1.0, v105\n"
"v_mfma_f32_16x16x32_bf16 v[204:207], a[12:15], a[140:143], v[204:207]\n"
"ds_read_b64_tr_b16 a[26:27], v113 offset:38144\n"
"buffer_load_dwordx4 a[128:131], v175, s[52:55], s57 offen offset:0 sc1\n"
"v_mfma_f32_16x16x32_bf16 v[244:247], a[8:11], a[144:147], v[244:247]\n"
"ds_read_b64_tr_b16 a[28:29], v113 offset:43264\n"
"v_add_f32 v106, 1.0, v106\n"
"v_mfma_f32_16x16x32_bf16 v[244:247], a[12:15], a[148:151], v[244:247]\n"
"s_waitcnt vmcnt(23) lgkmcnt(3)\n"
"ds_read_b64_tr_b16 a[30:31], v113 offset:48384\n"
"v_add_f32 v107, 1.0, v107\n"
"v_rcp_f32 v104, v104\n"
"v_mfma_f32_16x16x32_bf16 v[208:211], a[16:19], a[136:139], v[208:211]\n"
"s_barrier\n"
"ds_read_b64_tr_b16 a[0:1], v119 offset:0\n"
"v_rcp_f32 v105, v105\n"
"v_mfma_f32_16x16x32_bf16 v[208:211], a[20:23], a[140:143], v[208:211]\n"
"ds_read_b64_tr_b16 a[2:3], v119 offset:5120\n"
"v_rcp_f32 v106, v106\n"
"v_rcp_f32 v107, v107\n"
"v_mfma_f32_16x16x32_bf16 v[248:251], a[16:19], a[144:147], v[248:251]\n"
"ds_read_b64_tr_b16 a[4:5], v119 offset:10240\n"
"v_mul_f32 v92, v92, v104\n"
"v_mfma_f32_16x16x32_bf16 v[248:251], a[20:23], a[148:151], v[248:251]\n"
"s_waitcnt lgkmcnt(3)\n"
"ds_read_b64_tr_b16 a[6:7], v119 offset:15360\n"
"v_mul_f32 v93, v93, v105\n"
"v_mul_f32 v94, v94, v106\n"
"v_mfma_f32_16x16x32_bf16 v[212:215], a[24:27], a[136:139], v[212:215]\n"
"ds_read_b64_tr_b16 a[8:9], v120 offset:0\n"
"v_mul_f32 v95, v95, v107\n"
"v_mfma_f32_16x16x32_bf16 v[212:215], a[28:31], a[140:143], v[212:215]\n"
"ds_read_b64_tr_b16 a[10:11], v120 offset:5120\n"
"v_cvt_pk_bf16_f32 v24, v92, v93\n"
"v_cvt_pk_bf16_f32 v25, v94, v95\n"
"v_mfma_f32_16x16x32_bf16 v[252:255], a[24:27], a[144:147], v[252:255]\n"
"s_nop 1\n"
"ds_read_b64_tr_b16 a[12:13], v120 offset:10240\n"
"buffer_load_dwordx4 a[132:135], v175, s[52:55], s57 offen offset:1024 sc1\n"
"v_mfma_f32_16x16x32_bf16 v[252:255], a[28:31], a[148:151], v[252:255]\n"
"s_waitcnt lgkmcnt(3)\n"
"ds_read_b64_tr_b16 a[14:15], v120 offset:15360\n"
"v_permlane16_swap_b32 v24, v26\n"
"v_permlane16_swap_b32 v25, v27\n"
"s_add_u32 s57, s57, 2048\n"
"v_mfma_f32_16x16x32_bf16 v[176:179], a[0:3], a[152:155], v[176:179]\n"
"s_add_u32 m0, s63, 0\n"
"ds_read_b64_tr_b16 a[16:17], v119 offset:64\n"
"buffer_store_dwordx4 v[24:27], v2, s[40:43], s94 offen offset:0 sc1\n"
"v_mfma_f32_16x16x32_bf16 v[176:179], a[4:7], a[156:159], v[176:179]\n"
"ds_read_b64_tr_b16 a[18:19], v119 offset:5184\n"
"v_mfma_f32_16x16x32_bf16 v[216:219], a[0:3], a[160:163], v[216:219]\n"
"ds_read_b64_tr_b16 a[20:21], v119 offset:10304\n"
"buffer_load_dwordx4 v114, s[96:99], s62 offen offset:0 lds\n"
"v_mfma_f32_16x16x32_bf16 v[216:219], a[4:7], a[164:167], v[216:219]\n"
"s_waitcnt lgkmcnt(3)\n"
"ds_read_b64_tr_b16 a[22:23], v119 offset:15424\n"
"v_mfma_f32_16x16x32_bf16 v[180:183], a[8:11], a[152:155], v[180:183]\n"
"ds_read_b64_tr_b16 a[24:25], v120 offset:64\n"
"v_mfma_f32_16x16x32_bf16 v[180:183], a[12:15], a[156:159], v[180:183]\n"
"ds_read_b64_tr_b16 a[26:27], v120 offset:5184\n"
"buffer_load_dwordx4 v115, s[96:99], s62 offen offset:1024 lds\n"
"v_mfma_f32_16x16x32_bf16 v[220:223], a[8:11], a[160:163], v[220:223]\n"
"ds_read_b64_tr_b16 a[28:29], v120 offset:10304\n"
"v_mfma_f32_16x16x32_bf16 v[220:223], a[12:15], a[164:167], v[220:223]\n"
"s_waitcnt lgkmcnt(3)\n"
"ds_read_b64_tr_b16 a[30:31], v120 offset:15424\n"
"v_mfma_f32_16x16x32_bf16 v[184:187], a[16:19], a[152:155], v[184:187]\n"
"ds_read_b64_tr_b16 a[0:1], v119 offset:128\n"
"buffer_load_dwordx4 v116, s[96:99], s62 offen offset:2048 lds\n"
"v_mfma_f32_16x16x32_bf16 v[184:187], a[20:23], a[156:159], v[184:187]\n"
"ds_read_b64_tr_b16 a[2:3], v119 offset:5248\n"
"v_mfma_f32_16x16x32_bf16 v[224:227], a[16:19], a[160:163], v[224:227]\n"
"ds_read_b64_tr_b16 a[4:5], v119 offset:10368\n"
"v_mfma_f32_16x16x32_bf16 v[224:227], a[20:23], a[164:167], v[224:227]\n"
"s_waitcnt lgkmcnt(3)\n"
"ds_read_b64_tr_b16 a[6:7], v119 offset:15488\n"
"v_mfma_f32_16x16x32_bf16 v[188:191], a[24:27], a[152:155], v[188:191]\n"
"ds_read_b64_tr_b16 a[8:9], v120 offset:128\n"
"v_mfma_f32_16x16x32_bf16 v[188:191], a[28:31], a[156:159], v[188:191]\n"
"ds_read_b64_tr_b16 a[10:11], v120 offset:5248\n"
"v_mfma_f32_16x16x32_bf16 v[228:231], a[24:27], a[160:163], v[228:231]\n"
"ds_read_b64_tr_b16 a[12:13], v120 offset:10368\n"
"buffer_load_dwordx4 v117, s[96:99], s62 offen offset:3072 lds\n"
"v_mfma_f32_16x16x32_bf16 v[228:231], a[28:31], a[164:167], v[228:231]\n"
"s_waitcnt lgkmcnt(3)\n"
"ds_read_b64_tr_b16 a[14:15], v120 offset:15488\n"
"v_mfma_f32_16x16x32_bf16 v[192:195], a[0:3], a[152:155], v[192:195]\n"
"s_add_u32 m0, s63, 4096\n"
"ds_read_b64_tr_b16 a[16:17], v119 offset:192\n"
"v_mfma_f32_16x16x32_bf16 v[192:195], a[4:7], a[156:159], v[192:195]\n"
"ds_read_b64_tr_b16 a[18:19], v119 offset:5312\n"
"buffer_load_dwordx4 v118, s[96:99], s62 offen offset:0 lds\n"
"v_mfma_f32_16x16x32_bf16 v[232:235], a[0:3], a[160:163], v[232:235]\n"
"s_add_u32 s62, s62, 81920\n"
"ds_read_b64_tr_b16 a[20:21], v119 offset:10432\n"
"v_mfma_f32_16x16x32_bf16 v[232:235], a[4:7], a[164:167], v[232:235]\n"
"s_waitcnt lgkmcnt(3)\n"
"ds_read_b64_tr_b16 a[22:23], v119 offset:15552\n"
"v_mfma_f32_16x16x32_bf16 v[196:199], a[8:11], a[152:155], v[196:199]\n"
"ds_read_b64_tr_b16 a[24:25], v120 offset:192\n"
"buffer_load_dwordx4 a[136:139], v175, s[52:55], s56 offen offset:0 sc1\n"
"v_mfma_f32_16x16x32_bf16 v[196:199], a[12:15], a[156:159], v[196:199]\n"
"ds_read_b64_tr_b16 a[26:27], v120 offset:5312\n"
"v_mfma_f32_16x16x32_bf16 v[236:239], a[8:11], a[160:163], v[236:239]\n"
"ds_read_b64_tr_b16 a[28:29], v120 offset:10432\n"
"v_mfma_f32_16x16x32_bf16 v[236:239], a[12:15], a[164:167], v[236:239]\n"
"s_waitcnt lgkmcnt(3)\n"
"ds_read_b64_tr_b16 a[30:31], v120 offset:15552\n"
"v_mfma_f32_16x16x32_bf16 v[200:203], a[16:19], a[152:155], v[200:203]\n"
"ds_read_b64_tr_b16 a[0:1], v119 offset:256\n"
"v_mfma_f32_16x16x32_bf16 v[200:203], a[20:23], a[156:159], v[200:203]\n"
"ds_read_b64_tr_b16 a[2:3], v119 offset:5376\n"
"v_mfma_f32_16x16x32_bf16 v[240:243], a[16:19], a[160:163], v[240:243]\n"
"ds_read_b64_tr_b16 a[4:5], v119 offset:10496\n"
"buffer_load_dwordx4 a[140:143], v175, s[52:55], s56 offen offset:1024 sc1\n"
"v_mfma_f32_16x16x32_bf16 v[240:243], a[20:23], a[164:167], v[240:243]\n"
"s_waitcnt lgkmcnt(3)\n"
"ds_read_b64_tr_b16 a[6:7], v119 offset:15616\n"
"v_mfma_f32_16x16x32_bf16 v[204:207], a[24:27], a[152:155], v[204:207]\n"
"s_add_u32 s56, s56, 2048\n"
"ds_read_b64_tr_b16 a[8:9], v120 offset:256\n"
"v_mfma_f32_16x16x32_bf16 v[204:207], a[28:31], a[156:159], v[204:207]\n"
"ds_read_b64_tr_b16 a[10:11], v120 offset:5376\n"
"buffer_load_dwordx4 a[144:147], v175, s[52:55], s57 offen offset:0 sc1\n"
"v_mfma_f32_16x16x32_bf16 v[244:247], a[24:27], a[160:163], v[244:247]\n"
"ds_read_b64_tr_b16 a[12:13], v120 offset:10496\n"
"v_mfma_f32_16x16x32_bf16 v[244:247], a[28:31], a[164:167], v[244:247]\n"
"s_waitcnt vmcnt(23) lgkmcnt(3)\n"
"ds_read_b64_tr_b16 a[14:15], v120 offset:15616\n"
"v_mfma_f32_16x16x32_bf16 v[208:211], a[0:3], a[152:155], v[208:211]\n"
"s_barrier\n"
"ds_read_b64_tr_b16 a[16:17], v119 offset:49152\n"
"v_mfma_f32_16x16x32_bf16 v[208:211], a[4:7], a[156:159], v[208:211]\n"
"ds_read_b64_tr_b16 a[18:19], v119 offset:54272\n"
"v_mfma_f32_16x16x32_bf16 v[248:251], a[0:3], a[160:163], v[248:251]\n"
"ds_read_b64_tr_b16 a[20:21], v119 offset:59392\n"
"v_mfma_f32_16x16x32_bf16 v[248:251], a[4:7], a[164:167], v[248:251]\n"
"s_waitcnt lgkmcnt(3)\n"
"ds_read_b64_tr_b16 a[22:23], v119 offset:64512\n"
"v_mfma_f32_16x16x32_bf16 v[212:215], a[8:11], a[152:155], v[212:215]\n"
"ds_read_b64_tr_b16 a[24:25], v120 offset:49152\n"
"v_mfma_f32_16x16x32_bf16 v[212:215], a[12:15], a[156:159], v[212:215]\n"
"ds_read_b64_tr_b16 a[26:27], v120 offset:54272\n"
"v_mfma_f32_16x16x32_bf16 v[252:255], a[8:11], a[160:163], v[252:255]\n"
"ds_read_b64_tr_b16 a[28:29], v120 offset:59392\n"
"buffer_load_dwordx4 a[148:151], v175, s[52:55], s57 offen offset:1024 sc1\n"
"v_mfma_f32_16x16x32_bf16 v[252:255], a[12:15], a[164:167], v[252:255]\n"
"s_waitcnt lgkmcnt(3)\n"
"ds_read_b64_tr_b16 a[30:31], v120 offset:64512\n"
"s_add_u32 s57, s57, 2048\n"
"v_mfma_f32_16x16x32_bf16 v[176:179], a[16:19], a[40:43], v[176:179]\n"
"s_add_u32 m0, s63, 32768\n"
"ds_read_b64_tr_b16 a[0:1], v119 offset:49216\n"
"v_mfma_f32_16x16x32_bf16 v[176:179], a[20:23], a[44:47], v[176:179]\n"
"ds_read_b64_tr_b16 a[2:3], v119 offset:54336\n"
"v_mfma_f32_16x16x32_bf16 v[216:219], a[16:19], a[48:51], v[216:219]\n"
"ds_read_b64_tr_b16 a[4:5], v119 offset:59456\n"
"buffer_load_dwordx4 v114, s[96:99], s62 offen offset:0 lds\n"
"v_mfma_f32_16x16x32_bf16 v[216:219], a[20:23], a[52:55], v[216:219]\n"
"s_waitcnt lgkmcnt(3)\n"
"ds_read_b64_tr_b16 a[6:7], v119 offset:64576\n"
"v_mfma_f32_16x16x32_bf16 v[180:183], a[24:27], a[40:43], v[180:183]\n"
"ds_read_b64_tr_b16 a[8:9], v120 offset:49216\n"
"v_mfma_f32_16x16x32_bf16 v[180:183], a[28:31], a[44:47], v[180:183]\n"
"ds_read_b64_tr_b16 a[10:11], v120 offset:54336\n"
"buffer_load_dwordx4 v115, s[96:99], s62 offen offset:1024 lds\n"
"v_mfma_f32_16x16x32_bf16 v[220:223], a[24:27], a[48:51], v[220:223]\n"
"ds_read_b64_tr_b16 a[12:13], v120 offset:59456\n"
"v_mfma_f32_16x16x32_bf16 v[220:223], a[28:31], a[52:55], v[220:223]\n"
"s_waitcnt lgkmcnt(3)\n"
"ds_read_b64_tr_b16 a[14:15], v120 offset:64576\n"
"v_mfma_f32_16x16x32_bf16 v[184:187], a[0:3], a[40:43], v[184:187]\n"
"ds_read_b64_tr_b16 a[16:17], v119 offset:49280\n"
"buffer_load_dwordx4 v116, s[96:99], s62 offen offset:2048 lds\n"
"v_mfma_f32_16x16x32_bf16 v[184:187], a[4:7], a[44:47], v[184:187]\n"
"ds_read_b64_tr_b16 a[18:19], v119 offset:54400\n"
"v_mfma_f32_16x16x32_bf16 v[224:227], a[0:3], a[48:51], v[224:227]\n"
"ds_read_b64_tr_b16 a[20:21], v119 offset:59520\n"
"v_mfma_f32_16x16x32_bf16 v[224:227], a[4:7], a[52:55], v[224:227]\n"
"s_waitcnt lgkmcnt(3)\n"
"ds_read_b64_tr_b16 a[22:23], v119 offset:64640\n"
"v_mfma_f32_16x16x32_bf16 v[188:191], a[8:11], a[40:43], v[188:191]\n"
"ds_read_b64_tr_b16 a[24:25], v120 offset:49280\n"
"v_mfma_f32_16x16x32_bf16 v[188:191], a[12:15], a[44:47], v[188:191]\n"
"ds_read_b64_tr_b16 a[26:27], v120 offset:54400\n"
"v_mfma_f32_16x16x32_bf16 v[228:231], a[8:11], a[48:51], v[228:231]\n"
"ds_read_b64_tr_b16 a[28:29], v120 offset:59520\n"
"buffer_load_dwordx4 v117, s[96:99], s62 offen offset:3072 lds\n"
"v_mfma_f32_16x16x32_bf16 v[228:231], a[12:15], a[52:55], v[228:231]\n"
"s_waitcnt lgkmcnt(3)\n"
"ds_read_b64_tr_b16 a[30:31], v120 offset:64640\n"
"v_mfma_f32_16x16x32_bf16 v[192:195], a[16:19], a[40:43], v[192:195]\n"
"s_add_u32 m0, s63, 36864\n"
"ds_read_b64_tr_b16 a[0:1], v119 offset:49344\n"
"v_mfma_f32_16x16x32_bf16 v[192:195], a[20:23], a[44:47], v[192:195]\n"
"ds_read_b64_tr_b16 a[2:3], v119 offset:54464\n"
"buffer_load_dwordx4 v118, s[96:99], s62 offen offset:0 lds\n"
"v_mfma_f32_16x16x32_bf16 v[232:235], a[16:19], a[48:51], v[232:235]\n"
"s_add_u32 s62, s62, 81920\n"
"ds_read_b64_tr_b16 a[4:5], v119 offset:59584\n"
"v_mfma_f32_16x16x32_bf16 v[232:235], a[20:23], a[52:55], v[232:235]\n"
"s_waitcnt lgkmcnt(3)\n"
"ds_read_b64_tr_b16 a[6:7], v119 offset:64704\n"
"v_mfma_f32_16x16x32_bf16 v[196:199], a[24:27], a[40:43], v[196:199]\n"
"ds_read_b64_tr_b16 a[8:9], v120 offset:49344\n"
"buffer_load_dwordx4 a[152:155], v175, s[52:55], s56 offen offset:0 sc1\n"
"v_mfma_f32_16x16x32_bf16 v[196:199], a[28:31], a[44:47], v[196:199]\n"
"ds_read_b64_tr_b16 a[10:11], v120 offset:54464\n"
"v_mfma_f32_16x16x32_bf16 v[236:239], a[24:27], a[48:51], v[236:239]\n"
"ds_read_b64_tr_b16 a[12:13], v120 offset:59584\n"
"v_mfma_f32_16x16x32_bf16 v[236:239], a[28:31], a[52:55], v[236:239]\n"
"s_waitcnt lgkmcnt(3)\n"
"ds_read_b64_tr_b16 a[14:15], v120 offset:64704\n"
"v_mfma_f32_16x16x32_bf16 v[200:203], a[0:3], a[40:43], v[200:203]\n"
"ds_read_b64_tr_b16 a[16:17], v119 offset:49408\n"
"v_mfma_f32_16x16x32_bf16 v[200:203], a[4:7], a[44:47], v[200:203]\n"
"ds_read_b64_tr_b16 a[18:19], v119 offset:54528\n"
"v_mfma_f32_16x16x32_bf16 v[240:243], a[0:3], a[48:51], v[240:243]\n"
"ds_read_b64_tr_b16 a[20:21], v119 offset:59648\n"
"buffer_load_dwordx4 a[156:159], v175, s[52:55], s56 offen offset:1024 sc1\n"
"v_mfma_f32_16x16x32_bf16 v[240:243], a[4:7], a[52:55], v[240:243]\n"
"s_waitcnt lgkmcnt(3)\n"
"ds_read_b64_tr_b16 a[22:23], v119 offset:64768\n"
"v_mfma_f32_16x16x32_bf16 v[204:207], a[8:11], a[40:43], v[204:207]\n"
"s_add_u32 s56, s56, 2048\n"
"ds_read_b64_tr_b16 a[24:25], v120 offset:49408\n"
"v_mfma_f32_16x16x32_bf16 v[204:207], a[12:15], a[44:47], v[204:207]\n"
"ds_read_b64_tr_b16 a[26:27], v120 offset:54528\n"
"buffer_load_dwordx4 a[160:163], v175, s[52:55], s57 offen offset:0 sc1\n"
"v_mfma_f32_16x16x32_bf16 v[244:247], a[8:11], a[48:51], v[244:247]\n"
"ds_read_b64_tr_b16 a[28:29], v120 offset:59648\n"
"v_mfma_f32_16x16x32_bf16 v[244:247], a[12:15], a[52:55], v[244:247]\n"
"s_waitcnt vmcnt(22) lgkmcnt(3)\n"
"s_waitcnt vmcnt(17)\n"
"s_cmp_eq_u32 s66, 0\n"
"s_cbranch_scc1 L_nouf_%=\n"
"s_mov_b64 exec, 1\n"
"global_store_dwordx2 v8, v[10:11], s[6:7] sc1\n"
"s_mov_b64 exec, -1\n"
"L_nouf_%=:\n"
"ds_read_b64_tr_b16 a[30:31], v120 offset:64768\n"
"v_mfma_f32_16x16x32_bf16 v[208:211], a[16:19], a[40:43], v[208:211]\n"
"s_barrier\n"
"ds_read_b64_tr_b16 a[0:1], v122 offset:8192\n"
"v_mfma_f32_16x16x32_bf16 v[208:211], a[20:23], a[44:47], v[208:211]\n"
"ds_read_b64_tr_b16 a[2:3], v122 offset:13312\n"
"v_mfma_f32_16x16x32_bf16 v[248:251], a[16:19], a[48:51], v[248:251]\n"
"ds_read_b64_tr_b16 a[4:5], v122 offset:18432\n"
"v_mfma_f32_16x16x32_bf16 v[248:251], a[20:23], a[52:55], v[248:251]\n"
"s_waitcnt lgkmcnt(3)\n"
"ds_read_b64_tr_b16 a[6:7], v122 offset:23552\n"
"v_mfma_f32_16x16x32_bf16 v[212:215], a[24:27], a[40:43], v[212:215]\n"
"ds_read_b64_tr_b16 a[8:9], v123 offset:8192\n"
"v_mfma_f32_16x16x32_bf16 v[212:215], a[28:31], a[44:47], v[212:215]\n"
"ds_read_b64_tr_b16 a[10:11], v123 offset:13312\n"
"v_mfma_f32_16x16x32_bf16 v[252:255], a[24:27], a[48:51], v[252:255]\n"
"ds_read_b64_tr_b16 a[12:13], v123 offset:18432\n"
"buffer_load_dwordx4 a[164:167], v175, s[52:55], s57 offen offset:1024 sc1\n"
"v_mfma_f32_16x16x32_bf16 v[252:255], a[28:31], a[52:55], v[252:255]\n"
"s_waitcnt lgkmcnt(3)\n"
"ds_read_b64_tr_b16 a[14:15], v123 offset:23552\n"
"s_add_u32 s57, s57, 2048\n"
"v_mfma_f32_16x16x32_bf16 v[176:179], a[0:3], a[56:59], v[176:179]\n"
"s_add_u32 m0, s63, 53248\n"
"ds_read_b64_tr_b16 a[16:17], v122 offset:8256\n"
"v_mfma_f32_16x16x32_bf16 v[176:179], a[4:7], a[60:63], v[176:179]\n"
"ds_read_b64_tr_b16 a[18:19], v122 offset:13376\n"
"v_mfma_f32_16x16x32_bf16 v[216:219], a[0:3], a[64:67], v[216:219]\n"
"ds_read_b64_tr_b16 a[20:21], v122 offset:18496\n"
"buffer_load_dwordx4 v114, s[96:99], s62 offen offset:0 lds\n"
"v_mfma_f32_16x16x32_bf16 v[216:219], a[4:7], a[68:71], v[216:219]\n"
"s_waitcnt lgkmcnt(3)\n"
"ds_read_b64_tr_b16 a[22:23], v122 offset:23616\n"
"v_mfma_f32_16x16x32_bf16 v[180:183], a[8:11], a[56:59], v[180:183]\n"
"ds_read_b64_tr_b16 a[24:25], v123 offset:8256\n"
"v_mfma_f32_16x16x32_bf16 v[180:183], a[12:15], a[60:63], v[180:183]\n"
"ds_read_b64_tr_b16 a[26:27], v123 offset:13376\n"
"buffer_load_dwordx4 v115, s[96:99], s62 offen offset:1024 lds\n"
"v_mfma_f32_16x16x32_bf16 v[220:223], a[8:11], a[64:67], v[220:223]\n"
"ds_read_b64_tr_b16 a[28:29], v123 offset:18496\n"
"v_mfma_f32_16x16x32_bf16 v[220:223], a[12:15], a[68:71], v[220:223]\n"
"s_waitcnt lgkmcnt(3)\n"
"ds_read_b64_tr_b16 a[30:31], v123 offset:23616\n"
"v_mfma_f32_16x16x32_bf16 v[184:187], a[16:19], a[56:59], v[184:187]\n"
"ds_read_b64_tr_b16 a[0:1], v122 offset:8320\n"
"buffer_load_dwordx4 v116, s[96:99], s62 offen offset:2048 lds\n"
"v_mfma_f32_16x16x32_bf16 v[184:187], a[20:23], a[60:63], v[184:187]\n"
"ds_read_b64_tr_b16 a[2:3], v122 offset:13440\n"
"v_mfma_f32_16x16x32_bf16 v[224:227], a[16:19], a[64:67], v[224:227]\n"
"ds_read_b64_tr_b16 a[4:5], v122 offset:18560\n"
"v_mfma_f32_16x16x32_bf16 v[224:227], a[20:23], a[68:71], v[224:227]\n"
"s_waitcnt lgkmcnt(3)\n"
"ds_read_b64_tr_b16 a[6:7], v122 offset:23680\n"
"v_mfma_f32_16x16x32_bf16 v[188:191], a[24:27], a[56:59], v[188:191]\n"
"ds_read_b64_tr_b16 a[8:9], v123 offset:8320\n"
"v_mfma_f32_16x16x32_bf16 v[188:191], a[28:31], a[60:63], v[188:191]\n"
"ds_read_b64_tr_b16 a[10:11], v123 offset:13440\n"
"v_mfma_f32_16x16x32_bf16 v[228:231], a[24:27], a[64:67], v[228:231]\n"
"ds_read_b64_tr_b16 a[12:13], v123 offset:18560\n"
"buffer_load_dwordx4 v117, s[96:99], s62 offen offset:3072 lds\n"
"v_mfma_f32_16x16x32_bf16 v[228:231], a[28:31], a[68:71], v[228:231]\n"
"s_waitcnt lgkmcnt(3)\n"
"ds_read_b64_tr_b16 a[14:15], v123 offset:23680\n"
"v_mfma_f32_16x16x32_bf16 v[192:195], a[0:3], a[56:59], v[192:195]\n"
"s_add_u32 m0, s63, 57344\n"
"ds_read_b64_tr_b16 a[16:17], v122 offset:8384\n"
"v_mfma_f32_16x16x32_bf16 v[192:195], a[4:7], a[60:63], v[192:195]\n"
"ds_read_b64_tr_b16 a[18:19], v122 offset:13504\n"
"buffer_load_dwordx4 v118, s[96:99], s62 offen offset:0 lds\n"
"v_mfma_f32_16x16x32_bf16 v[232:235], a[0:3], a[64:67], v[232:235]\n"
"s_add_u32 s62, s62, 81920\n"
"ds_read_b64_tr_b16 a[20:21], v122 offset:18624\n"
"v_mfma_f32_16x16x32_bf16 v[232:235], a[4:7], a[68:71], v[232:235]\n"
"s_waitcnt lgkmcnt(3)\n"
"ds_read_b64_tr_b16 a[22:23], v122 offset:23744\n"
"v_mfma_f32_16x16x32_bf16 v[196:199], a[8:11], a[56:59], v[196:199]\n"
"ds_read_b64_tr_b16 a[24:25], v123 offset:8384\n"
"buffer_load_dwordx4 a[40:43], v175, s[52:55], s56 offen offset:0 sc1\n"
"v_mfma_f32_16x16x32_bf16 v[196:199], a[12:15], a[60:63], v[196:199]\n"
"ds_read_b64_tr_b16 a[26:27], v123 offset:13504\n"
"v_mfma_f32_16x16x32_bf16 v[236:239], a[8:11], a[64:67], v[236:239]\n"
"ds_read_b64_tr_b16 a[28:29], v123 offset:18624\n"
"v_mfma_f32_16x16x32_bf16 v[236:239], a[12:15], a[68:71], v[236:239]\n"
"s_waitcnt lgkmcnt(3)\n"
"ds_read_b64_tr_b16 a[30:31], v123 offset:23744\n"
"v_mfma_f32_16x16x32_bf16 v[200:203], a[16:19], a[56:59], v[200:203]\n"
"ds_read_b64_tr_b16 a[0:1], v122 offset:8448\n"
"v_mfma_f32_16x16x32_bf16 v[200:203], a[20:23], a[60:63], v[200:203]\n"
"ds_read_b64_tr_b16 a[2:3], v122 offset:13568\n"
"v_mfma_f32_16x16x32_bf16 v[240:243], a[16:19], a[64:67], v[240:243]\n"
"ds_read_b64_tr_b16 a[4:5], v122 offset:18688\n"
"buffer_load_dwordx4 a[44:47], v175, s[52:55], s56 offen offset:1024 sc1\n"
"v_mfma_f32_16x16x32_bf16 v[240:243], a[20:23], a[68:71], v[240:243]\n"
"s_waitcnt lgkmcnt(3)\n"
"ds_read_b64_tr_b16 a[6:7], v122 offset:23808\n"
"v_mfma_f32_16x16x32_bf16 v[204:207], a[24:27], a[56:59], v[204:207]\n"
"s_add_u32 s56, s56, 2048\n"
"ds_read_b64_tr_b16 a[8:9], v123 offset:8448\n"
"v_mfma_f32_16x16x32_bf16 v[204:207], a[28:31], a[60:63], v[204:207]\n"
"ds_read_b64_tr_b16 a[10:11], v123 offset:13568\n"
"buffer_load_dwordx4 a[48:51], v175, s[52:55], s57 offen offset:0 sc1\n"
"v_mfma_f32_16x16x32_bf16 v[244:247], a[24:27], a[64:67], v[244:247]\n"
"ds_read_b64_tr_b16 a[12:13], v123 offset:18688\n"
"v_mfma_f32_16x16x32_bf16 v[244:247], a[28:31], a[68:71], v[244:247]\n"
"s_waitcnt vmcnt(21) lgkmcnt(3)\n"
"ds_read_b64_tr_b16 a[14:15], v123 offset:23808\n"
"v_mfma_f32_16x16x32_bf16 v[208:211], a[0:3], a[56:59], v[208:211]\n"
"s_barrier\n"
"ds_read_b64_tr_b16 a[16:17], v112 offset:0\n"
"v_mfma_f32_16x16x32_bf16 v[208:211], a[4:7], a[60:63], v[208:211]\n"
"ds_read_b64_tr_b16 a[18:19], v112 offset:5120\n"
"v_mfma_f32_16x16x32_bf16 v[248:251], a[0:3], a[64:67], v[248:251]\n"
"ds_read_b64_tr_b16 a[20:21], v112 offset:10240\n"
"v_mfma_f32_16x16x32_bf16 v[248:251], a[4:7], a[68:71], v[248:251]\n"
"s_waitcnt lgkmcnt(3)\n"
"ds_read_b64_tr_b16 a[22:23], v112 offset:15360\n"
"v_mfma_f32_16x16x32_bf16 v[212:215], a[8:11], a[56:59], v[212:215]\n"
"ds_read_b64_tr_b16 a[24:25], v113 offset:0\n"
"v_mfma_f32_16x16x32_bf16 v[212:215], a[12:15], a[60:63], v[212:215]\n"
"ds_read_b64_tr_b16 a[26:27], v113 offset:5120\n"
"v_mfma_f32_16x16x32_bf16 v[252:255], a[8:11], a[64:67], v[252:255]\n"
"ds_read_b64_tr_b16 a[28:29], v113 offset:10240\n"
"buffer_load_dwordx4 a[52:55], v175, s[52:55], s57 offen offset:1024 sc1\n"
"v_mfma_f32_16x16x32_bf16 v[252:255], a[12:15], a[68:71], v[252:255]\n"
"s_waitcnt lgkmcnt(3)\n"
"ds_read_b64_tr_b16 a[30:31], v113 offset:15360\n"
"s_add_u32 s57, s57, 2048\n"
"v_mfma_f32_16x16x32_bf16 v[176:179], a[16:19], a[72:75], v[176:179]\n"
"s_add_u32 m0, s63, 102400\n"
"ds_read_b64_tr_b16 a[0:1], v112 offset:64\n"
"v_mfma_f32_16x16x32_bf16 v[176:179], a[20:23], a[76:79], v[176:179]\n"
"s_lshr_b32 s69, s64, 1\n"
"ds_read_b64_tr_b16 a[2:3], v112 offset:5184\n"
"v_mfma_f32_16x16x32_bf16 v[216:219], a[16:19], a[80:83], v[216:219]\n"
"s_lshl_b32 s66, s65, 1\n"
"ds_read_b64_tr_b16 a[4:5], v112 offset:10304\n"
"buffer_load_dwordx4 v114, s[96:99], s62 offen offset:0 lds\n"
"v_mfma_f32_16x16x32_bf16 v[216:219], a[20:23], a[84:87], v[216:219]\n"
"s_waitcnt lgkmcnt(3)\n"
"ds_read_b64_tr_b16 a[6:7], v112 offset:15424\n"
"v_mfma_f32_16x16x32_bf16 v[180:183], a[24:27], a[72:75], v[180:183]\n"
"s_add_u32 s66, s66, s69\n"
"ds_read_b64_tr_b16 a[8:9], v113 offset:64\n"
"v_mfma_f32_16x16x32_bf16 v[180:183], a[28:31], a[76:79], v[180:183]\n"
"s_and_b32 s69, s64, 1\n"
"ds_read_b64_tr_b16 a[10:11], v113 offset:5184\n"
"buffer_load_dwordx4 v115, s[96:99], s62 offen offset:1024 lds\n"
"v_mfma_f32_16x16x32_bf16 v[220:223], a[24:27], a[80:83], v[220:223]\n"
"s_lshl_b32 s67, s68, 1\n"
"ds_read_b64_tr_b16 a[12:13], v113 offset:10304\n"
"v_mfma_f32_16x16x32_bf16 v[220:223], a[28:31], a[84:87], v[220:223]\n"
"s_waitcnt lgkmcnt(3)\n"
"ds_read_b64_tr_b16 a[14:15], v113 offset:15424\n"
"v_mfma_f32_16x16x32_bf16 v[184:187], a[0:3], a[72:75], v[184:187]\n"
"s_add_u32 s67, s67, s69\n"
"ds_read_b64_tr_b16 a[16:17], v112 offset:128\n"
"buffer_load_dwordx4 v116, s[96:99], s62 offen offset:2048 lds\n"
"v_mfma_f32_16x16x32_bf16 v[184:187], a[4:7], a[76:79], v[184:187]\n"
"s_mul_i32 s70, s30, 71680\n"
"ds_read_b64_tr_b16 a[18:19], v112 offset:5248\n"
"v_mfma_f32_16x16x32_bf16 v[224:227], a[0:3], a[80:83], v[224:227]\n"
"s_mul_i32 s69, s67, 276480\n"
"ds_read_b64_tr_b16 a[20:21], v112 offset:10368\n"
"v_mfma_f32_16x16x32_bf16 v[224:227], a[4:7], a[84:87], v[224:227]\n"
"s_waitcnt lgkmcnt(3)\n"
"ds_read_b64_tr_b16 a[22:23], v112 offset:15488\n"
"v_mfma_f32_16x16x32_bf16 v[188:191], a[8:11], a[72:75], v[188:191]\n"
"s_add_u32 s69, s69, s70\n"
"ds_read_b64_tr_b16 a[24:25], v113 offset:128\n"
"v_mfma_f32_16x16x32_bf16 v[188:191], a[12:15], a[76:79], v[188:191]\n"
"s_add_u32 s69, s69, 8388608\n"
"ds_read_b64_tr_b16 a[26:27], v113 offset:5248\n"
"v_mfma_f32_16x16x32_bf16 v[228:231], a[8:11], a[80:83], v[228:231]\n"
"s_sub_u32 s70, 276480, s70\n"
"ds_read_b64_tr_b16 a[28:29], v113 offset:10368\n"
"buffer_load_dwordx4 v117, s[96:99], s62 offen offset:3072 lds\n"
"v_mfma_f32_16x16x32_bf16 v[228:231], a[12:15], a[84:87], v[228:231]\n"
"s_waitcnt lgkmcnt(3)\n"
"ds_read_b64_tr_b16 a[30:31], v113 offset:15488\n"
"v_mfma_f32_16x16x32_bf16 v[192:195], a[16:19], a[72:75], v[192:195]\n"
"s_add_u32 m0, s63, 106496\n"
"ds_read_b64_tr_b16 a[0:1], v112 offset:192\n"
"v_mfma_f32_16x16x32_bf16 v[192:195], a[20:23], a[76:79], v[192:195]\n"
"s_lshl_b32 s71, s66, 7\n"
"ds_read_b64_tr_b16 a[2:3], v112 offset:5312\n"
"buffer_load_dwordx4 v118, s[96:99], s62 offen offset:0 lds\n"
"v_mfma_f32_16x16x32_bf16 v[232:235], a[16:19], a[80:83], v[232:235]\n"
"s_add_u32 s62, s62, 81920\n"
"ds_read_b64_tr_b16 a[4:5], v112 offset:10432\n"
"v_mfma_f32_16x16x32_bf16 v[232:235], a[20:23], a[84:87], v[232:235]\n"
"s_waitcnt lgkmcnt(3)\n"
"ds_read_b64_tr_b16 a[6:7], v112 offset:15552\n"
"v_mfma_f32_16x16x32_bf16 v[196:199], a[24:27], a[72:75], v[196:199]\n"
"s_mul_i32 s36, s30, 1835008\n"
"ds_read_b64_tr_b16 a[8:9], v113 offset:192\n"
"buffer_load_dwordx4 a[56:59], v175, s[52:55], s56 offen offset:0 sc1\n"
"v_mfma_f32_16x16x32_bf16 v[196:199], a[28:31], a[76:79], v[196:199]\n"
"s_add_u32 s71, s71, s36\n"
"ds_read_b64_tr_b16 a[10:11], v113 offset:5312\n"
"v_mfma_f32_16x16x32_bf16 v[236:239], a[24:27], a[80:83], v[236:239]\n"
"s_sub_u32 s36, 7077888, s71\n"
"ds_read_b64_tr_b16 a[12:13], v113 offset:10432\n"
"v_mfma_f32_16x16x32_bf16 v[236:239], a[28:31], a[84:87], v[236:239]\n"
"s_waitcnt lgkmcnt(3)\n"
"ds_read_b64_tr_b16 a[14:15], v113 offset:15552\n"
"v_mfma_f32_16x16x32_bf16 v[200:203], a[0:3], a[72:75], v[200:203]\n"
"s_add_u32 s44, s28, s69\n"
"s_addc_u32 s45, s29, 0\n"
"ds_read_b64_tr_b16 a[16:17], v112 offset:256\n"
"v_mfma_f32_16x16x32_bf16 v[200:203], a[4:7], a[76:79], v[200:203]\n"
"s_mov_b32 s46, s70\n"
"ds_read_b64_tr_b16 a[18:19], v112 offset:5376\n"
"v_mfma_f32_16x16x32_bf16 v[240:243], a[0:3], a[80:83], v[240:243]\n"
"s_mov_b32 s47, 0x20000\n"
"ds_read_b64_tr_b16 a[20:21], v112 offset:10496\n"
"buffer_load_dwordx4 a[60:63], v175, s[52:55], s56 offen offset:1024 sc1\n"
"v_mfma_f32_16x16x32_bf16 v[240:243], a[4:7], a[84:87], v[240:243]\n"
"s_waitcnt lgkmcnt(3)\n"
"ds_read_b64_tr_b16 a[22:23], v112 offset:15616\n"
"v_mfma_f32_16x16x32_bf16 v[204:207], a[8:11], a[72:75], v[204:207]\n"
"s_add_u32 s56, s56, 2048\n"
"ds_read_b64_tr_b16 a[24:25], v113 offset:256\n"
"v_mfma_f32_16x16x32_bf16 v[204:207], a[12:15], a[76:79], v[204:207]\n"
"s_add_u32 s48, s22, s71\n"
"s_addc_u32 s49, s23, 0\n"
"ds_read_b64_tr_b16 a[26:27], v113 offset:5376\n"
"buffer_load_dwordx4 a[64:67], v175, s[52:55], s57 offen offset:0 sc1\n"
"v_mfma_f32_16x16x32_bf16 v[244:247], a[8:11], a[80:83], v[244:247]\n"
"s_mov_b32 s50, s36\n"
"ds_read_b64_tr_b16 a[28:29], v113 offset:10496\n"
"v_mfma_f32_16x16x32_bf16 v[244:247], a[12:15], a[84:87], v[244:247]\n"
"s_waitcnt vmcnt(21) lgkmcnt(3)\n"
"ds_read_b64_tr_b16 a[30:31], v113 offset:15616\n"
"v_mfma_f32_16x16x32_bf16 v[208:211], a[16:19], a[72:75], v[208:211]\n"
"s_barrier\n"
"ds_read_b64_tr_b16 a[0:1], v112 offset:32768\n"
"v_mfma_f32_16x16x32_bf16 v[208:211], a[20:23], a[76:79], v[208:211]\n"
"s_mov_b32 s51, 0x20000\n"
"ds_read_b64_tr_b16 a[2:3], v112 offset:37888\n"
"v_mfma_f32_16x16x32_bf16 v[248:251], a[16:19], a[80:83], v[248:251]\n"
"s_lshl_b32 s61, s30, 13\n"
"ds_read_b64_tr_b16 a[4:5], v112 offset:43008\n"
"v_lshrrev_b32 v1, 3, v0\n"
"v_mfma_f32_16x16x32_bf16 v[248:251], a[20:23], a[84:87], v[248:251]\n"
"s_waitcnt lgkmcnt(3)\n"
"ds_read_b64_tr_b16 a[6:7], v112 offset:48128\n"
"v_and_b32 v2, 7, v0\n"
"v_mfma_f32_16x16x32_bf16 v[212:215], a[24:27], a[72:75], v[212:215]\n"
"s_mov_b32 s92, 4096\n"
"ds_read_b64_tr_b16 a[8:9], v113 offset:32768\n"
"v_mul_lo_u32 v3, v1, s92\n"
"v_mfma_f32_16x16x32_bf16 v[212:215], a[28:31], a[76:79], v[212:215]\n"
"ds_read_b64_tr_b16 a[10:11], v113 offset:37888\n"
"v_bfe_u32 v4, v0, 4, 1\n"
"v_mfma_f32_16x16x32_bf16 v[252:255], a[24:27], a[80:83], v[252:255]\n"
"ds_read_b64_tr_b16 a[12:13], v113 offset:43008\n"
"buffer_load_dwordx4 a[68:71], v175, s[52:55], s57 offen offset:1024 sc1\n"
"v_mfma_f32_16x16x32_bf16 v[252:255], a[28:31], a[84:87], v[252:255]\n"
"s_waitcnt lgkmcnt(3)\n"
"ds_read_b64_tr_b16 a[14:15], v113 offset:48128\n"
"v_lshlrev_b32 v4, 1, v4\n"
"s_add_u32 s57, s57, 2048\n"
"v_mfma_f32_16x16x32_bf16 v[176:179], a[0:3], a[88:91], v[176:179]\n"
"s_add_u32 m0, s63, 139264\n"
"ds_read_b64_tr_b16 a[16:17], v112 offset:32832\n"
"v_xor_b32 v5, v4, v2\n"
"v_mfma_f32_16x16x32_bf16 v[176:179], a[4:7], a[92:95], v[176:179]\n"
"ds_read_b64_tr_b16 a[18:19], v112 offset:37952\n"
"v_lshl_add_u32 v5, v5, 4, v3\n"
"v_mfma_f32_16x16x32_bf16 v[216:219], a[0:3], a[96:99], v[216:219]\n"
"ds_read_b64_tr_b16 a[20:21], v112 offset:43072\n"
"buffer_load_dwordx4 v114, s[96:99], s62 offen offset:0 lds\n"
"v_mfma_f32_16x16x32_bf16 v[216:219], a[4:7], a[100:103], v[216:219]\n"
"s_waitcnt lgkmcnt(3)\n"
"ds_read_b64_tr_b16 a[22:23], v112 offset:48192\n"
"v_add_u32 v124, 0, v5\n"
"v_mfma_f32_16x16x32_bf16 v[180:183], a[8:11], a[88:91], v[180:183]\n"
"ds_read_b64_tr_b16 a[24:25], v113 offset:32832\n"
"v_or_b32 v5, 4, v4\n"
"v_mfma_f32_16x16x32_bf16 v[180:183], a[12:15], a[92:95], v[180:183]\n"
"ds_read_b64_tr_b16 a[26:27], v113 offset:37952\n"
"buffer_load_dwordx4 v115, s[96:99], s62 offen offset:1024 lds\n"
"v_mfma_f32_16x16x32_bf16 v[220:223], a[8:11], a[96:99], v[220:223]\n"
"ds_read_b64_tr_b16 a[28:29], v113 offset:43072\n"
"v_xor_b32 v5, v5, v2\n"
"v_mfma_f32_16x16x32_bf16 v[220:223], a[12:15], a[100:103], v[220:223]\n"
"s_waitcnt lgkmcnt(3)\n"
"ds_read_b64_tr_b16 a[30:31], v113 offset:48192\n"
"v_lshl_add_u32 v5, v5, 4, v3\n"
"v_mfma_f32_16x16x32_bf16 v[184:187], a[16:19], a[88:91], v[184:187]\n"
"ds_read_b64_tr_b16 a[0:1], v112 offset:32896\n"
"buffer_load_dwordx4 v116, s[96:99], s62 offen offset:2048 lds\n"
"v_mfma_f32_16x16x32_bf16 v[184:187], a[20:23], a[92:95], v[184:187]\n"
"ds_read_b64_tr_b16 a[2:3], v112 offset:38016\n"
"v_add_u32 v125, 31744, v5\n"
"v_mfma_f32_16x16x32_bf16 v[224:227], a[16:19], a[96:99], v[224:227]\n"
"ds_read_b64_tr_b16 a[4:5], v112 offset:43136\n"
"v_xor_b32 v5, v4, v2\n"
"v_mfma_f32_16x16x32_bf16 v[224:227], a[20:23], a[100:103], v[224:227]\n"
"s_waitcnt lgkmcnt(3)\n"
"ds_read_b64_tr_b16 a[6:7], v112 offset:48256\n"
"v_lshl_add_u32 v5, v5, 4, v3\n"
"v_mfma_f32_16x16x32_bf16 v[188:191], a[24:27], a[88:91], v[188:191]\n"
"ds_read_b64_tr_b16 a[8:9], v113 offset:32896\n"
"v_add_u32 v126, 63488, v5\n"
"v_mfma_f32_16x16x32_bf16 v[188:191], a[28:31], a[92:95], v[188:191]\n"
"ds_read_b64_tr_b16 a[10:11], v113 offset:38016\n"
"v_or_b32 v5, 4, v4\n"
"v_mfma_f32_16x16x32_bf16 v[228:231], a[24:27], a[96:99], v[228:231]\n"
"ds_read_b64_tr_b16 a[12:13], v113 offset:43136\n"
"buffer_load_dwordx4 v117, s[96:99], s62 offen offset:3072 lds\n"
"v_mfma_f32_16x16x32_bf16 v[228:231], a[28:31], a[100:103], v[228:231]\n"
"s_waitcnt lgkmcnt(3)\n"
"ds_read_b64_tr_b16 a[14:15], v113 offset:48256\n"
"v_xor_b32 v5, v5, v2\n"
"v_mfma_f32_16x16x32_bf16 v[192:195], a[0:3], a[88:91], v[192:195]\n"
"s_add_u32 m0, s63, 143360\n"
"ds_read_b64_tr_b16 a[16:17], v112 offset:32960\n"
"v_lshl_add_u32 v5, v5, 4, v3\n"
"v_mfma_f32_16x16x32_bf16 v[192:195], a[4:7], a[92:95], v[192:195]\n"
"ds_read_b64_tr_b16 a[18:19], v112 offset:38080\n"
"buffer_load_dwordx4 v118, s[96:99], s62 offen offset:0 lds\n"
"v_mfma_f32_16x16x32_bf16 v[232:235], a[0:3], a[96:99], v[232:235]\n"
"s_add_u32 s62, s62, 81920\n"
"ds_read_b64_tr_b16 a[20:21], v112 offset:43200\n"
"v_add_u32 v127, 95232, v5\n"
"v_mfma_f32_16x16x32_bf16 v[232:235], a[4:7], a[100:103], v[232:235]\n"
"s_waitcnt lgkmcnt(3)\n"
"ds_read_b64_tr_b16 a[22:23], v112 offset:48320\n"
"v_lshrrev_b32 v1, 4, v0\n"
"v_mfma_f32_16x16x32_bf16 v[196:199], a[8:11], a[88:91], v[196:199]\n"
"ds_read_b64_tr_b16 a[24:25], v113 offset:32960\n"
"buffer_load_dwordx4 a[72:75], v175, s[52:55], s56 offen offset:0 sc1\n"
"v_mfma_f32_16x16x32_bf16 v[196:199], a[12:15], a[92:95], v[196:199]\n"
"ds_read_b64_tr_b16 a[26:27], v113 offset:38080\n"
"v_bfe_u32 v2, v0, 2, 2\n"
"v_mfma_f32_16x16x32_bf16 v[236:239], a[8:11], a[96:99], v[236:239]\n"
"ds_read_b64_tr_b16 a[28:29], v113 offset:43200\n"
"v_and_b32 v3, 3, v0\n"
"v_mfma_f32_16x16x32_bf16 v[236:239], a[12:15], a[100:103], v[236:239]\n"
"s_waitcnt lgkmcnt(3)\n"
"ds_read_b64_tr_b16 a[30:31], v113 offset:48320\n"
"v_lshl_add_u32 v4, v1, 3, v2\n"
"v_mfma_f32_16x16x32_bf16 v[200:203], a[16:19], a[88:91], v[200:203]\n"
"ds_read_b64_tr_b16 a[0:1], v112 offset:33024\n"
"v_lshrrev_b32 v5, 1, v2\n"
"v_mfma_f32_16x16x32_bf16 v[200:203], a[20:23], a[92:95], v[200:203]\n"
"ds_read_b64_tr_b16 a[2:3], v112 offset:38144\n"
"v_and_b32 v6, 1, v1\n"
"v_mfma_f32_16x16x32_bf16 v[240:243], a[16:19], a[96:99], v[240:243]\n"
"ds_read_b64_tr_b16 a[4:5], v112 offset:43264\n"
"buffer_load_dwordx4 a[76:79], v175, s[52:55], s56 offen offset:1024 sc1\n"
"v_mfma_f32_16x16x32_bf16 v[240:243], a[20:23], a[100:103], v[240:243]\n"
"s_waitcnt lgkmcnt(3)\n"
"ds_read_b64_tr_b16 a[6:7], v112 offset:48384\n"
"v_lshl_or_b32 v5, v6, 1, v5\n"
"v_mfma_f32_16x16x32_bf16 v[204:207], a[24:27], a[88:91], v[204:207]\n"
"s_add_u32 s56, s56, 2048\n"
"ds_read_b64_tr_b16 a[8:9], v113 offset:33024\n"
"v_lshlrev_b32 v4, 7, v4\n"
"v_mfma_f32_16x16x32_bf16 v[204:207], a[28:31], a[92:95], v[204:207]\n"
"ds_read_b64_tr_b16 a[10:11], v113 offset:38144\n"
"buffer_load_dwordx4 a[80:83], v175, s[52:55], s57 offen offset:0 sc1\n"
"v_mfma_f32_16x16x32_bf16 v[244:247], a[24:27], a[96:99], v[244:247]\n"
"ds_read_b64_tr_b16 a[12:13], v113 offset:43264\n"
"v_lshl_add_u32 v4, v3, 3, v4\n"
"v_mfma_f32_16x16x32_bf16 v[244:247], a[28:31], a[100:103], v[244:247]\n"
"s_waitcnt vmcnt(21) lgkmcnt(3)\n"
"ds_read_b64_tr_b16 a[14:15], v113 offset:48384\n"
"v_add_u32 v4, s61, v4\n"
"v_mfma_f32_16x16x32_bf16 v[208:211], a[0:3], a[88:91], v[208:211]\n"
"s_barrier\n"
"ds_read_b64_tr_b16 a[16:17], v119 offset:0\n"
"v_mfma_f32_16x16x32_bf16 v[208:211], a[4:7], a[92:95], v[208:211]\n"
"s_add_u32 s92, s30, 1\n"
"ds_read_b64_tr_b16 a[18:19], v119 offset:5120\n"
"v_mfma_f32_16x16x32_bf16 v[248:251], a[0:3], a[96:99], v[248:251]\n"
"s_and_b32 s92, s92, 3\n"
"ds_read_b64_tr_b16 a[20:21], v119 offset:10240\n"
"v_xor_b32 v6, s92, v5\n"
"v_mfma_f32_16x16x32_bf16 v[248:251], a[4:7], a[100:103], v[248:251]\n"
"s_waitcnt lgkmcnt(3)\n"
"ds_read_b64_tr_b16 a[22:23], v119 offset:15360\n"
"v_lshl_add_u32 v160, v6, 5, v4\n"
"v_mfma_f32_16x16x32_bf16 v[212:215], a[8:11], a[88:91], v[212:215]\n"
"ds_read_b64_tr_b16 a[24:25], v120 offset:0\n"
"v_add_u32 v164, 0x10000, v160\n"
"v_mfma_f32_16x16x32_bf16 v[212:215], a[12:15], a[92:95], v[212:215]\n"
"ds_read_b64_tr_b16 a[26:27], v120 offset:5120\n"
"v_add_u32 v168, 0x20000, v160\n"
"v_mfma_f32_16x16x32_bf16 v[252:255], a[8:11], a[96:99], v[252:255]\n"
"s_add_u32 s92, s30, 2\n"
"ds_read_b64_tr_b16 a[28:29], v120 offset:10240\n"
"buffer_load_dwordx4 a[84:87], v175, s[52:55], s57 offen offset:1024 sc1\n"
"v_mfma_f32_16x16x32_bf16 v[252:255], a[12:15], a[100:103], v[252:255]\n"
"s_waitcnt lgkmcnt(3)\n"
"ds_read_b64_tr_b16 a[30:31], v120 offset:15360\n"
"s_add_u32 s57, s57, 2048\n"
"v_mfma_f32_16x16x32_bf16 v[176:179], a[16:19], a[104:107], v[176:179]\n"
"s_add_u32 m0, s63, 0\n"
"ds_read_b64_tr_b16 a[0:1], v119 offset:64\n"
"v_mfma_f32_16x16x32_bf16 v[176:179], a[20:23], a[108:111], v[176:179]\n"
"s_and_b32 s92, s92, 3\n"
"ds_read_b64_tr_b16 a[2:3], v119 offset:5184\n"
"v_xor_b32 v6, s92, v5\n"
"v_mfma_f32_16x16x32_bf16 v[216:219], a[16:19], a[112:115], v[216:219]\n"
"ds_read_b64_tr_b16 a[4:5], v119 offset:10304\n"
"buffer_load_dwordx4 v114, s[96:99], s62 offen offset:0 lds\n"
"v_mfma_f32_16x16x32_bf16 v[216:219], a[20:23], a[116:119], v[216:219]\n"
"s_waitcnt lgkmcnt(3)\n"
"ds_read_b64_tr_b16 a[6:7], v119 offset:15424\n"
"v_lshl_add_u32 v161, v6, 5, v4\n"
"v_mfma_f32_16x16x32_bf16 v[180:183], a[24:27], a[104:107], v[180:183]\n"
"ds_read_b64_tr_b16 a[8:9], v120 offset:64\n"
"v_add_u32 v165, 0x10000, v161\n"
"v_mfma_f32_16x16x32_bf16 v[180:183], a[28:31], a[108:111], v[180:183]\n"
"ds_read_b64_tr_b16 a[10:11], v120 offset:5184\n"
"buffer_load_dwordx4 v115, s[96:99], s62 offen offset:1024 lds\n"
"v_mfma_f32_16x16x32_bf16 v[220:223], a[24:27], a[112:115], v[220:223]\n"
"ds_read_b64_tr_b16 a[12:13], v120 offset:10304\n"
"v_add_u32 v169, 0x20000, v161\n"
"v_mfma_f32_16x16x32_bf16 v[220:223], a[28:31], a[116:119], v[220:223]\n"
"s_waitcnt lgkmcnt(3)\n"
"ds_read_b64_tr_b16 a[14:15], v120 offset:15424\n"
"v_mfma_f32_16x16x32_bf16 v[184:187], a[0:3], a[104:107], v[184:187]\n"
"s_add_u32 s92, s30, 3\n"
"ds_read_b64_tr_b16 a[16:17], v119 offset:128\n"
"buffer_load_dwordx4 v116, s[96:99], s62 offen offset:2048 lds\n"
"v_mfma_f32_16x16x32_bf16 v[184:187], a[4:7], a[108:111], v[184:187]\n"
"s_and_b32 s92, s92, 3\n"
"ds_read_b64_tr_b16 a[18:19], v119 offset:5248\n"
"v_xor_b32 v6, s92, v5\n"
"v_mfma_f32_16x16x32_bf16 v[224:227], a[0:3], a[112:115], v[224:227]\n"
"ds_read_b64_tr_b16 a[20:21], v119 offset:10368\n"
"v_lshl_add_u32 v162, v6, 5, v4\n"
"v_mfma_f32_16x16x32_bf16 v[224:227], a[4:7], a[116:119], v[224:227]\n"
"s_waitcnt lgkmcnt(3)\n"
"ds_read_b64_tr_b16 a[22:23], v119 offset:15488\n"
"v_add_u32 v166, 0x10000, v162\n"
"v_mfma_f32_16x16x32_bf16 v[188:191], a[8:11], a[104:107], v[188:191]\n"
"ds_read_b64_tr_b16 a[24:25], v120 offset:128\n"
"v_add_u32 v170, 0x20000, v162\n"
"v_mfma_f32_16x16x32_bf16 v[188:191], a[12:15], a[108:111], v[188:191]\n"
"s_add_u32 s92, s30, 4\n"
"ds_read_b64_tr_b16 a[26:27], v120 offset:5248\n"
"v_mfma_f32_16x16x32_bf16 v[228:231], a[8:11], a[112:115], v[228:231]\n"
"s_and_b32 s92, s92, 3\n"
"ds_read_b64_tr_b16 a[28:29], v120 offset:10368\n"
"buffer_load_dwordx4 v117, s[96:99], s62 offen offset:3072 lds\n"
"v_mfma_f32_16x16x32_bf16 v[228:231], a[12:15], a[116:119], v[228:231]\n"
"s_waitcnt lgkmcnt(3)\n"
"ds_read_b64_tr_b16 a[30:31], v120 offset:15488\n"
"v_xor_b32 v6, s92, v5\n"
"v_mfma_f32_16x16x32_bf16 v[192:195], a[16:19], a[104:107], v[192:195]\n"
"s_add_u32 m0, s63, 4096\n"
"ds_read_b64_tr_b16 a[0:1], v119 offset:192\n"
"v_lshl_add_u32 v163, v6, 5, v4\n"
"v_mfma_f32_16x16x32_bf16 v[192:195], a[20:23], a[108:111], v[192:195]\n"
"ds_read_b64_tr_b16 a[2:3], v119 offset:5312\n"
"buffer_load_dwordx4 v118, s[96:99], s62 offen offset:0 lds\n"
"v_mfma_f32_16x16x32_bf16 v[232:235], a[16:19], a[112:115], v[232:235]\n"
"s_add_u32 s62, s62, 81920\n"
"ds_read_b64_tr_b16 a[4:5], v119 offset:10432\n"
"v_add_u32 v167, 0x10000, v163\n"
"v_mfma_f32_16x16x32_bf16 v[232:235], a[20:23], a[116:119], v[232:235]\n"
"s_waitcnt lgkmcnt(3)\n"
"ds_read_b64_tr_b16 a[6:7], v119 offset:15552\n"
"v_add_u32 v171, 0x20000, v163\n"
"v_mfma_f32_16x16x32_bf16 v[196:199], a[24:27], a[104:107], v[196:199]\n"
"s_and_b32 s69, s5, 3\n"
"ds_read_b64_tr_b16 a[8:9], v120 offset:192\n"
"v_mfma_f32_16x16x32_bf16 v[196:199], a[28:31], a[108:111], v[196:199]\n"
"s_lshl_b32 s69, s69, 1\n"
"ds_read_b64_tr_b16 a[10:11], v120 offset:5312\n"
"v_mfma_f32_16x16x32_bf16 v[236:239], a[24:27], a[112:115], v[236:239]\n"
"s_lshr_b32 s70, s5, 2\n"
"ds_read_b64_tr_b16 a[12:13], v120 offset:10432\n"
"v_mfma_f32_16x16x32_bf16 v[236:239], a[28:31], a[116:119], v[236:239]\n"
"s_waitcnt lgkmcnt(3)\n"
"ds_read_b64_tr_b16 a[14:15], v120 offset:15552\n"
"v_mfma_f32_16x16x32_bf16 v[200:203], a[0:3], a[104:107], v[200:203]\n"
"s_add_u32 s69, s69, s70\n"
"ds_read_b64_tr_b16 a[16:17], v119 offset:256\n"
"v_mfma_f32_16x16x32_bf16 v[200:203], a[4:7], a[108:111], v[200:203]\n"
"s_sub_u32 s70, s69, 7\n"
"s_cselect_b32 s69, s69, s70\n"
"ds_read_b64_tr_b16 a[18:19], v119 offset:5376\n"
"v_mfma_f32_16x16x32_bf16 v[240:243], a[0:3], a[112:115], v[240:243]\n"
"s_mov_b32 s69, 0\n"
"ds_read_b64_tr_b16 a[20:21], v119 offset:10496\n"
"v_mfma_f32_16x16x32_bf16 v[240:243], a[4:7], a[116:119], v[240:243]\n"
"s_waitcnt lgkmcnt(3)\n"
"ds_read_b64_tr_b16 a[22:23], v119 offset:15616\n"
"v_mfma_f32_16x16x32_bf16 v[204:207], a[8:11], a[104:107], v[204:207]\n"
"s_mul_i32 s58, s69, 10240\n"
"ds_read_b64_tr_b16 a[24:25], v120 offset:256\n"
"v_mfma_f32_16x16x32_bf16 v[204:207], a[12:15], a[108:111], v[204:207]\n"
"s_mul_i32 s60, s69, 262144\n"
"ds_read_b64_tr_b16 a[26:27], v120 offset:5376\n"
"v_mfma_f32_16x16x32_bf16 v[244:247], a[8:11], a[112:115], v[244:247]\n"
"s_mul_i32 s70, s30, 28\n"
"ds_read_b64_tr_b16 a[28:29], v120 offset:10496\n"
"v_min_u32 v4, 27, v0\n"
"v_mfma_f32_16x16x32_bf16 v[244:247], a[12:15], a[116:119], v[244:247]\n"
"s_waitcnt vmcnt(18) lgkmcnt(3)\n"
"ds_read_b64_tr_b16 a[30:31], v120 offset:15616\n"
"v_add_u32 v4, s70, v4\n"
"v_mfma_f32_16x16x32_bf16 v[208:211], a[16:19], a[104:107], v[208:211]\n"
"s_barrier\n"
"ds_read_b64_tr_b16 a[0:1], v119 offset:49152\n"
"v_min_u32 v4, 107, v4\n"
"v_mfma_f32_16x16x32_bf16 v[208:211], a[20:23], a[108:111], v[208:211]\n"
"s_lshl_b32 s70, s67, 10\n"
"ds_read_b64_tr_b16 a[2:3], v119 offset:54272\n"
"v_mfma_f32_16x16x32_bf16 v[248:251], a[16:19], a[112:115], v[248:251]\n"
"s_add_u32 s70, s70, 20480\n"
"ds_read_b64_tr_b16 a[4:5], v119 offset:59392\n"
"v_lshlrev_b32 v4, 3, v4\n"
"v_mfma_f32_16x16x32_bf16 v[248:251], a[20:23], a[116:119], v[248:251]\n"
"s_waitcnt lgkmcnt(3)\n"
"ds_read_b64_tr_b16 a[6:7], v119 offset:64512\n"
"v_add_u32 v4, s70, v4\n"
"v_mfma_f32_16x16x32_bf16 v[212:215], a[24:27], a[104:107], v[212:215]\n"
"s_lshl_b32 s69, s65, 2\n"
"ds_read_b64_tr_b16 a[8:9], v120 offset:49152\n"
"v_mfma_f32_16x16x32_bf16 v[212:215], a[28:31], a[108:111], v[212:215]\n"
"s_add_u32 s69, s69, s68\n"
"ds_read_b64_tr_b16 a[10:11], v120 offset:54272\n"
"v_mfma_f32_16x16x32_bf16 v[252:255], a[24:27], a[112:115], v[252:255]\n"
"s_mul_i32 s70, s69, 163840\n"
"ds_read_b64_tr_b16 a[12:13], v120 offset:59392\n"
"v_mfma_f32_16x16x32_bf16 v[252:255], a[28:31], a[116:119], v[252:255]\n"
"s_waitcnt lgkmcnt(3)\n"
"ds_read_b64_tr_b16 a[14:15], v120 offset:64512\n"
"v_mfma_f32_16x16x32_bf16 v[176:179], a[0:3], a[120:123], v[176:179]\n"
"s_add_u32 m0, s63, 32768\n"
"ds_read_b64_tr_b16 a[16:17], v119 offset:49216\n"
"v_mfma_f32_16x16x32_bf16 v[176:179], a[4:7], a[124:127], v[176:179]\n"
"s_add_u32 s70, s70, 67108864\n"
"ds_read_b64_tr_b16 a[18:19], v119 offset:54336\n"
"v_mfma_f32_16x16x32_bf16 v[216:219], a[0:3], a[128:131], v[216:219]\n"
"s_mul_i32 s71, s64, 10240\n"
"ds_read_b64_tr_b16 a[20:21], v119 offset:59456\n"
"buffer_load_dwordx4 v114, s[96:99], s62 offen offset:0 lds\n"
"v_mfma_f32_16x16x32_bf16 v[216:219], a[4:7], a[132:135], v[216:219]\n"
"s_waitcnt lgkmcnt(3)\n"
"ds_read_b64_tr_b16 a[22:23], v119 offset:64576\n"
"v_mfma_f32_16x16x32_bf16 v[180:183], a[8:11], a[120:123], v[180:183]\n"
"s_add_u32 s71, s71, s70\n"
"ds_read_b64_tr_b16 a[24:25], v120 offset:49216\n"
"v_mfma_f32_16x16x32_bf16 v[180:183], a[12:15], a[124:127], v[180:183]\n"
"s_lshl_b32 s69, s69, 7\n"
"ds_read_b64_tr_b16 a[26:27], v120 offset:54336\n"
"buffer_load_dwordx4 v115, s[96:99], s62 offen offset:1024 lds\n"
"v_mfma_f32_16x16x32_bf16 v[220:223], a[8:11], a[128:131], v[220:223]\n"
"s_add_u32 s69, s69, 28672\n"
"ds_read_b64_tr_b16 a[28:29], v120 offset:59456\n"
"v_min_u32 v9, 2, v0\n"
"v_mfma_f32_16x16x32_bf16 v[220:223], a[12:15], a[132:135], v[220:223]\n"
"s_waitcnt lgkmcnt(3)\n"
"ds_read_b64_tr_b16 a[30:31], v120 offset:64576\n"
"v_add_u32 v9, s64, v9\n"
"v_mfma_f32_16x16x32_bf16 v[184:187], a[16:19], a[120:123], v[184:187]\n"
"ds_read_b64_tr_b16 a[0:1], v119 offset:49280\n"
"buffer_load_dwordx4 v116, s[96:99], s62 offen offset:2048 lds\n"
"v_mfma_f32_16x16x32_bf16 v[184:187], a[20:23], a[124:127], v[184:187]\n"
"ds_read_b64_tr_b16 a[2:3], v119 offset:54400\n"
"v_add_u32 v9, 1, v9\n"
"v_mfma_f32_16x16x32_bf16 v[224:227], a[16:19], a[128:131], v[224:227]\n"
"ds_read_b64_tr_b16 a[4:5], v119 offset:59520\n"
"v_and_b32 v9, 3, v9\n"
"v_mfma_f32_16x16x32_bf16 v[224:227], a[20:23], a[132:135], v[224:227]\n"
"s_waitcnt lgkmcnt(3)\n"
"ds_read_b64_tr_b16 a[6:7], v119 offset:64640\n"
"v_lshlrev_b32 v9, 5, v9\n"
"v_mfma_f32_16x16x32_bf16 v[188:191], a[24:27], a[120:123], v[188:191]\n"
"ds_read_b64_tr_b16 a[8:9], v120 offset:49280\n"
"v_add_u32 v9, s69, v9\n"
"v_mfma_f32_16x16x32_bf16 v[188:191], a[28:31], a[124:127], v[188:191]\n"
"s_and_b32 s65, s64, 2\n"
"ds_read_b64_tr_b16 a[10:11], v120 offset:54400\n"
"v_mfma_f32_16x16x32_bf16 v[228:231], a[24:27], a[128:131], v[228:231]\n"
"s_lshr_b32 s68, s30, 1\n"
"ds_read_b64_tr_b16 a[12:13], v120 offset:59520\n"
"buffer_load_dwordx4 v117, s[96:99], s62 offen offset:3072 lds\n"
"v_mfma_f32_16x16x32_bf16 v[228:231], a[28:31], a[132:135], v[228:231]\n"
"s_waitcnt lgkmcnt(3)\n"
"ds_read_b64_tr_b16 a[14:15], v120 offset:64640\n"
"v_mfma_f32_16x16x32_bf16 v[192:195], a[0:3], a[120:123], v[192:195]\n"
"s_add_u32 m0, s63, 36864\n"
"ds_read_b64_tr_b16 a[16:17], v119 offset:49344\n"
"v_mfma_f32_16x16x32_bf16 v[192:195], a[4:7], a[124:127], v[192:195]\n"
"s_add_u32 s65, s65, s68\n"
"ds_read_b64_tr_b16 a[18:19], v119 offset:54464\n"
"buffer_load_dwordx4 v118, s[96:99], s62 offen offset:0 lds\n"
"v_mfma_f32_16x16x32_bf16 v[232:235], a[0:3], a[128:131], v[232:235]\n"
"s_add_u32 s62, s62, 81920\n"
"ds_read_b64_tr_b16 a[20:21], v119 offset:59584\n"
"v_mfma_f32_16x16x32_bf16 v[232:235], a[4:7], a[132:135], v[232:235]\n"
"s_waitcnt lgkmcnt(3)\n"
"ds_read_b64_tr_b16 a[22:23], v119 offset:64704\n"
"v_mfma_f32_16x16x32_bf16 v[196:199], a[8:11], a[120:123], v[196:199]\n"
"s_lshl_b32 s65, s65, 3\n"
"ds_read_b64_tr_b16 a[24:25], v120 offset:49344\n"
"v_add_u32 v9, s65, v9\n"
"v_mfma_f32_16x16x32_bf16 v[196:199], a[12:15], a[124:127], v[196:199]\n"
"s_lshl_b32 s65, s64, 2\n"
"ds_read_b64_tr_b16 a[26:27], v120 offset:54464\n"
"v_mfma_f32_16x16x32_bf16 v[236:239], a[8:11], a[128:131], v[236:239]\n"
"s_add_u32 s65, s65, s30\n"
"ds_read_b64_tr_b16 a[28:29], v120 offset:59584\n"
"v_mfma_f32_16x16x32_bf16 v[236:239], a[12:15], a[132:135], v[236:239]\n"
"s_waitcnt lgkmcnt(3)\n"
"ds_read_b64_tr_b16 a[30:31], v120 offset:64704\n"
"v_mfma_f32_16x16x32_bf16 v[200:203], a[16:19], a[120:123], v[200:203]\n"
"s_lshl_b32 s65, s65, 3\n"
"ds_read_b64_tr_b16 a[0:1], v119 offset:49408\n"
"v_mfma_f32_16x16x32_bf16 v[200:203], a[20:23], a[124:127], v[200:203]\n"
"s_add_u32 s65, s65, s69\n"
"ds_read_b64_tr_b16 a[2:3], v119 offset:54528\n"
"v_mov_b32 v12, s65\n"
"v_mfma_f32_16x16x32_bf16 v[240:243], a[16:19], a[128:131], v[240:243]\n"
"ds_read_b64_tr_b16 a[4:5], v119 offset:59648\n"
"v_mov_b32 v14, s34\n"
"v_mfma_f32_16x16x32_bf16 v[240:243], a[20:23], a[132:135], v[240:243]\n"
"s_waitcnt lgkmcnt(3)\n"
"ds_read_b64_tr_b16 a[6:7], v119 offset:64768\n"
"v_mov_b32 v15, s35\n"
"v_mfma_f32_16x16x32_bf16 v[204:207], a[24:27], a[120:123], v[204:207]\n"
"s_add_u32 s52, s28, s71\n"
"s_addc_u32 s53, s29, 0\n"
"ds_read_b64_tr_b16 a[8:9], v120 offset:49408\n"
"v_mfma_f32_16x16x32_bf16 v[204:207], a[28:31], a[124:127], v[204:207]\n"
"s_mov_b32 s54, 163840\n"
"ds_read_b64_tr_b16 a[10:11], v120 offset:54528\n"
"v_mfma_f32_16x16x32_bf16 v[244:247], a[24:27], a[128:131], v[244:247]\n"
"s_mov_b32 s55, 0x20000\n"
"ds_read_b64_tr_b16 a[12:13], v120 offset:59648\n"
"v_mfma_f32_16x16x32_bf16 v[244:247], a[28:31], a[132:135], v[244:247]\n"
"s_waitcnt vmcnt(14) lgkmcnt(3)\n"
"ds_read_b64_tr_b16 a[14:15], v120 offset:64768\n"
"v_mfma_f32_16x16x32_bf16 v[208:211], a[0:3], a[120:123], v[208:211]\n"
"s_barrier\n"
"ds_read_b64_tr_b16 a[16:17], v122 offset:8192\n"
"v_mfma_f32_16x16x32_bf16 v[208:211], a[4:7], a[124:127], v[208:211]\n"
"s_and_b32 s93, s30, 1\n"
"ds_read_b64_tr_b16 a[18:19], v122 offset:13312\n"
"v_mfma_f32_16x16x32_bf16 v[248:251], a[0:3], a[128:131], v[248:251]\n"
"s_mul_i32 s94, s93, 5120\n"
"ds_read_b64_tr_b16 a[20:21], v122 offset:18432\n"
"v_mfma_f32_16x16x32_bf16 v[248:251], a[4:7], a[132:135], v[248:251]\n"
"s_waitcnt lgkmcnt(3)\n"
"ds_read_b64_tr_b16 a[22:23], v122 offset:23552\n"
"v_mfma_f32_16x16x32_bf16 v[212:215], a[8:11], a[120:123], v[212:215]\n"
"s_mul_i32 s93, s93, 10240\n"
"ds_read_b64_tr_b16 a[24:25], v123 offset:8192\n"
"v_add_u32 v158, s94, v175\n"
"v_mfma_f32_16x16x32_bf16 v[212:215], a[12:15], a[124:127], v[212:215]\n"
"ds_read_b64_tr_b16 a[26:27], v123 offset:13312\n"
"v_add_u32 v159, 2560, v158\n"
"v_mfma_f32_16x16x32_bf16 v[252:255], a[8:11], a[128:131], v[252:255]\n"
"ds_read_b64_tr_b16 a[28:29], v123 offset:18432\n"
"v_lshlrev_b32 v172, 3, v0\n"
"v_mfma_f32_16x16x32_bf16 v[252:255], a[12:15], a[132:135], v[252:255]\n"
"s_waitcnt lgkmcnt(3)\n"
"ds_read_b64_tr_b16 a[30:31], v123 offset:23552\n"
"v_add_u32 v172, s94, v172\n"
"global_load_dwordx2 v[6:7], v4, s[6:7] sc1\n"
"v_mfma_f32_16x16x32_bf16 v[176:179], a[16:19], a[136:139], v[176:179]\n"
"s_add_u32 m0, s63, 53248\n"
"ds_read_b64_tr_b16 a[0:1], v122 offset:8256\n"
"v_add_u32 v173, 2560, v172\n"
"v_mfma_f32_16x16x32_bf16 v[176:179], a[20:23], a[140:143], v[176:179]\n"
"ds_read_b64_tr_b16 a[2:3], v122 offset:13376\n"
"v_add_u32 v174, s93, v175\n"
"v_mfma_f32_16x16x32_bf16 v[216:219], a[16:19], a[144:147], v[216:219]\n"
"ds_read_b64_tr_b16 a[4:5], v122 offset:18496\n"
"buffer_load_dwordx4 v114, s[96:99], s62 offen offset:0 lds\n"
"v_mfma_f32_16x16x32_bf16 v[216:219], a[20:23], a[148:151], v[216:219]\n"
"s_waitcnt lgkmcnt(3)\n"
"ds_read_b64_tr_b16 a[6:7], v122 offset:23616\n"
"v_add_u32 v174, 98304, v174\n"
"v_mfma_f32_16x16x32_bf16 v[180:183], a[24:27], a[136:139], v[180:183]\n"
"s_mul_i32 s71, s64, 40960\n"
"ds_read_b64_tr_b16 a[8:9], v123 offset:8256\n"
"v_mfma_f32_16x16x32_bf16 v[180:183], a[28:31], a[140:143], v[180:183]\n"
"s_add_u32 s70, s70, s71\n"
"ds_read_b64_tr_b16 a[10:11], v123 offset:13376\n"
"buffer_load_dwordx4 v115, s[96:99], s62 offen offset:1024 lds\n"
"v_mfma_f32_16x16x32_bf16 v[220:223], a[24:27], a[144:147], v[220:223]\n"
"s_mul_i32 s71, s30, 2560\n"
"ds_read_b64_tr_b16 a[12:13], v123 offset:18496\n"
"v_mfma_f32_16x16x32_bf16 v[220:223], a[28:31], a[148:151], v[220:223]\n"
"s_waitcnt lgkmcnt(3)\n"
"ds_read_b64_tr_b16 a[14:15], v123 offset:23616\n"
"v_mfma_f32_16x16x32_bf16 v[184:187], a[0:3], a[136:139], v[184:187]\n"
"s_add_u32 s70, s70, s71\n"
"ds_read_b64_tr_b16 a[16:17], v122 offset:8320\n"
"buffer_load_dwordx4 v116, s[96:99], s62 offen offset:2048 lds\n"
"v_mfma_f32_16x16x32_bf16 v[184:187], a[4:7], a[140:143], v[184:187]\n"
"s_lshl_b32 s71, s66, 2\n"
"ds_read_b64_tr_b16 a[18:19], v122 offset:13440\n"
"v_mfma_f32_16x16x32_bf16 v[224:227], a[0:3], a[144:147], v[224:227]\n"
"s_add_u32 s71, s71, s30\n"
"ds_read_b64_tr_b16 a[20:21], v122 offset:18560\n"
"v_mfma_f32_16x16x32_bf16 v[224:227], a[4:7], a[148:151], v[224:227]\n"
"s_waitcnt lgkmcnt(3)\n"
"ds_read_b64_tr_b16 a[22:23], v122 offset:23680\n"
"v_mfma_f32_16x16x32_bf16 v[188:191], a[8:11], a[136:139], v[188:191]\n"
"s_lshl_b32 s69, s71, 5\n"
"ds_read_b64_tr_b16 a[24:25], v123 offset:8320\n"
"v_mfma_f32_16x16x32_bf16 v[188:191], a[12:15], a[140:143], v[188:191]\n"
"s_mul_i32 s71, s71, 20480\n"
"ds_read_b64_tr_b16 a[26:27], v123 offset:13440\n"
"v_mfma_f32_16x16x32_bf16 v[228:231], a[8:11], a[144:147], v[228:231]\n"
"s_mul_i32 s65, s67, 160\n"
"ds_read_b64_tr_b16 a[28:29], v123 offset:18560\n"
"buffer_load_dwordx4 v117, s[96:99], s62 offen offset:3072 lds\n"
"v_mfma_f32_16x16x32_bf16 v[228:231], a[12:15], a[148:151], v[228:231]\n"
"s_waitcnt lgkmcnt(3)\n"
"ds_read_b64_tr_b16 a[30:31], v123 offset:23680\n"
"v_mfma_f32_16x16x32_bf16 v[192:195], a[16:19], a[136:139], v[192:195]\n"
"s_add_u32 m0, s63, 57344\n"
"ds_read_b64_tr_b16 a[0:1], v122 offset:8384\n"
"v_mfma_f32_16x16x32_bf16 v[192:195], a[20:23], a[140:143], v[192:195]\n"
"s_add_u32 s65, s65, s71\n"
"ds_read_b64_tr_b16 a[2:3], v122 offset:13504\n"
"buffer_load_dwordx4 v118, s[96:99], s62 offen offset:0 lds\n"
"v_mfma_f32_16x16x32_bf16 v[232:235], a[16:19], a[144:147], v[232:235]\n"
"s_add_u32 s62, s62, 81920\n"
"ds_read_b64_tr_b16 a[4:5], v122 offset:18624\n"
"v_mfma_f32_16x16x32_bf16 v[232:235], a[20:23], a[148:151], v[232:235]\n"
"s_waitcnt lgkmcnt(3)\n"
"ds_read_b64_tr_b16 a[6:7], v122 offset:23744\n"
"v_mfma_f32_16x16x32_bf16 v[196:199], a[24:27], a[136:139], v[196:199]\n"
"s_mov_b32 s68, s69\n"
"ds_read_b64_tr_b16 a[8:9], v123 offset:8384\n"
"v_mfma_f32_16x16x32_bf16 v[196:199], a[28:31], a[140:143], v[196:199]\n"
"s_add_u32 s36, s28, s70\n"
"s_addc_u32 s37, s29, 0\n"
"ds_read_b64_tr_b16 a[10:11], v123 offset:13504\n"
"v_mfma_f32_16x16x32_bf16 v[236:239], a[24:27], a[144:147], v[236:239]\n"
"s_mov_b32 s38, 40960\n"
"ds_read_b64_tr_b16 a[12:13], v123 offset:18624\n"
"v_mfma_f32_16x16x32_bf16 v[236:239], a[28:31], a[148:151], v[236:239]\n"
"s_waitcnt lgkmcnt(3)\n"
"ds_read_b64_tr_b16 a[14:15], v123 offset:23744\n"
"v_mfma_f32_16x16x32_bf16 v[200:203], a[0:3], a[136:139], v[200:203]\n"
"s_mov_b32 s39, 0x20000\n"
"ds_read_b64_tr_b16 a[16:17], v122 offset:8448\n"
"v_mfma_f32_16x16x32_bf16 v[200:203], a[4:7], a[140:143], v[200:203]\n"
"s_add_u32 s40, s8, s65\n"
"s_addc_u32 s41, s9, 0\n"
"ds_read_b64_tr_b16 a[18:19], v122 offset:13568\n"
"v_mfma_f32_16x16x32_bf16 v[240:243], a[0:3], a[144:147], v[240:243]\n"
"s_mov_b32 s42, 20480\n"
"ds_read_b64_tr_b16 a[20:21], v122 offset:18688\n"
"v_mfma_f32_16x16x32_bf16 v[240:243], a[4:7], a[148:151], v[240:243]\n"
"s_waitcnt lgkmcnt(3)\n"
"ds_read_b64_tr_b16 a[22:23], v122 offset:23808\n"
"v_mfma_f32_16x16x32_bf16 v[204:207], a[8:11], a[136:139], v[204:207]\n"
"s_mov_b32 s43, 0x20000\n"
"ds_read_b64_tr_b16 a[24:25], v123 offset:8448\n"
"v_and_b32 v1, 15, v0\n"
"v_mfma_f32_16x16x32_bf16 v[204:207], a[12:15], a[140:143], v[204:207]\n"
"ds_read_b64_tr_b16 a[26:27], v123 offset:13568\n"
"v_lshrrev_b32 v11, 4, v0\n"
"v_mfma_f32_16x16x32_bf16 v[244:247], a[8:11], a[144:147], v[244:247]\n"
"ds_read_b64_tr_b16 a[28:29], v123 offset:18688\n"
"v_lshlrev_b32 v10, 1, v1\n"
"v_mfma_f32_16x16x32_bf16 v[244:247], a[12:15], a[148:151], v[244:247]\n"
"s_waitcnt vmcnt(11) lgkmcnt(3)\n"
"ds_read_b64_tr_b16 a[30:31], v123 offset:23808\n"
"v_mul_u32_u24 v1, 1280, v1\n"
"v_mfma_f32_16x16x32_bf16 v[208:211], a[16:19], a[136:139], v[208:211]\n"
"s_barrier\n"
"ds_read_b64_tr_b16 a[0:1], v112 offset:0\n"
"v_lshl_add_u32 v11, v11, 3, v1\n"
"v_mfma_f32_16x16x32_bf16 v[208:211], a[20:23], a[140:143], v[208:211]\n"
"ds_read_b64_tr_b16 a[2:3], v112 offset:5120\n"
"v_lshlrev_b32 v13, 3, v0\n"
"v_mfma_f32_16x16x32_bf16 v[248:251], a[16:19], a[144:147], v[248:251]\n"
"s_add_u32 s92, s64, 1\n"
"ds_read_b64_tr_b16 a[4:5], v112 offset:10240\n"
"v_mfma_f32_16x16x32_bf16 v[248:251], a[20:23], a[148:151], v[248:251]\n"
"s_waitcnt lgkmcnt(3)\n"
"ds_read_b64_tr_b16 a[6:7], v112 offset:15360\n"
"v_mfma_f32_16x16x32_bf16 v[212:215], a[24:27], a[136:139], v[212:215]\n"
"s_and_b32 s92, s92, 3\n"
"ds_read_b64_tr_b16 a[8:9], v113 offset:0\n"
"v_mfma_f32_16x16x32_bf16 v[212:215], a[28:31], a[140:143], v[212:215]\n"
"s_mul_i32 s92, s92, 10240\n"
"ds_read_b64_tr_b16 a[10:11], v113 offset:5120\n"
"v_mfma_f32_16x16x32_bf16 v[252:255], a[24:27], a[144:147], v[252:255]\n"
"s_add_u32 s93, s64, 2\n"
"ds_read_b64_tr_b16 a[12:13], v113 offset:10240\n"
"v_mfma_f32_16x16x32_bf16 v[252:255], a[28:31], a[148:151], v[252:255]\n"
"s_waitcnt lgkmcnt(3)\n"
"ds_read_b64_tr_b16 a[14:15], v113 offset:15360\n"
"v_mfma_f32_16x16x32_bf16 v[176:179], a[0:3], a[152:155], v[176:179]\n"
"s_add_u32 m0, s63, 102400\n"
"ds_read_b64_tr_b16 a[16:17], v112 offset:64\n"
"v_mfma_f32_16x16x32_bf16 v[176:179], a[4:7], a[156:159], v[176:179]\n"
"s_and_b32 s93, s93, 3\n"
"ds_read_b64_tr_b16 a[18:19], v112 offset:5184\n"
"v_mfma_f32_16x16x32_bf16 v[216:219], a[0:3], a[160:163], v[216:219]\n"
"s_mul_i32 s93, s93, 10240\n"
"ds_read_b64_tr_b16 a[20:21], v112 offset:10304\n"
"buffer_load_dwordx4 v114, s[96:99], s62 offen offset:0 lds\n"
"v_mfma_f32_16x16x32_bf16 v[216:219], a[4:7], a[164:167], v[216:219]\n"
"s_waitcnt lgkmcnt(3)\n"
"ds_read_b64_tr_b16 a[22:23], v112 offset:15424\n"
"v_mfma_f32_16x16x32_bf16 v[180:183], a[8:11], a[152:155], v[180:183]\n"
"s_add_u32 s94, s64, 3\n"
"ds_read_b64_tr_b16 a[24:25], v113 offset:64\n"
"v_mfma_f32_16x16x32_bf16 v[180:183], a[12:15], a[156:159], v[180:183]\n"
"s_and_b32 s94, s94, 3\n"
"ds_read_b64_tr_b16 a[26:27], v113 offset:5184\n"
"buffer_load_dwordx4 v115, s[96:99], s62 offen offset:1024 lds\n"
"v_mfma_f32_16x16x32_bf16 v[220:223], a[8:11], a[160:163], v[220:223]\n"
"s_mul_i32 s94, s94, 10240\n"
"ds_read_b64_tr_b16 a[28:29], v113 offset:10304\n"
"v_mfma_f32_16x16x32_bf16 v[220:223], a[12:15], a[164:167], v[220:223]\n"
"s_waitcnt lgkmcnt(3)\n"
"ds_read_b64_tr_b16 a[30:31], v113 offset:15424\n"
"v_mfma_f32_16x16x32_bf16 v[184:187], a[16:19], a[152:155], v[184:187]\n"
"ds_read_b64_tr_b16 a[0:1], v112 offset:128\n"
"buffer_load_dwordx4 v116, s[96:99], s62 offen offset:2048 lds\n"
"v_mfma_f32_16x16x32_bf16 v[184:187], a[20:23], a[156:159], v[184:187]\n"
"ds_read_b64_tr_b16 a[2:3], v112 offset:5248\n"
"v_mfma_f32_16x16x32_bf16 v[224:227], a[16:19], a[160:163], v[224:227]\n"
"ds_read_b64_tr_b16 a[4:5], v112 offset:10368\n"
"v_mfma_f32_16x16x32_bf16 v[224:227], a[20:23], a[164:167], v[224:227]\n"
"s_waitcnt lgkmcnt(3)\n"
"ds_read_b64_tr_b16 a[6:7], v112 offset:15488\n"
"v_mfma_f32_16x16x32_bf16 v[188:191], a[24:27], a[152:155], v[188:191]\n"
"ds_read_b64_tr_b16 a[8:9], v113 offset:128\n"
"v_mfma_f32_16x16x32_bf16 v[188:191], a[28:31], a[156:159], v[188:191]\n"
"ds_read_b64_tr_b16 a[10:11], v113 offset:5248\n"
"v_mfma_f32_16x16x32_bf16 v[228:231], a[24:27], a[160:163], v[228:231]\n"
"ds_read_b64_tr_b16 a[12:13], v113 offset:10368\n"
"buffer_load_dwordx4 v117, s[96:99], s62 offen offset:3072 lds\n"
"v_mfma_f32_16x16x32_bf16 v[228:231], a[28:31], a[164:167], v[228:231]\n"
"s_waitcnt lgkmcnt(3)\n"
"ds_read_b64_tr_b16 a[14:15], v113 offset:15488\n"
"v_mfma_f32_16x16x32_bf16 v[192:195], a[0:3], a[152:155], v[192:195]\n"
"s_add_u32 m0, s63, 106496\n"
"ds_read_b64_tr_b16 a[16:17], v112 offset:192\n"
"v_mfma_f32_16x16x32_bf16 v[192:195], a[4:7], a[156:159], v[192:195]\n"
"ds_read_b64_tr_b16 a[18:19], v112 offset:5312\n"
"buffer_load_dwordx4 v118, s[96:99], s62 offen offset:0 lds\n"
"v_mfma_f32_16x16x32_bf16 v[232:235], a[0:3], a[160:163], v[232:235]\n"
"s_add_u32 s62, s62, 81920\n"
"ds_read_b64_tr_b16 a[20:21], v112 offset:10432\n"
"v_mfma_f32_16x16x32_bf16 v[232:235], a[4:7], a[164:167], v[232:235]\n"
"s_waitcnt lgkmcnt(3)\n"
"ds_read_b64_tr_b16 a[22:23], v112 offset:15552\n"
"v_mfma_f32_16x16x32_bf16 v[196:199], a[8:11], a[152:155], v[196:199]\n"
"ds_read_b64_tr_b16 a[24:25], v113 offset:192\n"
"v_mfma_f32_16x16x32_bf16 v[196:199], a[12:15], a[156:159], v[196:199]\n"
"ds_read_b64_tr_b16 a[26:27], v113 offset:5312\n"
"v_mfma_f32_16x16x32_bf16 v[236:239], a[8:11], a[160:163], v[236:239]\n"
"ds_read_b64_tr_b16 a[28:29], v113 offset:10432\n"
"v_mfma_f32_16x16x32_bf16 v[236:239], a[12:15], a[164:167], v[236:239]\n"
"s_waitcnt lgkmcnt(3)\n"
"ds_read_b64_tr_b16 a[30:31], v113 offset:15552\n"
"v_mfma_f32_16x16x32_bf16 v[200:203], a[16:19], a[152:155], v[200:203]\n"
"ds_read_b64_tr_b16 a[0:1], v112 offset:256\n"
"v_mfma_f32_16x16x32_bf16 v[200:203], a[20:23], a[156:159], v[200:203]\n"
"ds_read_b64_tr_b16 a[2:3], v112 offset:5376\n"
"v_mfma_f32_16x16x32_bf16 v[240:243], a[16:19], a[160:163], v[240:243]\n"
"ds_read_b64_tr_b16 a[4:5], v112 offset:10496\n"
"v_mfma_f32_16x16x32_bf16 v[240:243], a[20:23], a[164:167], v[240:243]\n"
"s_waitcnt lgkmcnt(3)\n"
"ds_read_b64_tr_b16 a[6:7], v112 offset:15616\n"
"v_mfma_f32_16x16x32_bf16 v[204:207], a[24:27], a[152:155], v[204:207]\n"
"ds_read_b64_tr_b16 a[8:9], v113 offset:256\n"
"v_mfma_f32_16x16x32_bf16 v[204:207], a[28:31], a[156:159], v[204:207]\n"
"ds_read_b64_tr_b16 a[10:11], v113 offset:5376\n"
"v_mfma_f32_16x16x32_bf16 v[244:247], a[24:27], a[160:163], v[244:247]\n"
"ds_read_b64_tr_b16 a[12:13], v113 offset:10496\n"
"v_mfma_f32_16x16x32_bf16 v[244:247], a[28:31], a[164:167], v[244:247]\n"
"s_waitcnt vmcnt(11) lgkmcnt(3)\n"
"s_waitcnt vmcnt(10)\n"
"v_cmp_ne_u64 vcc, s[34:35], v[6:7]\n"
"s_nop 0\n"
"s_cmp_eq_u64 vcc, 0\n"
"s_cbranch_scc0 L_slow_pollU_%=\n"
"L_back_pollU_%=:\n"
"ds_read_b64_tr_b16 a[14:15], v113 offset:15616\n"
"v_mfma_f32_16x16x32_bf16 v[208:211], a[0:3], a[152:155], v[208:211]\n"
"s_barrier\n"
"ds_read_b64_tr_b16 a[16:17], v112 offset:32768\n"
"v_mfma_f32_16x16x32_bf16 v[208:211], a[4:7], a[156:159], v[208:211]\n"
"ds_read_b64_tr_b16 a[18:19], v112 offset:37888\n"
"v_mfma_f32_16x16x32_bf16 v[248:251], a[0:3], a[160:163], v[248:251]\n"
"ds_read_b64_tr_b16 a[20:21], v112 offset:43008\n"
"v_mfma_f32_16x16x32_bf16 v[248:251], a[4:7], a[164:167], v[248:251]\n"
"s_waitcnt lgkmcnt(3)\n"
"ds_read_b64_tr_b16 a[22:23], v112 offset:48128\n"
"v_mfma_f32_16x16x32_bf16 v[212:215], a[8:11], a[152:155], v[212:215]\n"
"ds_read_b64_tr_b16 a[24:25], v113 offset:32768\n"
"v_mfma_f32_16x16x32_bf16 v[212:215], a[12:15], a[156:159], v[212:215]\n"
"ds_read_b64_tr_b16 a[26:27], v113 offset:37888\n"
"v_mfma_f32_16x16x32_bf16 v[252:255], a[8:11], a[160:163], v[252:255]\n"
"ds_read_b64_tr_b16 a[28:29], v113 offset:43008\n"
"v_mfma_f32_16x16x32_bf16 v[252:255], a[12:15], a[164:167], v[252:255]\n"
"s_waitcnt lgkmcnt(3)\n"
"ds_read_b64_tr_b16 a[30:31], v113 offset:48128\n"
"v_mfma_f32_16x16x32_bf16 v[176:179], a[16:19], a[40:43], v[176:179]\n"
"s_add_u32 m0, s61, 131072\n"
"ds_read_b64_tr_b16 a[0:1], v112 offset:32832\n"
"buffer_load_dwordx4 v124, s[48:51], s60 offen offset:0 lds\n"
"v_mfma_f32_16x16x32_bf16 v[176:179], a[20:23], a[44:47], v[176:179]\n"
"ds_read_b64_tr_b16 a[2:3], v112 offset:37952\n"
"v_mfma_f32_16x16x32_bf16 v[216:219], a[16:19], a[48:51], v[216:219]\n"
"ds_read_b64_tr_b16 a[4:5], v112 offset:43072\n"
"buffer_load_dwordx4 v125, s[48:51], s60 offen offset:1024 lds\n"
"v_mfma_f32_16x16x32_bf16 v[216:219], a[20:23], a[52:55], v[216:219]\n"
"s_waitcnt lgkmcnt(3)\n"
"ds_read_b64_tr_b16 a[6:7], v112 offset:48192\n"
"v_mfma_f32_16x16x32_bf16 v[180:183], a[24:27], a[40:43], v[180:183]\n"
"ds_read_b64_tr_b16 a[8:9], v113 offset:32832\n"
"buffer_load_dwordx4 v126, s[48:51], s60 offen offset:2048 lds\n"
"v_mfma_f32_16x16x32_bf16 v[180:183], a[28:31], a[44:47], v[180:183]\n"
"ds_read_b64_tr_b16 a[10:11], v113 offset:37952\n"
"buffer_load_dwordx4 v127, s[48:51], s60 offen offset:3072 lds\n"
"v_mfma_f32_16x16x32_bf16 v[220:223], a[24:27], a[48:51], v[220:223]\n"
"s_add_u32 s60, s60, 131072\n"
"ds_read_b64_tr_b16 a[12:13], v113 offset:43072\n"
"buffer_load_dwordx4 a[128:131], v175, s[44:47], s58 offen offset:0 sc1\n"
"v_mfma_f32_16x16x32_bf16 v[220:223], a[28:31], a[52:55], v[220:223]\n"
"s_waitcnt lgkmcnt(3)\n"
"ds_read_b64_tr_b16 a[14:15], v113 offset:48192\n"
"v_mfma_f32_16x16x32_bf16 v[184:187], a[0:3], a[40:43], v[184:187]\n"
"ds_read_b64_tr_b16 a[16:17], v112 offset:32896\n"
"buffer_load_dwordx4 a[132:135], v175, s[44:47], s58 offen offset:1024 sc1\n"
"v_mfma_f32_16x16x32_bf16 v[184:187], a[4:7], a[44:47], v[184:187]\n"
"ds_read_b64_tr_b16 a[18:19], v112 offset:38016\n"
"v_mfma_f32_16x16x32_bf16 v[224:227], a[0:3], a[48:51], v[224:227]\n"
"ds_read_b64_tr_b16 a[20:21], v112 offset:43136\n"
"buffer_load_dwordx4 a[136:139], v175, s[44:47], s58 offen offset:2048 sc1\n"
"v_mfma_f32_16x16x32_bf16 v[224:227], a[4:7], a[52:55], v[224:227]\n"
"s_waitcnt lgkmcnt(3)\n"
"ds_read_b64_tr_b16 a[22:23], v112 offset:48256\n"
"v_mfma_f32_16x16x32_bf16 v[188:191], a[8:11], a[40:43], v[188:191]\n"
"ds_read_b64_tr_b16 a[24:25], v113 offset:32896\n"
"buffer_load_dwordx4 a[140:143], v175, s[44:47], s58 offen offset:3072 sc1\n"
"v_mfma_f32_16x16x32_bf16 v[188:191], a[12:15], a[44:47], v[188:191]\n"
"s_add_u32 s58, s58, 4096\n"
"ds_read_b64_tr_b16 a[26:27], v113 offset:38016\n"
"v_mfma_f32_16x16x32_bf16 v[228:231], a[8:11], a[48:51], v[228:231]\n"
"ds_read_b64_tr_b16 a[28:29], v113 offset:43136\n"
"buffer_load_dwordx4 a[144:147], v175, s[44:47], s58 offen offset:0 sc1\n"
"v_mfma_f32_16x16x32_bf16 v[228:231], a[12:15], a[52:55], v[228:231]\n"
"s_waitcnt lgkmcnt(3)\n"
"ds_read_b64_tr_b16 a[30:31], v113 offset:48256\n"
"v_mfma_f32_16x16x32_bf16 v[192:195], a[16:19], a[40:43], v[192:195]\n"
"s_add_u32 s58, s58, 1024\n"
"ds_read_b64_tr_b16 a[0:1], v112 offset:32960\n"
"v_mfma_f32_16x16x32_bf16 v[192:195], a[20:23], a[44:47], v[192:195]\n"
"s_add_u32 m0, s61, 135168\n"
"ds_read_b64_tr_b16 a[2:3], v112 offset:38080\n"
"buffer_load_dwordx4 v124, s[48:51], s60 offen offset:0 lds\n"
"v_mfma_f32_16x16x32_bf16 v[232:235], a[16:19], a[48:51], v[232:235]\n"
"ds_read_b64_tr_b16 a[4:5], v112 offset:43200\n"
"buffer_load_dwordx4 v125, s[48:51], s60 offen offset:1024 lds\n"
"v_mfma_f32_16x16x32_bf16 v[232:235], a[20:23], a[52:55], v[232:235]\n"
"s_waitcnt lgkmcnt(3)\n"
"ds_read_b64_tr_b16 a[6:7], v112 offset:48320\n"
"v_mfma_f32_16x16x32_bf16 v[196:199], a[24:27], a[40:43], v[196:199]\n"
"ds_read_b64_tr_b16 a[8:9], v113 offset:32960\n"
"buffer_load_dwordx4 v126, s[48:51], s60 offen offset:2048 lds\n"
"v_mfma_f32_16x16x32_bf16 v[196:199], a[28:31], a[44:47], v[196:199]\n"
"ds_read_b64_tr_b16 a[10:11], v113 offset:38080\n"
"v_mfma_f32_16x16x32_bf16 v[236:239], a[24:27], a[48:51], v[236:239]\n"
"ds_read_b64_tr_b16 a[12:13], v113 offset:43200\n"
"buffer_load_dwordx4 v127, s[48:51], s60 offen offset:3072 lds\n"
"v_mfma_f32_16x16x32_bf16 v[236:239], a[28:31], a[52:55], v[236:239]\n"
"s_waitcnt lgkmcnt(3)\n"
"ds_read_b64_tr_b16 a[14:15], v113 offset:48320\n"
"v_mfma_f32_16x16x32_bf16 v[200:203], a[0:3], a[40:43], v[200:203]\n"
"s_add_u32 s60, s60, 131072\n"
"s_cmp_eq_u32 s60, 1835008\n"
"s_cselect_b32 s60, 0, s60\n"
"ds_read_b64_tr_b16 a[16:17], v112 offset:33024\n"
"buffer_load_dwordx4 a[148:151], v175, s[44:47], s58 offen offset:0 sc1\n"
"v_mfma_f32_16x16x32_bf16 v[200:203], a[4:7], a[44:47], v[200:203]\n"
"ds_read_b64_tr_b16 a[18:19], v112 offset:38144\n"
"v_mfma_f32_16x16x32_bf16 v[240:243], a[0:3], a[48:51], v[240:243]\n"
"ds_read_b64_tr_b16 a[20:21], v112 offset:43264\n"
"buffer_load_dwordx4 a[152:155], v175, s[44:47], s58 offen offset:1024 sc1\n"
"v_mfma_f32_16x16x32_bf16 v[240:243], a[4:7], a[52:55], v[240:243]\n"
"s_waitcnt lgkmcnt(3)\n"
"ds_read_b64_tr_b16 a[22:23], v112 offset:48384\n"
"v_mfma_f32_16x16x32_bf16 v[204:207], a[8:11], a[40:43], v[204:207]\n"
"ds_read_b64_tr_b16 a[24:25], v113 offset:33024\n"
"buffer_load_dwordx4 a[156:159], v175, s[44:47], s58 offen offset:2048 sc1\n"
"v_mfma_f32_16x16x32_bf16 v[204:207], a[12:15], a[44:47], v[204:207]\n"
"ds_read_b64_tr_b16 a[26:27], v113 offset:38144\n"
"buffer_load_dwordx4 a[160:163], v175, s[44:47], s58 offen offset:3072 sc1\n"
"v_mfma_f32_16x16x32_bf16 v[244:247], a[8:11], a[48:51], v[244:247]\n"
"s_add_u32 s58, s58, 4096\n"
"ds_read_b64_tr_b16 a[28:29], v113 offset:43264\n"
"buffer_load_dwordx4 a[164:167], v175, s[44:47], s58 offen offset:0 sc1\n"
"v_mfma_f32_16x16x32_bf16 v[244:247], a[12:15], a[52:55], v[244:247]\n"
"s_waitcnt vmcnt(23) lgkmcnt(3)\n"
"ds_read_b64_tr_b16 a[30:31], v113 offset:48384\n"
"v_mfma_f32_16x16x32_bf16 v[208:211], a[16:19], a[40:43], v[208:211]\n"
"s_barrier\n"
"ds_read_b64_tr_b16 a[0:1], v119 offset:0\n"
"v_mfma_f32_16x16x32_bf16 v[208:211], a[20:23], a[44:47], v[208:211]\n"
"s_add_u32 s58, s58, 1024\n"
"s_cmp_eq_u32 s58, 71680\n"
"s_cselect_b32 s58, 0, s58\n"
"ds_read_b64_tr_b16 a[2:3], v119 offset:5120\n"
"v_mfma_f32_16x16x32_bf16 v[248:251], a[16:19], a[48:51], v[248:251]\n"
"ds_read_b64_tr_b16 a[4:5], v119 offset:10240\n"
"v_mfma_f32_16x16x32_bf16 v[248:251], a[20:23], a[52:55], v[248:251]\n"
"s_waitcnt lgkmcnt(3)\n"
"ds_read_b64_tr_b16 a[6:7], v119 offset:15360\n"
"v_mfma_f32_16x16x32_bf16 v[212:215], a[24:27], a[40:43], v[212:215]\n"
"ds_read_b64_tr_b16 a[8:9], v120 offset:0\n"
"v_mfma_f32_16x16x32_bf16 v[212:215], a[28:31], a[44:47], v[212:215]\n"
"ds_read_b64_tr_b16 a[10:11], v120 offset:5120\n"
"v_mfma_f32_16x16x32_bf16 v[252:255], a[24:27], a[48:51], v[252:255]\n"
"ds_read_b64_tr_b16 a[12:13], v120 offset:10240\n"
"v_mfma_f32_16x16x32_bf16 v[252:255], a[28:31], a[52:55], v[252:255]\n"
"s_waitcnt lgkmcnt(3)\n"
"ds_read_b64_tr_b16 a[14:15], v120 offset:15360\n"
"v_mfma_f32_16x16x32_bf16 v[176:179], a[0:3], a[56:59], v[176:179]\n"
"s_add_u32 m0, s61, 0\n"
"ds_read_b64_tr_b16 a[16:17], v119 offset:64\n"
"buffer_load_dwordx4 v124, s[48:51], s60 offen offset:0 lds\n"
"v_mfma_f32_16x16x32_bf16 v[176:179], a[4:7], a[60:63], v[176:179]\n"
"ds_read_b64_tr_b16 a[18:19], v119 offset:5184\n"
"v_mfma_f32_16x16x32_bf16 v[216:219], a[0:3], a[64:67], v[216:219]\n"
"ds_read_b64_tr_b16 a[20:21], v119 offset:10304\n"
"buffer_load_dwordx4 v125, s[48:51], s60 offen offset:1024 lds\n"
"v_mfma_f32_16x16x32_bf16 v[216:219], a[4:7], a[68:71], v[216:219]\n"
"s_waitcnt lgkmcnt(3)\n"
"ds_read_b64_tr_b16 a[22:23], v119 offset:15424\n"
"v_mfma_f32_16x16x32_bf16 v[180:183], a[8:11], a[56:59], v[180:183]\n"
"ds_read_b64_tr_b16 a[24:25], v120 offset:64\n"
"buffer_load_dwordx4 v126, s[48:51], s60 offen offset:2048 lds\n"
"v_mfma_f32_16x16x32_bf16 v[180:183], a[12:15], a[60:63], v[180:183]\n"
"ds_read_b64_tr_b16 a[26:27], v120 offset:5184\n"
"buffer_load_dwordx4 v127, s[48:51], s60 offen offset:3072 lds\n"
"v_mfma_f32_16x16x32_bf16 v[220:223], a[8:11], a[64:67], v[220:223]\n"
"s_add_u32 s60, s60, 131072\n"
"ds_read_b64_tr_b16 a[28:29], v120 offset:10304\n"
"buffer_load_dwordx4 a[168:171], v175, s[44:47], s58 offen offset:0 sc1\n"
"v_mfma_f32_16x16x32_bf16 v[220:223], a[12:15], a[68:71], v[220:223]\n"
"s_waitcnt lgkmcnt(3)\n"
"ds_read_b64_tr_b16 a[30:31], v120 offset:15424\n"
"v_mfma_f32_16x16x32_bf16 v[184:187], a[16:19], a[56:59], v[184:187]\n"
"ds_read_b64_tr_b16 a[0:1], v119 offset:128\n"
"buffer_load_dwordx4 a[172:175], v175, s[44:47], s58 offen offset:1024 sc1\n"
"v_mfma_f32_16x16x32_bf16 v[184:187], a[20:23], a[60:63], v[184:187]\n"
"ds_read_b64_tr_b16 a[2:3], v119 offset:5248\n"
"v_mfma_f32_16x16x32_bf16 v[224:227], a[16:19], a[64:67], v[224:227]\n"
"ds_read_b64_tr_b16 a[4:5], v119 offset:10368\n"
"buffer_load_dwordx4 a[176:179], v175, s[44:47], s58 offen offset:2048 sc1\n"
"v_mfma_f32_16x16x32_bf16 v[224:227], a[20:23], a[68:71], v[224:227]\n"
"s_waitcnt lgkmcnt(3)\n"
"ds_read_b64_tr_b16 a[6:7], v119 offset:15488\n"
"v_mfma_f32_16x16x32_bf16 v[188:191], a[24:27], a[56:59], v[188:191]\n"
"ds_read_b64_tr_b16 a[8:9], v120 offset:128\n"
"buffer_load_dwordx4 a[180:183], v175, s[44:47], s58 offen offset:3072 sc1\n"
"v_mfma_f32_16x16x32_bf16 v[188:191], a[28:31], a[60:63], v[188:191]\n"
"s_add_u32 s58, s58, 4096\n"
"ds_read_b64_tr_b16 a[10:11], v120 offset:5248\n"
"v_mfma_f32_16x16x32_bf16 v[228:231], a[24:27], a[64:67], v[228:231]\n"
"ds_read_b64_tr_b16 a[12:13], v120 offset:10368\n"
"buffer_load_dwordx4 a[184:187], v175, s[44:47], s58 offen offset:0 sc1\n"
"v_mfma_f32_16x16x32_bf16 v[228:231], a[28:31], a[68:71], v[228:231]\n"
"s_waitcnt lgkmcnt(3)\n"
"ds_read_b64_tr_b16 a[14:15], v120 offset:15488\n"
"v_mfma_f32_16x16x32_bf16 v[192:195], a[0:3], a[56:59], v[192:195]\n"
"s_add_u32 s58, s58, 1024\n"
"ds_read_b64_tr_b16 a[16:17], v119 offset:192\n"
"v_mfma_f32_16x16x32_bf16 v[192:195], a[4:7], a[60:63], v[192:195]\n"
"s_add_u32 m0, s61, 4096\n"
"ds_read_b64_tr_b16 a[18:19], v119 offset:5312\n"
"buffer_load_dwordx4 v124, s[48:51], s60 offen offset:0 lds\n"
"v_mfma_f32_16x16x32_bf16 v[232:235], a[0:3], a[64:67], v[232:235]\n"
"ds_read_b64_tr_b16 a[20:21], v119 offset:10432\n"
"buffer_load_dwordx4 v125, s[48:51], s60 offen offset:1024 lds\n"
"v_mfma_f32_16x16x32_bf16 v[232:235], a[4:7], a[68:71], v[232:235]\n"
"s_waitcnt lgkmcnt(3)\n"
"ds_read_b64_tr_b16 a[22:23], v119 offset:15552\n"
"v_mfma_f32_16x16x32_bf16 v[196:199], a[8:11], a[56:59], v[196:199]\n"
"ds_read_b64_tr_b16 a[24:25], v120 offset:192\n"
"buffer_load_dwordx4 v126, s[48:51], s60 offen offset:2048 lds\n"
"v_mfma_f32_16x16x32_bf16 v[196:199], a[12:15], a[60:63], v[196:199]\n"
"ds_read_b64_tr_b16 a[26:27], v120 offset:5312\n"
"v_mfma_f32_16x16x32_bf16 v[236:239], a[8:11], a[64:67], v[236:239]\n"
"ds_read_b64_tr_b16 a[28:29], v120 offset:10432\n"
"buffer_load_dwordx4 v127, s[48:51], s60 offen offset:3072 lds\n"
"v_mfma_f32_16x16x32_bf16 v[236:239], a[12:15], a[68:71], v[236:239]\n"
"s_waitcnt lgkmcnt(3)\n"
"ds_read_b64_tr_b16 a[30:31], v120 offset:15552\n"
"v_mfma_f32_16x16x32_bf16 v[200:203], a[16:19], a[56:59], v[200:203]\n"
"s_add_u32 s60, s60, 131072\n"
"s_cmp_eq_u32 s60, 1835008\n"
"s_cselect_b32 s60, 0, s60\n"
"ds_read_b64_tr_b16 a[0:1], v119 offset:256\n"
"buffer_load_dwordx4 a[188:191], v175, s[44:47], s58 offen offset:0 sc1\n"
"v_mfma_f32_16x16x32_bf16 v[200:203], a[20:23], a[60:63], v[200:203]\n"
"ds_read_b64_tr_b16 a[2:3], v119 offset:5376\n"
"v_mfma_f32_16x16x32_bf16 v[240:243], a[16:19], a[64:67], v[240:243]\n"
"ds_read_b64_tr_b16 a[4:5], v119 offset:10496\n"
"buffer_load_dwordx4 a[208:211], v175, s[44:47], s58 offen offset:1024 sc1\n"
"v_mfma_f32_16x16x32_bf16 v[240:243], a[20:23], a[68:71], v[240:243]\n"
"s_waitcnt lgkmcnt(3)\n"
"ds_read_b64_tr_b16 a[6:7], v119 offset:15616\n"
"v_mfma_f32_16x16x32_bf16 v[204:207], a[24:27], a[56:59], v[204:207]\n"
"ds_read_b64_tr_b16 a[8:9], v120 offset:256\n"
"buffer_load_dwordx4 a[212:215], v175, s[44:47], s58 offen offset:2048 sc1\n"
"v_mfma_f32_16x16x32_bf16 v[204:207], a[28:31], a[60:63], v[204:207]\n"
"ds_read_b64_tr_b16 a[10:11], v120 offset:5376\n"
"buffer_load_dwordx4 a[216:219], v175, s[44:47], s58 offen offset:3072 sc1\n"
"v_mfma_f32_16x16x32_bf16 v[244:247], a[24:27], a[64:67], v[244:247]\n"
"s_add_u32 s58, s58, 4096\n"
"ds_read_b64_tr_b16 a[12:13], v120 offset:10496\n"
"buffer_load_dwordx4 a[220:223], v175, s[44:47], s58 offen offset:0 sc1\n"
"v_mfma_f32_16x16x32_bf16 v[244:247], a[28:31], a[68:71], v[244:247]\n"
"s_waitcnt vmcnt(36) lgkmcnt(3)\n"
"ds_read_b64_tr_b16 a[14:15], v120 offset:15616\n"
"v_mfma_f32_16x16x32_bf16 v[208:211], a[0:3], a[56:59], v[208:211]\n"
"s_barrier\n"
"ds_read_b64_tr_b16 a[16:17], v119 offset:49152\n"
"v_mfma_f32_16x16x32_bf16 v[208:211], a[4:7], a[60:63], v[208:211]\n"
"s_add_u32 s58, s58, 1024\n"
"s_cmp_eq_u32 s58, 71680\n"
"s_cselect_b32 s58, 0, s58\n"
"ds_read_b64_tr_b16 a[18:19], v119 offset:54272\n"
"v_mfma_f32_16x16x32_bf16 v[248:251], a[0:3], a[64:67], v[248:251]\n"
"ds_read_b64_tr_b16 a[20:21], v119 offset:59392\n"
"v_mfma_f32_16x16x32_bf16 v[248:251], a[4:7], a[68:71], v[248:251]\n"
"s_waitcnt lgkmcnt(3)\n"
"ds_read_b64_tr_b16 a[22:23], v119 offset:64512\n"
"v_mfma_f32_16x16x32_bf16 v[212:215], a[8:11], a[56:59], v[212:215]\n"
"ds_read_b64_tr_b16 a[24:25], v120 offset:49152\n"
"v_mfma_f32_16x16x32_bf16 v[212:215], a[12:15], a[60:63], v[212:215]\n"
"ds_read_b64_tr_b16 a[26:27], v120 offset:54272\n"
"v_mfma_f32_16x16x32_bf16 v[252:255], a[8:11], a[64:67], v[252:255]\n"
"ds_read_b64_tr_b16 a[28:29], v120 offset:59392\n"
"v_mfma_f32_16x16x32_bf16 v[252:255], a[12:15], a[68:71], v[252:255]\n"
"s_waitcnt lgkmcnt(3)\n"
"ds_read_b64_tr_b16 a[30:31], v120 offset:64512\n"
"v_mfma_f32_16x16x32_bf16 v[176:179], a[16:19], a[72:75], v[176:179]\n"
"ds_read_b64_tr_b16 a[0:1], v119 offset:49216\n"
"v_mfma_f32_16x16x32_bf16 v[176:179], a[20:23], a[76:79], v[176:179]\n"
"ds_read_b64_tr_b16 a[2:3], v119 offset:54336\n"
"v_mfma_f32_16x16x32_bf16 v[216:219], a[16:19], a[80:83], v[216:219]\n"
"ds_read_b64_tr_b16 a[4:5], v119 offset:59456\n"
"buffer_load_dwordx4 a[224:227], v175, s[44:47], s58 offen offset:0 sc1\n"
"v_mfma_f32_16x16x32_bf16 v[216:219], a[20:23], a[84:87], v[216:219]\n"
"s_waitcnt lgkmcnt(3)\n"
"ds_read_b64_tr_b16 a[6:7], v119 offset:64576\n"
"v_mfma_f32_16x16x32_bf16 v[180:183], a[24:27], a[72:75], v[180:183]\n"
"ds_read_b64_tr_b16 a[8:9], v120 offset:49216\n"
"v_mfma_f32_16x16x32_bf16 v[180:183], a[28:31], a[76:79], v[180:183]\n"
"ds_read_b64_tr_b16 a[10:11], v120 offset:54336\n"
"buffer_load_dwordx4 a[228:231], v175, s[44:47], s58 offen offset:1024 sc1\n"
"v_mfma_f32_16x16x32_bf16 v[220:223], a[24:27], a[80:83], v[220:223]\n"
"ds_read_b64_tr_b16 a[12:13], v120 offset:59456\n"
"v_mfma_f32_16x16x32_bf16 v[220:223], a[28:31], a[84:87], v[220:223]\n"
"s_waitcnt lgkmcnt(3)\n"
"ds_read_b64_tr_b16 a[14:15], v120 offset:64576\n"
"v_mfma_f32_16x16x32_bf16 v[184:187], a[0:3], a[72:75], v[184:187]\n"
"ds_read_b64_tr_b16 a[16:17], v119 offset:49280\n"
"buffer_load_dwordx4 a[232:235], v175, s[44:47], s58 offen offset:2048 sc1\n"
"v_mfma_f32_16x16x32_bf16 v[184:187], a[4:7], a[76:79], v[184:187]\n"
"ds_read_b64_tr_b16 a[18:19], v119 offset:54400\n"
"v_mfma_f32_16x16x32_bf16 v[224:227], a[0:3], a[80:83], v[224:227]\n"
"ds_read_b64_tr_b16 a[20:21], v119 offset:59520\n"
"v_mfma_f32_16x16x32_bf16 v[224:227], a[4:7], a[84:87], v[224:227]\n"
"s_waitcnt lgkmcnt(3)\n"
"ds_read_b64_tr_b16 a[22:23], v119 offset:64640\n"
"v_mfma_f32_16x16x32_bf16 v[188:191], a[8:11], a[72:75], v[188:191]\n"
"ds_read_b64_tr_b16 a[24:25], v120 offset:49280\n"
"v_mfma_f32_16x16x32_bf16 v[188:191], a[12:15], a[76:79], v[188:191]\n"
"ds_read_b64_tr_b16 a[26:27], v120 offset:54400\n"
"v_mfma_f32_16x16x32_bf16 v[228:231], a[8:11], a[80:83], v[228:231]\n"
"ds_read_b64_tr_b16 a[28:29], v120 offset:59520\n"
"buffer_load_dwordx4 a[236:239], v175, s[44:47], s58 offen offset:3072 sc1\n"
"v_mfma_f32_16x16x32_bf16 v[228:231], a[12:15], a[84:87], v[228:231]\n"
"s_waitcnt lgkmcnt(3)\n"
"ds_read_b64_tr_b16 a[30:31], v120 offset:64640\n"
"v_mfma_f32_16x16x32_bf16 v[192:195], a[16:19], a[72:75], v[192:195]\n"
"s_add_u32 s58, s58, 4096\n"
"ds_read_b64_tr_b16 a[0:1], v119 offset:49344\n"
"v_mfma_f32_16x16x32_bf16 v[192:195], a[20:23], a[76:79], v[192:195]\n"
"ds_read_b64_tr_b16 a[2:3], v119 offset:54464\n"
"buffer_load_dwordx4 a[240:243], v175, s[44:47], s58 offen offset:0 sc1\n"
"v_mfma_f32_16x16x32_bf16 v[232:235], a[16:19], a[80:83], v[232:235]\n"
"s_add_u32 s58, s58, 1024\n"
"ds_read_b64_tr_b16 a[4:5], v119 offset:59584\n"
"v_mfma_f32_16x16x32_bf16 v[232:235], a[20:23], a[84:87], v[232:235]\n"
"s_waitcnt lgkmcnt(3)\n"
"ds_read_b64_tr_b16 a[6:7], v119 offset:64704\n"
"v_mfma_f32_16x16x32_bf16 v[196:199], a[24:27], a[72:75], v[196:199]\n"
"ds_read_b64_tr_b16 a[8:9], v120 offset:49344\n"
"buffer_load_dwordx4 a[244:247], v175, s[44:47], s58 offen offset:0 sc1\n"
"v_mfma_f32_16x16x32_bf16 v[196:199], a[28:31], a[76:79], v[196:199]\n"
"ds_read_b64_tr_b16 a[10:11], v120 offset:54464\n"
"v_mfma_f32_16x16x32_bf16 v[236:239], a[24:27], a[80:83], v[236:239]\n"
"ds_read_b64_tr_b16 a[12:13], v120 offset:59584\n"
"v_mfma_f32_16x16x32_bf16 v[236:239], a[28:31], a[84:87], v[236:239]\n"
"s_waitcnt lgkmcnt(3)\n"
"ds_read_b64_tr_b16 a[14:15], v120 offset:64704\n"
"v_mfma_f32_16x16x32_bf16 v[200:203], a[0:3], a[72:75], v[200:203]\n"
"ds_read_b64_tr_b16 a[16:17], v119 offset:49408\n"
"v_mfma_f32_16x16x32_bf16 v[200:203], a[4:7], a[76:79], v[200:203]\n"
"ds_read_b64_tr_b16 a[18:19], v119 offset:54528\n"
"v_mfma_f32_16x16x32_bf16 v[240:243], a[0:3], a[80:83], v[240:243]\n"
"ds_read_b64_tr_b16 a[20:21], v119 offset:59648\n"
"buffer_load_dwordx4 a[248:251], v175, s[44:47], s58 offen offset:1024 sc1\n"
"v_mfma_f32_16x16x32_bf16 v[240:243], a[4:7], a[84:87], v[240:243]\n"
"s_waitcnt lgkmcnt(3)\n"
"ds_read_b64_tr_b16 a[22:23], v119 offset:64768\n"
"v_mfma_f32_16x16x32_bf16 v[204:207], a[8:11], a[72:75], v[204:207]\n"
"ds_read_b64_tr_b16 a[24:25], v120 offset:49408\n"
"v_mfma_f32_16x16x32_bf16 v[204:207], a[12:15], a[76:79], v[204:207]\n"
"ds_read_b64_tr_b16 a[26:27], v120 offset:54528\n"
"buffer_load_dwordx4 a[252:255], v175, s[44:47], s58 offen offset:2048 sc1\n"
"v_mfma_f32_16x16x32_bf16 v[244:247], a[8:11], a[80:83], v[244:247]\n"
"ds_read_b64_tr_b16 a[28:29], v120 offset:59648\n"
"v_mfma_f32_16x16x32_bf16 v[244:247], a[12:15], a[84:87], v[244:247]\n"
"s_waitcnt lgkmcnt(3)\n"
"ds_read_b64_tr_b16 a[30:31], v120 offset:64768\n"
"v_mfma_f32_16x16x32_bf16 v[208:211], a[16:19], a[72:75], v[208:211]\n"
"s_nop 0\n"
"v_mfma_f32_16x16x32_bf16 v[208:211], a[20:23], a[76:79], v[208:211]\n"
"s_nop 0\n"
"v_mfma_f32_16x16x32_bf16 v[248:251], a[16:19], a[80:83], v[248:251]\n"
"s_nop 0\n"
"v_mfma_f32_16x16x32_bf16 v[248:251], a[20:23], a[84:87], v[248:251]\n"
"s_waitcnt lgkmcnt(0)\n"
"s_nop 0\n"
"v_mfma_f32_16x16x32_bf16 v[212:215], a[24:27], a[72:75], v[212:215]\n"
"s_nop 0\n"
"v_mfma_f32_16x16x32_bf16 v[212:215], a[28:31], a[76:79], v[212:215]\n"
"s_nop 0\n"
"v_mfma_f32_16x16x32_bf16 v[252:255], a[24:27], a[80:83], v[252:255]\n"
"s_nop 0\n"
"buffer_load_dwordx4 a[192:195], v175, s[44:47], s58 offen offset:3072 sc1\n"
"v_mfma_f32_16x16x32_bf16 v[252:255], a[28:31], a[84:87], v[252:255]\n"
"s_add_u32 s58, s58, 4096\n"
"s_nop 0\n"
"buffer_load_dwordx4 a[196:199], v175, s[44:47], s58 offen offset:0 sc1\n"
"s_add_u32 s58, s58, 1024\n"
"s_cmp_eq_u32 s58, 71680\n"
"s_cselect_b32 s58, 0, s58\n"
"s_barrier\n"
"s_add_u32 m0, s61, 32768\n"
"s_nop 0\n"
"buffer_load_dwordx4 v124, s[48:51], s60 offen offset:0 lds\n"
"buffer_load_dwordx4 v125, s[48:51], s60 offen offset:1024 lds\n"
"buffer_load_dwordx4 v126, s[48:51], s60 offen offset:2048 lds\n"
"buffer_load_dwordx4 v127, s[48:51], s60 offen offset:3072 lds\n"
"s_add_u32 s60, s60, 131072\n"
"s_add_u32 m0, s61, 36864\n"
"s_nop 0\n"
"buffer_load_dwordx4 v124, s[48:51], s60 offen offset:0 lds\n"
"buffer_load_dwordx4 v125, s[48:51], s60 offen offset:1024 lds\n"
"buffer_load_dwordx4 v126, s[48:51], s60 offen offset:2048 lds\n"
"buffer_load_dwordx4 v127, s[48:51], s60 offen offset:3072 lds\n"
"s_add_u32 s60, s60, 131072\n"
"s_cmp_eq_u32 s60, 1835008\n"
"s_cselect_b32 s60, 0, s60\n"
"s_add_u32 s96, s24, s68\n"
"s_addc_u32 s97, s25, 0\n"
"s_mov_b32 s98, 32\n"
"s_mov_b32 s99, 0x20000\n"
"s_waitcnt vmcnt(36)\n"
"ds_read_b64_tr_b16 v[96:97], v168 offset:0\n"
"ds_read_b64_tr_b16 v[98:99], v168 offset:512\n"
"ds_read_b64_tr_b16 v[100:101], v168 offset:4096\n"
"ds_read_b64_tr_b16 v[102:103], v168 offset:4608\n"
"s_waitcnt lgkmcnt(0)\n"
"v_mfma_f32_16x16x32_bf16 v[16:19], a[128:131], v[96:99], 0\n"
"s_add_u32 m0, s61, 65536\n"
"ds_read_b64_tr_b16 v[104:105], v169 offset:0\n"
"buffer_load_dwordx4 v124, s[48:51], s60 offen offset:0 lds\n"
"v_mfma_f32_16x16x32_bf16 v[16:19], a[148:151], v[100:103], v[16:19]\n"
"ds_read_b64_tr_b16 v[106:107], v169 offset:512\n"
"v_mfma_f32_16x16x32_bf16 v[20:23], a[132:135], v[96:99], 0\n"
"ds_read_b64_tr_b16 v[108:109], v169 offset:4096\n"
"v_mfma_f32_16x16x32_bf16 v[20:23], a[152:155], v[100:103], v[20:23]\n"
"ds_read_b64_tr_b16 v[110:111], v169 offset:4608\n"
"buffer_load_dwordx4 v125, s[48:51], s60 offen offset:1024 lds\n"
"v_mfma_f32_16x16x32_bf16 v[24:27], a[136:139], v[96:99], 0\n"
"v_mfma_f32_16x16x32_bf16 v[24:27], a[156:159], v[100:103], v[24:27]\n"
"v_mfma_f32_16x16x32_bf16 v[28:31], a[140:143], v[96:99], 0\n"
"buffer_load_dwordx4 v126, s[48:51], s60 offen offset:2048 lds\n"
"v_mfma_f32_16x16x32_bf16 v[28:31], a[160:163], v[100:103], v[28:31]\n"
"v_mfma_f32_16x16x32_bf16 v[32:35], a[144:147], v[96:99], 0\n"
"v_mfma_f32_16x16x32_bf16 v[32:35], a[164:167], v[100:103], v[32:35]\n"
"s_waitcnt lgkmcnt(0)\n"
"s_and_b32 s56, s30, 2\n"
"s_add_u32 s56, s56, 0\n"
"s_mul_i32 s57, s56, 40960\n"
"s_cmp_eq_u32 s56, s64\n"
"s_cbranch_scc1 L_own0_%=\n"
"v_cvt_pk_bf16_f32 v176, v176, v177\n"
"v_cvt_pk_bf16_f32 v177, v178, v179\n"
"v_cvt_pk_bf16_f32 v178, v180, v181\n"
"v_cvt_pk_bf16_f32 v179, v182, v183\n"
"v_cvt_pk_bf16_f32 v180, v184, v185\n"
"v_cvt_pk_bf16_f32 v181, v186, v187\n"
"v_cvt_pk_bf16_f32 v182, v188, v189\n"
"v_cvt_pk_bf16_f32 v183, v190, v191\n"
"v_cvt_pk_bf16_f32 v184, v192, v193\n"
"v_cvt_pk_bf16_f32 v185, v194, v195\n"
"buffer_store_dwordx4 v[176:179], v158, s[52:55], s57 offen sc1\n"
"buffer_store_dwordx4 v[180:183], v158, s[52:55], s57 offen offset:1024 sc1\n"
"buffer_store_dwordx2 v[184:185], v172, s[52:55], s57 offen offset:2048 sc1\n"
"v_cvt_pk_bf16_f32 v216, v216, v217\n"
"v_cvt_pk_bf16_f32 v217, v218, v219\n"
"v_cvt_pk_bf16_f32 v218, v220, v221\n"
"v_cvt_pk_bf16_f32 v219, v222, v223\n"
"v_cvt_pk_bf16_f32 v220, v224, v225\n"
"v_cvt_pk_bf16_f32 v221, v226, v227\n"
"v_cvt_pk_bf16_f32 v222, v228, v229\n"
"v_cvt_pk_bf16_f32 v223, v230, v231\n"
"v_cvt_pk_bf16_f32 v224, v232, v233\n"
"v_cvt_pk_bf16_f32 v225, v234, v235\n"
"buffer_store_dwordx4 v[216:219], v159, s[52:55], s57 offen sc1\n"
"buffer_store_dwordx4 v[220:223], v159, s[52:55], s57 offen offset:1024 sc1\n"
"buffer_store_dwordx2 v[224:225], v173, s[52:55], s57 offen offset:2048 sc1\n"
"s_branch L_done0_%=\n"
"L_own0_%=:\n"
"s_waitcnt lgkmcnt(0)\n"
"ds_write_b128 v174, v[176:179] offset:0\n"
"ds_write_b128 v174, v[180:183] offset:1024\n"
"ds_write_b128 v174, v[184:187] offset:2048\n"
"ds_write_b128 v174, v[188:191] offset:3072\n"
"ds_write_b128 v174, v[192:195] offset:4096\n"
"ds_write_b128 v174, v[216:219] offset:5120\n"
"ds_write_b128 v174, v[220:223] offset:6144\n"
"ds_write_b128 v174, v[224:227] offset:7168\n"
"ds_write_b128 v174, v[228:231] offset:8192\n"
"ds_write_b128 v174, v[232:235] offset:9216\n"
"L_done0_%=:\n"
"v_mfma_f32_16x16x32_bf16 v[36:39], a[128:131], v[104:107], 0\n"
"ds_read_b64_tr_b16 v[96:97], v170 offset:0\n"
"v_mfma_f32_16x16x32_bf16 v[36:39], a[148:151], v[108:111], v[36:39]\n"
"ds_read_b64_tr_b16 v[98:99], v170 offset:512\n"
"v_mfma_f32_16x16x32_bf16 v[40:43], a[132:135], v[104:107], 0\n"
"ds_read_b64_tr_b16 v[100:101], v170 offset:4096\n"
"buffer_load_dwordx4 v127, s[48:51], s60 offen offset:3072 lds\n"
"v_mfma_f32_16x16x32_bf16 v[40:43], a[152:155], v[108:111], v[40:43]\n"
"s_add_u32 s60, s60, 131072\n"
"ds_read_b64_tr_b16 v[102:103], v170 offset:4608\n"
"v_mfma_f32_16x16x32_bf16 v[44:47], a[136:139], v[104:107], 0\n"
"v_mfma_f32_16x16x32_bf16 v[44:47], a[156:159], v[108:111], v[44:47]\n"
"buffer_load_dwordx4 a[0:3], v175, s[44:47], s58 offen offset:0 sc1\n"
"v_mfma_f32_16x16x32_bf16 v[48:51], a[140:143], v[104:107], 0\n"
"v_mfma_f32_16x16x32_bf16 v[48:51], a[160:163], v[108:111], v[48:51]\n"
"v_mfma_f32_16x16x32_bf16 v[52:55], a[144:147], v[104:107], 0\n"
"v_mfma_f32_16x16x32_bf16 v[52:55], a[164:167], v[108:111], v[52:55]\n"
"s_waitcnt lgkmcnt(0)\n"
"buffer_load_dwordx4 a[4:7], v175, s[44:47], s58 offen offset:1024 sc1\n"
"v_mfma_f32_16x16x32_bf16 v[56:59], a[128:131], v[96:99], 0\n"
"ds_read_b64_tr_b16 v[104:105], v171 offset:0\n"
"v_mfma_f32_16x16x32_bf16 v[56:59], a[148:151], v[100:103], v[56:59]\n"
"ds_read_b64_tr_b16 v[106:107], v171 offset:512\n"
"v_mfma_f32_16x16x32_bf16 v[60:63], a[132:135], v[96:99], 0\n"
"ds_read_b64_tr_b16 v[108:109], v171 offset:4096\n"
"s_and_b32 s56, s30, 2\n"
"s_add_u32 s56, s56, 1\n"
"s_mul_i32 s57, s56, 40960\n"
"s_cmp_eq_u32 s56, s64\n"
"s_cbranch_scc1 L_own1_%=\n"
"v_cvt_pk_bf16_f32 v196, v196, v197\n"
"v_cvt_pk_bf16_f32 v197, v198, v199\n"
"v_cvt_pk_bf16_f32 v198, v200, v201\n"
"v_cvt_pk_bf16_f32 v199, v202, v203\n"
"v_cvt_pk_bf16_f32 v200, v204, v205\n"
"v_cvt_pk_bf16_f32 v201, v206, v207\n"
"v_cvt_pk_bf16_f32 v202, v208, v209\n"
"v_cvt_pk_bf16_f32 v203, v210, v211\n"
"v_cvt_pk_bf16_f32 v204, v212, v213\n"
"v_cvt_pk_bf16_f32 v205, v214, v215\n"
"buffer_store_dwordx4 v[196:199], v158, s[52:55], s57 offen sc1\n"
"buffer_store_dwordx4 v[200:203], v158, s[52:55], s57 offen offset:1024 sc1\n"
"buffer_store_dwordx2 v[204:205], v172, s[52:55], s57 offen offset:2048 sc1\n"
"v_cvt_pk_bf16_f32 v236, v236, v237\n"
"v_cvt_pk_bf16_f32 v237, v238, v239\n"
"v_cvt_pk_bf16_f32 v238, v240, v241\n"
"v_cvt_pk_bf16_f32 v239, v242, v243\n"
"v_cvt_pk_bf16_f32 v240, v244, v245\n"
"v_cvt_pk_bf16_f32 v241, v246, v247\n"
"v_cvt_pk_bf16_f32 v242, v248, v249\n"
"v_cvt_pk_bf16_f32 v243, v250, v251\n"
"v_cvt_pk_bf16_f32 v244, v252, v253\n"
"v_cvt_pk_bf16_f32 v245, v254, v255\n"
"buffer_store_dwordx4 v[236:239], v159, s[52:55], s57 offen sc1\n"
"buffer_store_dwordx4 v[240:243], v159, s[52:55], s57 offen offset:1024 sc1\n"
"buffer_store_dwordx2 v[244:245], v173, s[52:55], s57 offen offset:2048 sc1\n"
"s_branch L_done1_%=\n"
"L_own1_%=:\n"
"s_waitcnt lgkmcnt(0)\n"
"ds_write_b128 v174, v[196:199] offset:0\n"
"ds_write_b128 v174, v[200:203] offset:1024\n"
"ds_write_b128 v174, v[204:207] offset:2048\n"
"ds_write_b128 v174, v[208:211] offset:3072\n"
"ds_write_b128 v174, v[212:215] offset:4096\n"
"ds_write_b128 v174, v[236:239] offset:5120\n"
"ds_write_b128 v174, v[240:243] offset:6144\n"
"ds_write_b128 v174, v[244:247] offset:7168\n"
"ds_write_b128 v174, v[248:251] offset:8192\n"
"ds_write_b128 v174, v[252:255] offset:9216\n"
"L_done1_%=:\n"
"v_mfma_f32_16x16x32_bf16 v[60:63], a[152:155], v[100:103], v[60:63]\n"
"ds_read_b64_tr_b16 v[110:111], v171 offset:4608\n"
"v_mfma_f32_16x16x32_bf16 v[64:67], a[136:139], v[96:99], 0\n"
"v_mfma_f32_16x16x32_bf16 v[64:67], a[156:159], v[100:103], v[64:67]\n"
"buffer_load_dwordx4 a[8:11], v175, s[44:47], s58 offen offset:2048 sc1\n"
"v_mfma_f32_16x16x32_bf16 v[68:71], a[140:143], v[96:99], 0\n"
"v_mfma_f32_16x16x32_bf16 v[68:71], a[160:163], v[100:103], v[68:71]\n"
"v_mfma_f32_16x16x32_bf16 v[72:75], a[144:147], v[96:99], 0\n"
"buffer_load_dwordx4 a[12:15], v175, s[44:47], s58 offen offset:3072 sc1\n"
"v_mfma_f32_16x16x32_bf16 v[72:75], a[164:167], v[100:103], v[72:75]\n"
"s_waitcnt lgkmcnt(0)\n"
"v_mfma_f32_16x16x32_bf16 v[76:79], a[128:131], v[104:107], 0\n"
"s_waitcnt vmcnt(26)\n"
"ds_read_b64_tr_b16 v[96:97], v160 offset:0\n"
"v_mfma_f32_16x16x32_bf16 v[76:79], a[148:151], v[108:111], v[76:79]\n"
"s_add_u32 s58, s58, 4096\n"
"ds_read_b64_tr_b16 v[98:99], v160 offset:512\n"
"buffer_load_dwordx4 a[16:19], v175, s[44:47], s58 offen offset:0 sc1\n"
"v_mfma_f32_16x16x32_bf16 v[80:83], a[132:135], v[104:107], 0\n"
"s_add_u32 s58, s58, 1024\n"
"ds_read_b64_tr_b16 v[100:101], v160 offset:4096\n"
"v_mfma_f32_16x16x32_bf16 v[80:83], a[152:155], v[108:111], v[80:83]\n"
"s_add_u32 m0, s61, 69632\n"
"ds_read_b64_tr_b16 v[102:103], v160 offset:4608\n"
"v_mfma_f32_16x16x32_bf16 v[84:87], a[136:139], v[104:107], 0\n"
"buffer_load_dwordx4 v124, s[48:51], s60 offen offset:0 lds\n"
"v_mfma_f32_16x16x32_bf16 v[84:87], a[156:159], v[108:111], v[84:87]\n"
"v_mfma_f32_16x16x32_bf16 v[88:91], a[140:143], v[104:107], 0\n"
"v_mfma_f32_16x16x32_bf16 v[88:91], a[160:163], v[108:111], v[88:91]\n"
"v_mfma_f32_16x16x32_bf16 v[92:95], a[144:147], v[104:107], 0\n"
"v_mfma_f32_16x16x32_bf16 v[92:95], a[164:167], v[108:111], v[92:95]\n"
"s_waitcnt lgkmcnt(0)\n"
"v_mfma_f32_16x16x32_bf16 v[16:19], a[168:171], v[96:99], v[16:19]\n"
"ds_read_b64_tr_b16 v[104:105], v161 offset:0\n"
"buffer_load_dwordx4 v125, s[48:51], s60 offen offset:1024 lds\n"
"v_mfma_f32_16x16x32_bf16 v[16:19], a[188:191], v[100:103], v[16:19]\n"
"ds_read_b64_tr_b16 v[106:107], v161 offset:512\n"
"v_mfma_f32_16x16x32_bf16 v[20:23], a[172:175], v[96:99], v[20:23]\n"
"ds_read_b64_tr_b16 v[108:109], v161 offset:4096\n"
"buffer_load_dwordx4 v126, s[48:51], s60 offen offset:2048 lds\n"
"v_mfma_f32_16x16x32_bf16 v[20:23], a[208:211], v[100:103], v[20:23]\n"
"ds_read_b64_tr_b16 v[110:111], v161 offset:4608\n"
"v_mfma_f32_16x16x32_bf16 v[24:27], a[176:179], v[96:99], v[24:27]\n"
"v_mfma_f32_16x16x32_bf16 v[24:27], a[212:215], v[100:103], v[24:27]\n"
"buffer_load_dwordx4 v127, s[48:51], s60 offen offset:3072 lds\n"
"v_mfma_f32_16x16x32_bf16 v[28:31], a[180:183], v[96:99], v[28:31]\n"
"s_add_u32 s60, s60, 131072\n"
"s_cmp_eq_u32 s60, 1835008\n"
"s_cselect_b32 s60, 0, s60\n"
"v_mfma_f32_16x16x32_bf16 v[28:31], a[216:219], v[100:103], v[28:31]\n"
"v_mfma_f32_16x16x32_bf16 v[32:35], a[184:187], v[96:99], v[32:35]\n"
"buffer_load_dwordx4 a[20:23], v175, s[44:47], s58 offen offset:0 sc1\n"
"v_mfma_f32_16x16x32_bf16 v[32:35], a[220:223], v[100:103], v[32:35]\n"
"s_waitcnt lgkmcnt(0)\n"
"v_mfma_f32_16x16x32_bf16 v[36:39], a[168:171], v[104:107], v[36:39]\n"
"ds_read_b64_tr_b16 v[96:97], v162 offset:0\n"
"buffer_load_dwordx4 a[24:27], v175, s[44:47], s58 offen offset:1024 sc1\n"
"v_mfma_f32_16x16x32_bf16 v[36:39], a[188:191], v[108:111], v[36:39]\n"
"ds_read_b64_tr_b16 v[98:99], v162 offset:512\n"
"v_mfma_f32_16x16x32_bf16 v[40:43], a[172:175], v[104:107], v[40:43]\n"
"ds_read_b64_tr_b16 v[100:101], v162 offset:4096\n"
"v_mfma_f32_16x16x32_bf16 v[40:43], a[208:211], v[108:111], v[40:43]\n"
"ds_read_b64_tr_b16 v[102:103], v162 offset:4608\n"
"buffer_load_dwordx4 a[28:31], v175, s[44:47], s58 offen offset:2048 sc1\n"
"v_mfma_f32_16x16x32_bf16 v[44:47], a[176:179], v[104:107], v[44:47]\n"
"v_mfma_f32_16x16x32_bf16 v[44:47], a[212:215], v[108:111], v[44:47]\n"
"v_mfma_f32_16x16x32_bf16 v[48:51], a[180:183], v[104:107], v[48:51]\n"
"buffer_load_dwordx4 a[32:35], v175, s[44:47], s58 offen offset:3072 sc1\n"
"v_mfma_f32_16x16x32_bf16 v[48:51], a[216:219], v[108:111], v[48:51]\n"
"s_add_u32 s58, s58, 4096\n"
"v_mfma_f32_16x16x32_bf16 v[52:55], a[184:187], v[104:107], v[52:55]\n"
"v_mfma_f32_16x16x32_bf16 v[52:55], a[220:223], v[108:111], v[52:55]\n"
"s_waitcnt lgkmcnt(0)\n"
"buffer_load_dwordx4 a[36:39], v175, s[44:47], s58 offen offset:0 sc1\n"
"v_mfma_f32_16x16x32_bf16 v[56:59], a[168:171], v[96:99], v[56:59]\n"
"s_add_u32 s58, s58, 1024\n"
"s_cmp_eq_u32 s58, 71680\n"
"s_cselect_b32 s58, 0, s58\n"
"ds_read_b64_tr_b16 v[104:105], v163 offset:0\n"
"v_mfma_f32_16x16x32_bf16 v[56:59], a[188:191], v[100:103], v[56:59]\n"
"s_add_u32 m0, s61, 131072\n"
"ds_read_b64_tr_b16 v[106:107], v163 offset:512\n"
"buffer_load_dwordx4 v124, s[48:51], s60 offen offset:0 lds\n"
"v_mfma_f32_16x16x32_bf16 v[60:63], a[172:175], v[96:99], v[60:63]\n"
"ds_read_b64_tr_b16 v[108:109], v163 offset:4096\n"
"v_mfma_f32_16x16x32_bf16 v[60:63], a[208:211], v[100:103], v[60:63]\n"
"ds_read_b64_tr_b16 v[110:111], v163 offset:4608\n"
"v_mfma_f32_16x16x32_bf16 v[64:67], a[176:179], v[96:99], v[64:67]\n"
"buffer_load_dwordx4 v125, s[48:51], s60 offen offset:1024 lds\n"
"v_mfma_f32_16x16x32_bf16 v[64:67], a[212:215], v[100:103], v[64:67]\n"
"v_mfma_f32_16x16x32_bf16 v[68:71], a[180:183], v[96:99], v[68:71]\n"
"v_mfma_f32_16x16x32_bf16 v[68:71], a[216:219], v[100:103], v[68:71]\n"
"buffer_load_dwordx4 v126, s[48:51], s60 offen offset:2048 lds\n"
"v_mfma_f32_16x16x32_bf16 v[72:75], a[184:187], v[96:99], v[72:75]\n"
"v_mfma_f32_16x16x32_bf16 v[72:75], a[220:223], v[100:103], v[72:75]\n"
"s_waitcnt lgkmcnt(0)\n"
"buffer_load_dwordx4 v127, s[48:51], s60 offen offset:3072 lds\n"
"v_mfma_f32_16x16x32_bf16 v[76:79], a[168:171], v[104:107], v[76:79]\n"
"s_waitcnt vmcnt(22)\n"
"ds_read_b64_tr_b16 v[96:97], v160 offset:32768\n"
"v_mfma_f32_16x16x32_bf16 v[76:79], a[188:191], v[108:111], v[76:79]\n"
"s_add_u32 s60, s60, 131072\n"
"ds_read_b64_tr_b16 v[98:99], v160 offset:33280\n"
"v_mfma_f32_16x16x32_bf16 v[80:83], a[172:175], v[104:107], v[80:83]\n"
"ds_read_b64_tr_b16 v[100:101], v160 offset:36864\n"
"buffer_load_dwordx4 a[128:131], v175, s[44:47], s58 offen offset:0 sc1\n"
"v_mfma_f32_16x16x32_bf16 v[80:83], a[208:211], v[108:111], v[80:83]\n"
"ds_read_b64_tr_b16 v[102:103], v160 offset:37376\n"
"v_mfma_f32_16x16x32_bf16 v[84:87], a[176:179], v[104:107], v[84:87]\n"
"v_mfma_f32_16x16x32_bf16 v[84:87], a[212:215], v[108:111], v[84:87]\n"
"buffer_load_dwordx4 a[132:135], v175, s[44:47], s58 offen offset:1024 sc1\n"
"v_mfma_f32_16x16x32_bf16 v[88:91], a[180:183], v[104:107], v[88:91]\n"
"v_mfma_f32_16x16x32_bf16 v[88:91], a[216:219], v[108:111], v[88:91]\n"
"v_mfma_f32_16x16x32_bf16 v[92:95], a[184:187], v[104:107], v[92:95]\n"
"v_mfma_f32_16x16x32_bf16 v[92:95], a[220:223], v[108:111], v[92:95]\n"
"s_waitcnt lgkmcnt(0)\n"
"v_mfma_f32_16x16x32_bf16 v[16:19], a[224:227], v[96:99], v[16:19]\n"
"s_add_u32 s95, s30, 1\n"
"s_and_b32 s95, s95, 3\n"
"ds_read_b64_tr_b16 v[104:105], v161 offset:32768\n"
"buffer_load_dwordx4 a[136:139], v175, s[44:47], s58 offen offset:2048 sc1\n"
"v_mfma_f32_16x16x32_bf16 v[16:19], a[244:247], v[100:103], v[16:19]\n"
"s_cmp_ge_u32 s95, 2\n"
"s_addc_u32 s95, s95, 0\n"
"s_cmp_ge_u32 s95, 2\n"
"s_addc_u32 s95, s95, 0\n"
"ds_read_b64_tr_b16 v[106:107], v161 offset:33280\n"
"v_mfma_f32_16x16x32_bf16 v[20:23], a[228:231], v[96:99], v[20:23]\n"
"s_lshl_b32 s95, s95, 14\n"
"s_add_u32 s95, s95, 10240\n"
"ds_read_b64_tr_b16 v[108:109], v161 offset:36864\n"
"buffer_load_dwordx4 a[140:143], v175, s[44:47], s58 offen offset:3072 sc1\n"
"v_add_u32 v152, s95, v175\n"
"v_mfma_f32_16x16x32_bf16 v[20:23], a[248:251], v[100:103], v[20:23]\n"
"s_add_u32 s58, s58, 4096\n"
"ds_read_b64_tr_b16 v[110:111], v161 offset:37376\n"
"v_mfma_f32_16x16x32_bf16 v[24:27], a[232:235], v[96:99], v[24:27]\n"
"s_add_u32 s95, s30, 2\n"
"s_and_b32 s95, s95, 3\n"
"buffer_load_dwordx4 a[144:147], v175, s[44:47], s58 offen offset:0 sc1\n"
"v_mfma_f32_16x16x32_bf16 v[24:27], a[252:255], v[100:103], v[24:27]\n"
"s_add_u32 s58, s58, 1024\n"
"v_mfma_f32_16x16x32_bf16 v[28:31], a[236:239], v[96:99], v[28:31]\n"
"s_add_u32 m0, s61, 135168\n"
"v_mfma_f32_16x16x32_bf16 v[28:31], a[192:195], v[100:103], v[28:31]\n"
"s_cmp_ge_u32 s95, 2\n"
"s_addc_u32 s95, s95, 0\n"
"s_cmp_ge_u32 s95, 2\n"
"s_addc_u32 s95, s95, 0\n"
"buffer_load_dwordx4 v124, s[48:51], s60 offen offset:0 lds\n"
"v_mfma_f32_16x16x32_bf16 v[32:35], a[240:243], v[96:99], v[32:35]\n"
"s_lshl_b32 s95, s95, 14\n"
"s_add_u32 s95, s95, 5120\n"
"v_add_u32 v153, s95, v175\n"
"v_mfma_f32_16x16x32_bf16 v[32:35], a[196:199], v[100:103], v[32:35]\n"
"s_waitcnt lgkmcnt(0)\n"
"buffer_load_dwordx4 v125, s[48:51], s60 offen offset:1024 lds\n"
"v_mfma_f32_16x16x32_bf16 v[36:39], a[224:227], v[104:107], v[36:39]\n"
"s_add_u32 s95, s30, 3\n"
"s_and_b32 s95, s95, 3\n"
"ds_read_b64_tr_b16 v[96:97], v162 offset:32768\n"
"v_mfma_f32_16x16x32_bf16 v[36:39], a[244:247], v[108:111], v[36:39]\n"
"s_cmp_ge_u32 s95, 2\n"
"s_addc_u32 s95, s95, 0\n"
"s_cmp_ge_u32 s95, 2\n"
"s_addc_u32 s95, s95, 0\n"
"ds_read_b64_tr_b16 v[98:99], v162 offset:33280\n"
"buffer_load_dwordx4 v126, s[48:51], s60 offen offset:2048 lds\n"
"v_mfma_f32_16x16x32_bf16 v[40:43], a[228:231], v[104:107], v[40:43]\n"
"s_lshl_b32 s95, s95, 14\n"
"s_add_u32 s95, s95, 0\n"
"ds_read_b64_tr_b16 v[100:101], v162 offset:36864\n"
"v_add_u32 v154, s95, v175\n"
"v_mfma_f32_16x16x32_bf16 v[40:43], a[248:251], v[108:111], v[40:43]\n"
"s_mul_i32 s95, s30, 5120\n"
"s_add_u32 s95, s95, 98304\n"
"ds_read_b64_tr_b16 v[102:103], v162 offset:37376\n"
"v_add_u32 v1, s95, v175\n"
"v_mfma_f32_16x16x32_bf16 v[44:47], a[232:235], v[104:107], v[44:47]\n"
"buffer_load_dwordx4 v127, s[48:51], s60 offen offset:3072 lds\n"
"v_mfma_f32_16x16x32_bf16 v[44:47], a[252:255], v[108:111], v[44:47]\n"
"s_add_u32 s60, s60, 131072\n"
"s_cmp_eq_u32 s60, 1835008\n"
"s_cselect_b32 s60, 0, s60\n"
"v_mfma_f32_16x16x32_bf16 v[48:51], a[236:239], v[104:107], v[48:51]\n"
"buffer_load_dwordx4 a[148:151], v175, s[44:47], s58 offen offset:0 sc1\n"
"v_mfma_f32_16x16x32_bf16 v[48:51], a[192:195], v[108:111], v[48:51]\n"
"v_mfma_f32_16x16x32_bf16 v[52:55], a[240:243], v[104:107], v[52:55]\n"
"v_mfma_f32_16x16x32_bf16 v[52:55], a[196:199], v[108:111], v[52:55]\n"
"s_waitcnt lgkmcnt(0)\n"
"buffer_load_dwordx4 a[152:155], v175, s[44:47], s58 offen offset:1024 sc1\n"
"v_mfma_f32_16x16x32_bf16 v[56:59], a[224:227], v[96:99], v[56:59]\n"
"ds_read_b64_tr_b16 v[104:105], v163 offset:32768\n"
"v_mfma_f32_16x16x32_bf16 v[56:59], a[244:247], v[100:103], v[56:59]\n"
"ds_read_b64_tr_b16 v[106:107], v163 offset:33280\n"
"buffer_load_dwordx4 a[156:159], v175, s[44:47], s58 offen offset:2048 sc1\n"
"v_mfma_f32_16x16x32_bf16 v[60:63], a[228:231], v[96:99], v[60:63]\n"
"ds_read_b64_tr_b16 v[108:109], v163 offset:36864\n"
"v_mfma_f32_16x16x32_bf16 v[60:63], a[248:251], v[100:103], v[60:63]\n"
"ds_read_b64_tr_b16 v[110:111], v163 offset:37376\n"
"buffer_load_dwordx4 a[160:163], v175, s[44:47], s58 offen offset:3072 sc1\n"
"v_mfma_f32_16x16x32_bf16 v[64:67], a[232:235], v[96:99], v[64:67]\n"
"s_add_u32 s58, s58, 4096\n"
"v_mfma_f32_16x16x32_bf16 v[64:67], a[252:255], v[100:103], v[64:67]\n"
"v_mfma_f32_16x16x32_bf16 v[68:71], a[236:239], v[96:99], v[68:71]\n"
"buffer_load_dwordx4 a[164:167], v175, s[44:47], s58 offen offset:0 sc1\n"
"v_mfma_f32_16x16x32_bf16 v[68:71], a[192:195], v[100:103], v[68:71]\n"
"s_add_u32 s58, s58, 1024\n"
"s_cmp_eq_u32 s58, 71680\n"
"s_cselect_b32 s58, 0, s58\n"
"v_mfma_f32_16x16x32_bf16 v[72:75], a[240:243], v[96:99], v[72:75]\n"
"s_add_u32 m0, s61, 0\n"
"v_mfma_f32_16x16x32_bf16 v[72:75], a[196:199], v[100:103], v[72:75]\n"
"s_waitcnt lgkmcnt(0)\n"
"buffer_load_dwordx4 v124, s[48:51], s60 offen offset:0 lds\n"
"v_mfma_f32_16x16x32_bf16 v[76:79], a[224:227], v[104:107], v[76:79]\n"
"s_waitcnt vmcnt(19)\n"
"s_waitcnt vmcnt(31)\n"
"s_mov_b64 exec, 1\n"
"global_store_dwordx2 v12, v[14:15], s[6:7] sc1\n"
"s_mov_b64 exec, -1\n"
"ds_read_b64_tr_b16 v[96:97], v164 offset:0\n"
"buffer_load_dwordx4 v125, s[48:51], s60 offen offset:1024 lds\n"
"v_mfma_f32_16x16x32_bf16 v[76:79], a[244:247], v[108:111], v[76:79]\n"
"ds_read_b64_tr_b16 v[98:99], v164 offset:512\n"
"v_mfma_f32_16x16x32_bf16 v[80:83], a[228:231], v[104:107], v[80:83]\n"
"ds_read_b64_tr_b16 v[100:101], v164 offset:4096\n"
"v_mfma_f32_16x16x32_bf16 v[80:83], a[248:251], v[108:111], v[80:83]\n"
"ds_read_b64_tr_b16 v[102:103], v164 offset:4608\n"
"buffer_load_dwordx4 v126, s[48:51], s60 offen offset:2048 lds\n"
"v_mfma_f32_16x16x32_bf16 v[84:87], a[232:235], v[104:107], v[84:87]\n"
"v_mfma_f32_16x16x32_bf16 v[84:87], a[252:255], v[108:111], v[84:87]\n"
"buffer_load_dwordx4 v127, s[48:51], s60 offen offset:3072 lds\n"
"v_mfma_f32_16x16x32_bf16 v[88:91], a[236:239], v[104:107], v[88:91]\n"
"s_add_u32 s60, s60, 131072\n"
"v_mfma_f32_16x16x32_bf16 v[88:91], a[192:195], v[108:111], v[88:91]\n"
"v_mfma_f32_16x16x32_bf16 v[92:95], a[240:243], v[104:107], v[92:95]\n"
"v_mfma_f32_16x16x32_bf16 v[92:95], a[196:199], v[108:111], v[92:95]\n"
"s_waitcnt lgkmcnt(0)\n"
"v_mfma_f32_16x16x32_bf16 v[16:19], a[0:3], v[96:99], v[16:19]\n"
"ds_read_b64_tr_b16 v[104:105], v165 offset:0\n"
"buffer_load_dwordx4 a[168:171], v175, s[44:47], s58 offen offset:0 sc1\n"
"v_mfma_f32_16x16x32_bf16 v[16:19], a[20:23], v[100:103], v[16:19]\n"
"ds_read_b64_tr_b16 v[106:107], v165 offset:512\n"
"v_mfma_f32_16x16x32_bf16 v[20:23], a[4:7], v[96:99], v[20:23]\n"
"ds_read_b64_tr_b16 v[108:109], v165 offset:4096\n"
"buffer_load_dwordx4 a[172:175], v175, s[44:47], s58 offen offset:1024 sc1\n"
"v_mfma_f32_16x16x32_bf16 v[20:23], a[24:27], v[100:103], v[20:23]\n"
"ds_read_b64_tr_b16 v[110:111], v165 offset:4608\n"
"v_mfma_f32_16x16x32_bf16 v[24:27], a[8:11], v[96:99], v[24:27]\n"
"buffer_load_dwordx4 a[176:179], v175, s[44:47], s58 offen offset:2048 sc1\n"
"v_mfma_f32_16x16x32_bf16 v[24:27], a[28:31], v[100:103], v[24:27]\n"
"v_mfma_f32_16x16x32_bf16 v[28:31], a[12:15], v[96:99], v[28:31]\n"
"buffer_load_dwordx4 a[180:183], v175, s[44:47], s58 offen offset:3072 sc1\n"
"v_mfma_f32_16x16x32_bf16 v[28:31], a[32:35], v[100:103], v[28:31]\n"
"s_add_u32 s58, s58, 4096\n"
"v_mfma_f32_16x16x32_bf16 v[32:35], a[16:19], v[96:99], v[32:35]\n"
"buffer_load_dwordx4 a[184:187], v175, s[44:47], s58 offen offset:0 sc1\n"
"v_mfma_f32_16x16x32_bf16 v[32:35], a[36:39], v[100:103], v[32:35]\n"
"s_waitcnt lgkmcnt(0)\n"
"v_mfma_f32_16x16x32_bf16 v[36:39], a[0:3], v[104:107], v[36:39]\n"
"s_add_u32 s58, s58, 1024\n"
"ds_read_b64_tr_b16 v[96:97], v166 offset:0\n"
"v_mfma_f32_16x16x32_bf16 v[36:39], a[20:23], v[108:111], v[36:39]\n"
"s_add_u32 m0, s61, 4096\n"
"ds_read_b64_tr_b16 v[98:99], v166 offset:512\n"
"buffer_load_dwordx4 v124, s[48:51], s60 offen offset:0 lds\n"
"v_mfma_f32_16x16x32_bf16 v[40:43], a[4:7], v[104:107], v[40:43]\n"
"ds_read_b64_tr_b16 v[100:101], v166 offset:4096\n"
"buffer_load_dwordx4 v125, s[48:51], s60 offen offset:1024 lds\n"
"v_mfma_f32_16x16x32_bf16 v[40:43], a[24:27], v[108:111], v[40:43]\n"
"ds_read_b64_tr_b16 v[102:103], v166 offset:4608\n"
"v_mfma_f32_16x16x32_bf16 v[44:47], a[8:11], v[104:107], v[44:47]\n"
"buffer_load_dwordx4 v126, s[48:51], s60 offen offset:2048 lds\n"
"v_mfma_f32_16x16x32_bf16 v[44:47], a[28:31], v[108:111], v[44:47]\n"
"v_mfma_f32_16x16x32_bf16 v[48:51], a[12:15], v[104:107], v[48:51]\n"
"buffer_load_dwordx4 v127, s[48:51], s60 offen offset:3072 lds\n"
"v_mfma_f32_16x16x32_bf16 v[48:51], a[32:35], v[108:111], v[48:51]\n"
"s_add_u32 s60, s60, 131072\n"
"s_cmp_eq_u32 s60, 1835008\n"
"s_cselect_b32 s60, 0, s60\n"
"v_mfma_f32_16x16x32_bf16 v[52:55], a[16:19], v[104:107], v[52:55]\n"
"v_mfma_f32_16x16x32_bf16 v[52:55], a[36:39], v[108:111], v[52:55]\n"
"s_waitcnt lgkmcnt(0)\n"
"buffer_load_dwordx4 a[188:191], v175, s[44:47], s58 offen offset:0 sc1\n"
"v_mfma_f32_16x16x32_bf16 v[56:59], a[0:3], v[96:99], v[56:59]\n"
"ds_read_b64_tr_b16 v[104:105], v167 offset:0\n"
"v_mfma_f32_16x16x32_bf16 v[56:59], a[20:23], v[100:103], v[56:59]\n"
"ds_read_b64_tr_b16 v[106:107], v167 offset:512\n"
"buffer_load_dwordx4 a[208:211], v175, s[44:47], s58 offen offset:1024 sc1\n"
"v_mfma_f32_16x16x32_bf16 v[60:63], a[4:7], v[96:99], v[60:63]\n"
"ds_read_b64_tr_b16 v[108:109], v167 offset:4096\n"
"v_mfma_f32_16x16x32_bf16 v[60:63], a[24:27], v[100:103], v[60:63]\n"
"ds_read_b64_tr_b16 v[110:111], v167 offset:4608\n"
"buffer_load_dwordx4 a[212:215], v175, s[44:47], s58 offen offset:2048 sc1\n"
"v_mfma_f32_16x16x32_bf16 v[64:67], a[8:11], v[96:99], v[64:67]\n"
"v_mfma_f32_16x16x32_bf16 v[64:67], a[28:31], v[100:103], v[64:67]\n"
"buffer_load_dwordx4 a[216:219], v175, s[44:47], s58 offen offset:3072 sc1\n"
"v_mfma_f32_16x16x32_bf16 v[68:71], a[12:15], v[96:99], v[68:71]\n"
"s_add_u32 s58, s58, 4096\n"
"v_mfma_f32_16x16x32_bf16 v[68:71], a[32:35], v[100:103], v[68:71]\n"
"buffer_load_dwordx4 a[220:223], v175, s[44:47], s58 offen offset:0 sc1\n"
"v_mfma_f32_16x16x32_bf16 v[72:75], a[16:19], v[96:99], v[72:75]\n"
"s_add_u32 s58, s58, 1024\n"
"s_cmp_eq_u32 s58, 71680\n"
"s_cselect_b32 s58, 0, s58\n"
"v_mfma_f32_16x16x32_bf16 v[72:75], a[36:39], v[100:103], v[72:75]\n"
"s_waitcnt lgkmcnt(0)\n"
"v_mfma_f32_16x16x32_bf16 v[76:79], a[0:3], v[104:107], v[76:79]\n"
"s_waitcnt vmcnt(19)\n"
"ds_read_b64_tr_b16 v[96:97], v168 offset:0\n"
"v_mfma_f32_16x16x32_bf16 v[76:79], a[20:23], v[108:111], v[76:79]\n"
"s_add_u32 m0, s61, 32768\n"
"ds_read_b64_tr_b16 v[98:99], v168 offset:512\n"
"buffer_load_dwordx4 v124, s[48:51], s60 offen offset:0 lds\n"
"v_mfma_f32_16x16x32_bf16 v[80:83], a[4:7], v[104:107], v[80:83]\n"
"ds_read_b64_tr_b16 v[100:101], v168 offset:4096\n"
"buffer_load_dwordx4 v125, s[48:51], s60 offen offset:1024 lds\n"
"v_mfma_f32_16x16x32_bf16 v[80:83], a[24:27], v[108:111], v[80:83]\n"
"ds_read_b64_tr_b16 v[102:103], v168 offset:4608\n"
"buffer_load_dwordx4 v126, s[48:51], s60 offen offset:2048 lds\n"
"v_mfma_f32_16x16x32_bf16 v[84:87], a[8:11], v[104:107], v[84:87]\n"
"v_mfma_f32_16x16x32_bf16 v[84:87], a[28:31], v[108:111], v[84:87]\n"
"buffer_load_dwordx4 v127, s[48:51], s60 offen offset:3072 lds\n"
"v_mfma_f32_16x16x32_bf16 v[88:91], a[12:15], v[104:107], v[88:91]\n"
"s_add_u32 s60, s60, 131072\n"
"v_mfma_f32_16x16x32_bf16 v[88:91], a[32:35], v[108:111], v[88:91]\n"
"v_mfma_f32_16x16x32_bf16 v[92:95], a[16:19], v[104:107], v[92:95]\n"
"v_mfma_f32_16x16x32_bf16 v[92:95], a[36:39], v[108:111], v[92:95]\n"
"s_waitcnt lgkmcnt(0)\n"
"v_mfma_f32_16x16x32_bf16 v[16:19], a[128:131], v[96:99], v[16:19]\n"
"ds_read_b64_tr_b16 v[104:105], v169 offset:0\n"
"global_load_dwordx2 v[6:7], v9, s[6:7] sc1\n"
"v_mfma_f32_16x16x32_bf16 v[16:19], a[148:151], v[100:103], v[16:19]\n"
"ds_read_b64_tr_b16 v[106:107], v169 offset:512\n"
"buffer_load_dwordx4 a[224:227], v175, s[44:47], s58 offen offset:0 sc1\n"
"v_mfma_f32_16x16x32_bf16 v[20:23], a[132:135], v[96:99], v[20:23]\n"
"ds_read_b64_tr_b16 v[108:109], v169 offset:4096\n"
"buffer_load_dwordx4 a[228:231], v175, s[44:47], s58 offen offset:1024 sc1\n"
"v_mfma_f32_16x16x32_bf16 v[20:23], a[152:155], v[100:103], v[20:23]\n"
"ds_read_b64_tr_b16 v[110:111], v169 offset:4608\n"
"buffer_load_dwordx4 a[232:235], v175, s[44:47], s58 offen offset:2048 sc1\n"
"v_mfma_f32_16x16x32_bf16 v[24:27], a[136:139], v[96:99], v[24:27]\n"
"buffer_load_dwordx4 a[236:239], v175, s[44:47], s58 offen offset:3072 sc1\n"
"v_mfma_f32_16x16x32_bf16 v[24:27], a[156:159], v[100:103], v[24:27]\n"
"s_add_u32 s58, s58, 4096\n"
"buffer_load_dwordx4 a[240:243], v175, s[44:47], s58 offen offset:0 sc1\n"
"v_mfma_f32_16x16x32_bf16 v[28:31], a[140:143], v[96:99], v[28:31]\n"
"s_add_u32 s58, s58, 1024\n"
"v_mfma_f32_16x16x32_bf16 v[28:31], a[160:163], v[100:103], v[28:31]\n"
"s_add_u32 m0, s61, 36864\n"
"v_mfma_f32_16x16x32_bf16 v[32:35], a[144:147], v[96:99], v[32:35]\n"
"buffer_load_dwordx4 v124, s[48:51], s60 offen offset:0 lds\n"
"v_mfma_f32_16x16x32_bf16 v[32:35], a[164:167], v[100:103], v[32:35]\n"
"s_waitcnt lgkmcnt(0)\n"
"buffer_load_dwordx4 v125, s[48:51], s60 offen offset:1024 lds\n"
"v_mfma_f32_16x16x32_bf16 v[36:39], a[128:131], v[104:107], v[36:39]\n"
"ds_read_b64_tr_b16 v[96:97], v170 offset:0\n"
"buffer_load_dwordx4 v126, s[48:51], s60 offen offset:2048 lds\n"
"v_mfma_f32_16x16x32_bf16 v[36:39], a[148:151], v[108:111], v[36:39]\n"
"ds_read_b64_tr_b16 v[98:99], v170 offset:512\n"
"buffer_load_dwordx4 v127, s[48:51], s60 offen offset:3072 lds\n"
"v_mfma_f32_16x16x32_bf16 v[40:43], a[132:135], v[104:107], v[40:43]\n"
"s_add_u32 s60, s60, 131072\n"
"s_cmp_eq_u32 s60, 1835008\n"
"s_cselect_b32 s60, 0, s60\n"
"ds_read_b64_tr_b16 v[100:101], v170 offset:4096\n"
"buffer_load_dwordx4 a[244:247], v175, s[44:47], s58 offen offset:0 sc1\n"
"v_mfma_f32_16x16x32_bf16 v[40:43], a[152:155], v[108:111], v[40:43]\n"
"ds_read_b64_tr_b16 v[102:103], v170 offset:4608\n"
"buffer_load_dwordx4 a[248:251], v175, s[44:47], s58 offen offset:1024 sc1\n"
"v_mfma_f32_16x16x32_bf16 v[44:47], a[136:139], v[104:107], v[44:47]\n"
"buffer_load_dwordx4 a[252:255], v175, s[44:47], s58 offen offset:2048 sc1\n"
"v_mfma_f32_16x16x32_bf16 v[44:47], a[156:159], v[108:111], v[44:47]\n"
"buffer_load_dwordx4 a[192:195], v175, s[44:47], s58 offen offset:3072 sc1\n"
"v_mfma_f32_16x16x32_bf16 v[48:51], a[140:143], v[104:107], v[48:51]\n"
"s_add_u32 s58, s58, 4096\n"
"buffer_load_dwordx4 a[196:199], v175, s[44:47], s58 offen offset:0 sc1\n"
"v_mfma_f32_16x16x32_bf16 v[48:51], a[160:163], v[108:111], v[48:51]\n"
"s_add_u32 s58, s58, 1024\n"
"s_cmp_eq_u32 s58, 71680\n"
"s_cselect_b32 s58, 0, s58\n"
"buffer_load_ushort v254, v10, s[96:99], 0 offen\n"
"v_mfma_f32_16x16x32_bf16 v[52:55], a[144:147], v[104:107], v[52:55]\n"
"buffer_load_dwordx2 v[244:245], v11, s[40:43], 0 offen offset:0\n"
"v_mfma_f32_16x16x32_bf16 v[52:55], a[164:167], v[108:111], v[52:55]\n"
"s_waitcnt lgkmcnt(0)\n"
"buffer_load_dwordx2 v[246:247], v11, s[40:43], 0 offen offset:32\n"
"v_mfma_f32_16x16x32_bf16 v[56:59], a[128:131], v[96:99], v[56:59]\n"
"ds_read_b64_tr_b16 v[104:105], v171 offset:0\n"
"buffer_load_dwordx2 v[248:249], v11, s[40:43], 0 offen offset:64\n"
"v_mfma_f32_16x16x32_bf16 v[56:59], a[148:151], v[100:103], v[56:59]\n"
"ds_read_b64_tr_b16 v[106:107], v171 offset:512\n"
"buffer_load_dwordx2 v[250:251], v11, s[40:43], 0 offen offset:96\n"
"v_mfma_f32_16x16x32_bf16 v[60:63], a[132:135], v[96:99], v[60:63]\n"
"ds_read_b64_tr_b16 v[108:109], v171 offset:4096\n"
"buffer_load_dwordx2 v[252:253], v11, s[40:43], 0 offen offset:128\n"
"v_mfma_f32_16x16x32_bf16 v[60:63], a[152:155], v[100:103], v[60:63]\n"
"ds_read_b64_tr_b16 v[110:111], v171 offset:4608\n"
"v_mfma_f32_16x16x32_bf16 v[64:67], a[136:139], v[96:99], v[64:67]\n"
"v_mfma_f32_16x16x32_bf16 v[64:67], a[156:159], v[100:103], v[64:67]\n"
"v_mfma_f32_16x16x32_bf16 v[68:71], a[140:143], v[96:99], v[68:71]\n"
"v_mfma_f32_16x16x32_bf16 v[68:71], a[160:163], v[100:103], v[68:71]\n"
"v_mfma_f32_16x16x32_bf16 v[72:75], a[144:147], v[96:99], v[72:75]\n"
"v_mfma_f32_16x16x32_bf16 v[72:75], a[164:167], v[100:103], v[72:75]\n"
"s_waitcnt lgkmcnt(0)\n"
"v_mfma_f32_16x16x32_bf16 v[76:79], a[128:131], v[104:107], v[76:79]\n"
"s_waitcnt vmcnt(25)\n"
"ds_read_b64_tr_b16 v[96:97], v160 offset:0\n"
"v_mfma_f32_16x16x32_bf16 v[76:79], a[148:151], v[108:111], v[76:79]\n"
"ds_read_b64_tr_b16 v[98:99], v160 offset:512\n"
"v_mfma_f32_16x16x32_bf16 v[80:83], a[132:135], v[104:107], v[80:83]\n"
"ds_read_b64_tr_b16 v[100:101], v160 offset:4096\n"
"v_mfma_f32_16x16x32_bf16 v[80:83], a[152:155], v[108:111], v[80:83]\n"
"ds_read_b64_tr_b16 v[102:103], v160 offset:4608\n"
"v_mfma_f32_16x16x32_bf16 v[84:87], a[136:139], v[104:107], v[84:87]\n"
"v_mfma_f32_16x16x32_bf16 v[84:87], a[156:159], v[108:111], v[84:87]\n"
"v_mfma_f32_16x16x32_bf16 v[88:91], a[140:143], v[104:107], v[88:91]\n"
"v_mfma_f32_16x16x32_bf16 v[88:91], a[160:163], v[108:111], v[88:91]\n"
"v_mfma_f32_16x16x32_bf16 v[92:95], a[144:147], v[104:107], v[92:95]\n"
"v_mfma_f32_16x16x32_bf16 v[92:95], a[164:167], v[108:111], v[92:95]\n"
"s_waitcnt lgkmcnt(0)\n"
"v_mfma_f32_16x16x32_bf16 v[16:19], a[168:171], v[96:99], v[16:19]\n"
"ds_read_b64_tr_b16 v[104:105], v161 offset:0\n"
"v_mfma_f32_16x16x32_bf16 v[16:19], a[188:191], v[100:103], v[16:19]\n"
"ds_read_b64_tr_b16 v[106:107], v161 offset:512\n"
"v_mfma_f32_16x16x32_bf16 v[20:23], a[172:175], v[96:99], v[20:23]\n"
"ds_read_b64_tr_b16 v[108:109], v161 offset:4096\n"
"v_mfma_f32_16x16x32_bf16 v[20:23], a[208:211], v[100:103], v[20:23]\n"
"ds_read_b64_tr_b16 v[110:111], v161 offset:4608\n"
"v_mfma_f32_16x16x32_bf16 v[24:27], a[176:179], v[96:99], v[24:27]\n"
"v_mfma_f32_16x16x32_bf16 v[24:27], a[212:215], v[100:103], v[24:27]\n"
"v_mfma_f32_16x16x32_bf16 v[28:31], a[180:183], v[96:99], v[28:31]\n"
"v_mfma_f32_16x16x32_bf16 v[28:31], a[216:219], v[100:103], v[28:31]\n"
"v_mfma_f32_16x16x32_bf16 v[32:35], a[184:187], v[96:99], v[32:35]\n"
"v_mfma_f32_16x16x32_bf16 v[32:35], a[220:223], v[100:103], v[32:35]\n"
"s_waitcnt lgkmcnt(0)\n"
"v_mfma_f32_16x16x32_bf16 v[36:39], a[168:171], v[104:107], v[36:39]\n"
"ds_read_b64_tr_b16 v[96:97], v162 offset:0\n"
"v_mfma_f32_16x16x32_bf16 v[36:39], a[188:191], v[108:111], v[36:39]\n"
"ds_read_b64_tr_b16 v[98:99], v162 offset:512\n"
"v_mfma_f32_16x16x32_bf16 v[40:43], a[172:175], v[104:107], v[40:43]\n"
"ds_read_b64_tr_b16 v[100:101], v162 offset:4096\n"
"v_mfma_f32_16x16x32_bf16 v[40:43], a[208:211], v[108:111], v[40:43]\n"
"ds_read_b64_tr_b16 v[102:103], v162 offset:4608\n"
"v_mfma_f32_16x16x32_bf16 v[44:47], a[176:179], v[104:107], v[44:47]\n"
"v_mfma_f32_16x16x32_bf16 v[44:47], a[212:215], v[108:111], v[44:47]\n"
"v_mfma_f32_16x16x32_bf16 v[48:51], a[180:183], v[104:107], v[48:51]\n"
"v_mfma_f32_16x16x32_bf16 v[48:51], a[216:219], v[108:111], v[48:51]\n"
"v_mfma_f32_16x16x32_bf16 v[52:55], a[184:187], v[104:107], v[52:55]\n"
"v_mfma_f32_16x16x32_bf16 v[52:55], a[220:223], v[108:111], v[52:55]\n"
"s_waitcnt lgkmcnt(0)\n"
"v_mfma_f32_16x16x32_bf16 v[56:59], a[168:171], v[96:99], v[56:59]\n"
"ds_read_b64_tr_b16 v[104:105], v163 offset:0\n"
"v_mfma_f32_16x16x32_bf16 v[56:59], a[188:191], v[100:103], v[56:59]\n"
"ds_read_b64_tr_b16 v[106:107], v163 offset:512\n"
"v_mfma_f32_16x16x32_bf16 v[60:63], a[172:175], v[96:99], v[60:63]\n"
"ds_read_b64_tr_b16 v[108:109], v163 offset:4096\n"
"v_mfma_f32_16x16x32_bf16 v[60:63], a[208:211], v[100:103], v[60:63]\n"
"ds_read_b64_tr_b16 v[110:111], v163 offset:4608\n"
"v_mfma_f32_16x16x32_bf16 v[64:67], a[176:179], v[96:99], v[64:67]\n"
"v_mfma_f32_16x16x32_bf16 v[64:67], a[212:215], v[100:103], v[64:67]\n"
"v_mfma_f32_16x16x32_bf16 v[68:71], a[180:183], v[96:99], v[68:71]\n"
"v_mfma_f32_16x16x32_bf16 v[68:71], a[216:219], v[100:103], v[68:71]\n"
"v_mfma_f32_16x16x32_bf16 v[72:75], a[184:187], v[96:99], v[72:75]\n"
"v_mfma_f32_16x16x32_bf16 v[72:75], a[220:223], v[100:103], v[72:75]\n"
"s_waitcnt lgkmcnt(0)\n"
"v_mfma_f32_16x16x32_bf16 v[76:79], a[168:171], v[104:107], v[76:79]\n"
"s_waitcnt vmcnt(6)\n"
"s_waitcnt vmcnt(20)\n"
"v_cmp_ne_u64 vcc, s[34:35], v[6:7]\n"
"s_nop 0\n"
"s_cmp_eq_u64 vcc, 0\n"
"s_cbranch_scc0 L_slow_pollS_%=\n"
"L_back_pollS_%=:\n"
"ds_read_b64_tr_b16 v[96:97], v160 offset:32768\n"
"buffer_load_dwordx4 v[208:211], v175, s[36:39], s92 offen offset:0 sc1\n"
"v_mfma_f32_16x16x32_bf16 v[76:79], a[188:191], v[108:111], v[76:79]\n"
"ds_read_b64_tr_b16 v[98:99], v160 offset:33280\n"
"buffer_load_dwordx4 v[212:215], v175, s[36:39], s92 offen offset:1024 sc1\n"
"v_mfma_f32_16x16x32_bf16 v[80:83], a[172:175], v[104:107], v[80:83]\n"
"ds_read_b64_tr_b16 v[100:101], v160 offset:36864\n"
"buffer_load_dwordx2 v[216:217], v13, s[36:39], s92 offen offset:2048 sc1\n"
"v_mfma_f32_16x16x32_bf16 v[80:83], a[208:211], v[108:111], v[80:83]\n"
"ds_read_b64_tr_b16 v[102:103], v160 offset:37376\n"
"buffer_load_dwordx4 v[220:223], v175, s[36:39], s93 offen offset:0 sc1\n"
"v_mfma_f32_16x16x32_bf16 v[84:87], a[176:179], v[104:107], v[84:87]\n"
"buffer_load_dwordx4 v[224:227], v175, s[36:39], s93 offen offset:1024 sc1\n"
"v_mfma_f32_16x16x32_bf16 v[84:87], a[212:215], v[108:111], v[84:87]\n"
"buffer_load_dwordx2 v[228:229], v13, s[36:39], s93 offen offset:2048 sc1\n"
"v_mfma_f32_16x16x32_bf16 v[88:91], a[180:183], v[104:107], v[88:91]\n"
"buffer_load_dwordx4 v[232:235], v175, s[36:39], s94 offen offset:0 sc1\n"
"v_mfma_f32_16x16x32_bf16 v[88:91], a[216:219], v[108:111], v[88:91]\n"
"buffer_load_dwordx4 v[236:239], v175, s[36:39], s94 offen offset:1024 sc1\n"
"v_mfma_f32_16x16x32_bf16 v[92:95], a[184:187], v[104:107], v[92:95]\n"
"buffer_load_dwordx2 v[240:241], v13, s[36:39], s94 offen offset:2048 sc1\n"
"v_mfma_f32_16x16x32_bf16 v[92:95], a[220:223], v[108:111], v[92:95]\n"
"s_waitcnt lgkmcnt(0)\n"
"s_barrier\n"
"s_and_b32 s95, s61, 16384\n"
"s_add_u32 s95, s95, s61\n"
"s_lshl_b32 s95, s95, 1\n"
"v_add_u32 v7, s95, v175\n"
"v_mfma_f32_16x16x32_bf16 v[16:19], a[224:227], v[96:99], v[16:19]\n"
"s_add_u32 s40, s26, s65\n"
"s_addc_u32 s41, s27, 0\n"
"s_mov_b32 s42, 20480\n"
"ds_read_b64_tr_b16 v[104:105], v161 offset:32768\n"
"v_mfma_f32_16x16x32_bf16 v[16:19], a[244:247], v[100:103], v[16:19]\n"
"s_mov_b32 s43, 0x20000\n"
"ds_read_b64_tr_b16 v[106:107], v161 offset:33280\n"
"ds_read_b128 v[176:179], v1 offset:0\n"
"v_mfma_f32_16x16x32_bf16 v[20:23], a[228:231], v[96:99], v[20:23]\n"
"ds_read_b64_tr_b16 v[108:109], v161 offset:36864\n"
"ds_read_b128 v[180:183], v1 offset:1024\n"
"v_mfma_f32_16x16x32_bf16 v[20:23], a[248:251], v[100:103], v[20:23]\n"
"ds_read_b64_tr_b16 v[110:111], v161 offset:37376\n"
"ds_read_b128 v[184:187], v1 offset:2048\n"
"v_mfma_f32_16x16x32_bf16 v[24:27], a[232:235], v[96:99], v[24:27]\n"
"ds_read_b128 v[188:191], v1 offset:3072\n"
"ds_read_b128 v[192:195], v1 offset:4096\n"
"v_mfma_f32_16x16x32_bf16 v[24:27], a[252:255], v[100:103], v[24:27]\n"
"v_mfma_f32_16x16x32_bf16 v[28:31], a[236:239], v[96:99], v[28:31]\n"
"v_mfma_f32_16x16x32_bf16 v[28:31], a[192:195], v[100:103], v[28:31]\n"
"v_mfma_f32_16x16x32_bf16 v[32:35], a[240:243], v[96:99], v[32:35]\n"
"v_mfma_f32_16x16x32_bf16 v[32:35], a[196:199], v[100:103], v[32:35]\n"
"s_waitcnt lgkmcnt(0)\n"
"v_mfma_f32_16x16x32_bf16 v[36:39], a[224:227], v[104:107], v[36:39]\n"
"ds_read_b64_tr_b16 v[96:97], v162 offset:32768\n"
"v_mfma_f32_16x16x32_bf16 v[36:39], a[244:247], v[108:111], v[36:39]\n"
"ds_read_b64_tr_b16 v[98:99], v162 offset:33280\n"
"v_mfma_f32_16x16x32_bf16 v[40:43], a[228:231], v[104:107], v[40:43]\n"
"ds_read_b64_tr_b16 v[100:101], v162 offset:36864\n"
"v_mfma_f32_16x16x32_bf16 v[40:43], a[248:251], v[108:111], v[40:43]\n"
"ds_read_b64_tr_b16 v[102:103], v162 offset:37376\n"
"v_mfma_f32_16x16x32_bf16 v[44:47], a[232:235], v[104:107], v[44:47]\n"
"ds_write_b128 v7, v[16:19] offset:0\n"
"v_mfma_f32_16x16x32_bf16 v[44:47], a[252:255], v[108:111], v[44:47]\n"
"ds_write_b128 v7, v[20:23] offset:1024\n"
"v_mfma_f32_16x16x32_bf16 v[48:51], a[236:239], v[104:107], v[48:51]\n"
"ds_write_b128 v7, v[24:27] offset:2048\n"
"v_mfma_f32_16x16x32_bf16 v[48:51], a[192:195], v[108:111], v[48:51]\n"
"v_mfma_f32_16x16x32_bf16 v[52:55], a[240:243], v[104:107], v[52:55]\n"
"ds_write_b128 v7, v[28:31] offset:3072\n"
"v_mfma_f32_16x16x32_bf16 v[52:55], a[196:199], v[108:111], v[52:55]\n"
"s_waitcnt lgkmcnt(4)\n"
"ds_write_b128 v7, v[32:35] offset:4096\n"
"v_mfma_f32_16x16x32_bf16 v[56:59], a[224:227], v[96:99], v[56:59]\n"
"ds_read_b64_tr_b16 v[104:105], v163 offset:32768\n"
"v_mfma_f32_16x16x32_bf16 v[56:59], a[244:247], v[100:103], v[56:59]\n"
"ds_read_b64_tr_b16 v[106:107], v163 offset:33280\n"
"v_mfma_f32_16x16x32_bf16 v[60:63], a[228:231], v[96:99], v[60:63]\n"
"ds_read_b64_tr_b16 v[108:109], v163 offset:36864\n"
"v_mfma_f32_16x16x32_bf16 v[60:63], a[248:251], v[100:103], v[60:63]\n"
"ds_read_b64_tr_b16 v[110:111], v163 offset:37376\n"
"v_mfma_f32_16x16x32_bf16 v[64:67], a[232:235], v[96:99], v[64:67]\n"
"ds_write_b128 v7, v[36:39] offset:5120\n"
"v_mfma_f32_16x16x32_bf16 v[64:67], a[252:255], v[100:103], v[64:67]\n"
"ds_write_b128 v7, v[40:43] offset:6144\n"
"v_mfma_f32_16x16x32_bf16 v[68:71], a[236:239], v[96:99], v[68:71]\n"
"ds_write_b128 v7, v[44:47] offset:7168\n"
"v_mfma_f32_16x16x32_bf16 v[68:71], a[192:195], v[100:103], v[68:71]\n"
"v_mfma_f32_16x16x32_bf16 v[72:75], a[240:243], v[96:99], v[72:75]\n"
"ds_write_b128 v7, v[48:51] offset:8192\n"
"v_mfma_f32_16x16x32_bf16 v[72:75], a[196:199], v[100:103], v[72:75]\n"
"s_waitcnt lgkmcnt(4)\n"
"ds_write_b128 v7, v[52:55] offset:9216\n"
"v_mfma_f32_16x16x32_bf16 v[76:79], a[224:227], v[104:107], v[76:79]\n"
"v_mfma_f32_16x16x32_bf16 v[76:79], a[244:247], v[108:111], v[76:79]\n"
"ds_write_b128 v7, v[56:59] offset:10240\n"
"v_mfma_f32_16x16x32_bf16 v[80:83], a[228:231], v[104:107], v[80:83]\n"
"v_mfma_f32_16x16x32_bf16 v[80:83], a[248:251], v[108:111], v[80:83]\n"
"ds_write_b128 v7, v[60:63] offset:11264\n"
"v_mfma_f32_16x16x32_bf16 v[84:87], a[232:235], v[104:107], v[84:87]\n"
"ds_write_b128 v7, v[64:67] offset:12288\n"
"v_mfma_f32_16x16x32_bf16 v[84:87], a[252:255], v[108:111], v[84:87]\n"
"v_mfma_f32_16x16x32_bf16 v[88:91], a[236:239], v[104:107], v[88:91]\n"
"ds_write_b128 v7, v[68:71] offset:13312\n"
"v_mfma_f32_16x16x32_bf16 v[88:91], a[192:195], v[108:111], v[88:91]\n"
"ds_write_b128 v7, v[72:75] offset:14336\n"
"v_mfma_f32_16x16x32_bf16 v[92:95], a[240:243], v[104:107], v[92:95]\n"
"v_mfma_f32_16x16x32_bf16 v[92:95], a[196:199], v[108:111], v[92:95]\n"
"s_waitcnt lgkmcnt(0)\n"
"s_barrier\n"
"ds_read_b128 v[16:19], v152 offset:0\n"
"ds_read_b128 v[20:23], v152 offset:1024\n"
"ds_read_b128 v[24:27], v152 offset:2048\n"
"ds_read_b128 v[28:31], v152 offset:3072\n"
"ds_read_b128 v[32:35], v152 offset:4096\n"
"ds_read_b128 v[36:39], v153 offset:0\n"
"ds_read_b128 v[40:43], v153 offset:1024\n"
"ds_read_b128 v[44:47], v153 offset:2048\n"
"ds_read_b128 v[48:51], v153 offset:3072\n"
"ds_read_b128 v[52:55], v153 offset:4096\n"
"ds_read_b128 v[56:59], v154 offset:0\n"
"ds_read_b128 v[60:63], v154 offset:1024\n"
"ds_read_b128 v[64:67], v154 offset:2048\n"
"ds_read_b128 v[68:71], v154 offset:3072\n"
"ds_read_b128 v[72:75], v154 offset:4096\n"
"s_waitcnt vmcnt(0)\n"
"v_lshlrev_b32 v196, 16, v208\n"
"v_and_b32 v197, 0xffff0000, v208\n"
"v_lshlrev_b32 v198, 16, v209\n"
"v_and_b32 v199, 0xffff0000, v209\n"
"v_pk_add_f32 v[176:177], v[176:177], v[196:197]\n"
"v_pk_add_f32 v[178:179], v[178:179], v[198:199]\n"
"v_lshlrev_b32 v200, 16, v220\n"
"v_and_b32 v201, 0xffff0000, v220\n"
"v_lshlrev_b32 v202, 16, v221\n"
"v_and_b32 v203, 0xffff0000, v221\n"
"v_pk_add_f32 v[176:177], v[176:177], v[200:201]\n"
"v_pk_add_f32 v[178:179], v[178:179], v[202:203]\n"
"v_lshlrev_b32 v204, 16, v232\n"
"v_and_b32 v205, 0xffff0000, v232\n"
"v_lshlrev_b32 v206, 16, v233\n"
"v_and_b32 v207, 0xffff0000, v233\n"
"v_pk_add_f32 v[176:177], v[176:177], v[204:205]\n"
"v_pk_add_f32 v[178:179], v[178:179], v[206:207]\n"
"v_lshlrev_b32 v196, 16, v210\n"
"v_and_b32 v197, 0xffff0000, v210\n"
"v_lshlrev_b32 v198, 16, v211\n"
"v_and_b32 v199, 0xffff0000, v211\n"
"v_pk_add_f32 v[180:181], v[180:181], v[196:197]\n"
"v_pk_add_f32 v[182:183], v[182:183], v[198:199]\n"
"v_lshlrev_b32 v200, 16, v222\n"
"v_and_b32 v201, 0xffff0000, v222\n"
"v_lshlrev_b32 v202, 16, v223\n"
"v_and_b32 v203, 0xffff0000, v223\n"
"v_pk_add_f32 v[180:181], v[180:181], v[200:201]\n"
"v_pk_add_f32 v[182:183], v[182:183], v[202:203]\n"
"v_lshlrev_b32 v204, 16, v234\n"
"v_and_b32 v205, 0xffff0000, v234\n"
"v_lshlrev_b32 v206, 16, v235\n"
"v_and_b32 v207, 0xffff0000, v235\n"
"v_pk_add_f32 v[180:181], v[180:181], v[204:205]\n"
"v_pk_add_f32 v[182:183], v[182:183], v[206:207]\n"
"v_lshlrev_b32 v196, 16, v212\n"
"v_and_b32 v197, 0xffff0000, v212\n"
"v_lshlrev_b32 v198, 16, v213\n"
"v_and_b32 v199, 0xffff0000, v213\n"
"v_pk_add_f32 v[184:185], v[184:185], v[196:197]\n"
"v_pk_add_f32 v[186:187], v[186:187], v[198:199]\n"
"v_lshlrev_b32 v200, 16, v224\n"
"v_and_b32 v201, 0xffff0000, v224\n"
"v_lshlrev_b32 v202, 16, v225\n"
"v_and_b32 v203, 0xffff0000, v225\n"
"v_pk_add_f32 v[184:185], v[184:185], v[200:201]\n"
"v_pk_add_f32 v[186:187], v[186:187], v[202:203]\n"
"v_lshlrev_b32 v204, 16, v236\n"
"v_and_b32 v205, 0xffff0000, v236\n"
"v_lshlrev_b32 v206, 16, v237\n"
"v_and_b32 v207, 0xffff0000, v237\n"
"v_pk_add_f32 v[184:185], v[184:185], v[204:205]\n"
"v_pk_add_f32 v[186:187], v[186:187], v[206:207]\n"
"v_lshlrev_b32 v196, 16, v214\n"
"v_and_b32 v197, 0xffff0000, v214\n"
"v_lshlrev_b32 v198, 16, v215\n"
"v_and_b32 v199, 0xffff0000, v215\n"
"v_pk_add_f32 v[188:189], v[188:189], v[196:197]\n"
"v_pk_add_f32 v[190:191], v[190:191], v[198:199]\n"
"v_lshlrev_b32 v200, 16, v226\n"
"v_and_b32 v201, 0xffff0000, v226\n"
"v_lshlrev_b32 v202, 16, v227\n"
"v_and_b32 v203, 0xffff0000, v227\n"
"v_pk_add_f32 v[188:189], v[188:189], v[200:201]\n"
"v_pk_add_f32 v[190:191], v[190:191], v[202:203]\n"
"v_lshlrev_b32 v204, 16, v238\n"
"v_and_b32 v205, 0xffff0000, v238\n"
"v_lshlrev_b32 v206, 16, v239\n"
"v_and_b32 v207, 0xffff0000, v239\n"
"v_pk_add_f32 v[188:189], v[188:189], v[204:205]\n"
"v_pk_add_f32 v[190:191], v[190:191], v[206:207]\n"
"v_lshlrev_b32 v196, 16, v216\n"
"v_and_b32 v197, 0xffff0000, v216\n"
"v_lshlrev_b32 v198, 16, v217\n"
"v_and_b32 v199, 0xffff0000, v217\n"
"v_pk_add_f32 v[192:193], v[192:193], v[196:197]\n"
"v_pk_add_f32 v[194:195], v[194:195], v[198:199]\n"
"v_lshlrev_b32 v200, 16, v228\n"
"v_and_b32 v201, 0xffff0000, v228\n"
"v_lshlrev_b32 v202, 16, v229\n"
"v_and_b32 v203, 0xffff0000, v229\n"
"v_pk_add_f32 v[192:193], v[192:193], v[200:201]\n"
"v_pk_add_f32 v[194:195], v[194:195], v[202:203]\n"
"v_lshlrev_b32 v204, 16, v240\n"
"v_and_b32 v205, 0xffff0000, v240\n"
"v_lshlrev_b32 v206, 16, v241\n"
"v_and_b32 v207, 0xffff0000, v241\n"
"v_pk_add_f32 v[192:193], v[192:193], v[204:205]\n"
"v_pk_add_f32 v[194:195], v[194:195], v[206:207]\n"
"v_lshlrev_b32 v254, 16, v254\n"
"s_mov_b32 s92, 0xbfb8aa3b\n"
"s_waitcnt lgkmcnt(0)\n"
"v_pk_add_f32 v[76:77], v[76:77], v[16:17]\n"
"v_pk_add_f32 v[78:79], v[78:79], v[18:19]\n"
"v_pk_add_f32 v[36:37], v[36:37], v[56:57]\n"
"v_pk_add_f32 v[38:39], v[38:39], v[58:59]\n"
"v_pk_add_f32 v[76:77], v[76:77], v[36:37]\n"
"v_pk_add_f32 v[78:79], v[78:79], v[38:39]\n"
"v_add_f32 v76, v76, v254\n"
"v_add_f32 v77, v77, v254\n"
"v_add_f32 v78, v78, v254\n"
"v_add_f32 v79, v79, v254\n"
"v_mul_f32 v196, s92, v76\n"
"v_mul_f32 v197, s92, v77\n"
"v_mul_f32 v198, s92, v78\n"
"v_mul_f32 v199, s92, v79\n"
"v_exp_f32 v196, v196\n"
"v_exp_f32 v197, v197\n"
"v_exp_f32 v198, v198\n"
"v_exp_f32 v199, v199\n"
"v_pk_mul_f32 v[76:77], v[76:77], v[176:177]\n"
"v_pk_mul_f32 v[78:79], v[78:79], v[178:179]\n"
"v_lshlrev_b32 v176, 16, v244\n"
"v_and_b32 v177, 0xffff0000, v244\n"
"v_lshlrev_b32 v178, 16, v245\n"
"v_and_b32 v179, 0xffff0000, v245\n"
"v_add_f32 v196, 1.0, v196\n"
"v_add_f32 v197, 1.0, v197\n"
"v_add_f32 v198, 1.0, v198\n"
"v_add_f32 v199, 1.0, v199\n"
"v_rcp_f32 v196, v196\n"
"v_rcp_f32 v197, v197\n"
"v_rcp_f32 v198, v198\n"
"v_rcp_f32 v199, v199\n"
"s_nop 0\n"
"v_pk_fma_f32 v[76:77], v[76:77], v[196:197], v[176:177]\n"
"v_pk_fma_f32 v[78:79], v[78:79], v[198:199], v[178:179]\n"
"v_cvt_pk_bf16_f32 v196, v76, v77\n"
"v_cvt_pk_bf16_f32 v197, v78, v79\n"
"buffer_store_dwordx2 v[196:197], v11, s[40:43], 0 offen offset:0 sc1 nt\n"
"v_pk_add_f32 v[80:81], v[80:81], v[20:21]\n"
"v_pk_add_f32 v[82:83], v[82:83], v[22:23]\n"
"v_pk_add_f32 v[40:41], v[40:41], v[60:61]\n"
"v_pk_add_f32 v[42:43], v[42:43], v[62:63]\n"
"v_pk_add_f32 v[80:81], v[80:81], v[40:41]\n"
"v_pk_add_f32 v[82:83], v[82:83], v[42:43]\n"
"v_add_f32 v80, v80, v254\n"
"v_add_f32 v81, v81, v254\n"
"v_add_f32 v82, v82, v254\n"
"v_add_f32 v83, v83, v254\n"
"v_mul_f32 v200, s92, v80\n"
"v_mul_f32 v201, s92, v81\n"
"v_mul_f32 v202, s92, v82\n"
"v_mul_f32 v203, s92, v83\n"
"v_exp_f32 v200, v200\n"
"v_exp_f32 v201, v201\n"
"v_exp_f32 v202, v202\n"
"v_exp_f32 v203, v203\n"
"v_pk_mul_f32 v[80:81], v[80:81], v[180:181]\n"
"v_pk_mul_f32 v[82:83], v[82:83], v[182:183]\n"
"v_lshlrev_b32 v180, 16, v246\n"
"v_and_b32 v181, 0xffff0000, v246\n"
"v_lshlrev_b32 v182, 16, v247\n"
"v_and_b32 v183, 0xffff0000, v247\n"
"v_add_f32 v200, 1.0, v200\n"
"v_add_f32 v201, 1.0, v201\n"
"v_add_f32 v202, 1.0, v202\n"
"v_add_f32 v203, 1.0, v203\n"
"v_rcp_f32 v200, v200\n"
"v_rcp_f32 v201, v201\n"
"v_rcp_f32 v202, v202\n"
"v_rcp_f32 v203, v203\n"
"s_nop 0\n"
"v_pk_fma_f32 v[80:81], v[80:81], v[200:201], v[180:181]\n"
"v_pk_fma_f32 v[82:83], v[82:83], v[202:203], v[182:183]\n"
"v_cvt_pk_bf16_f32 v200, v80, v81\n"
"v_cvt_pk_bf16_f32 v201, v82, v83\n"
"buffer_store_dwordx2 v[200:201], v11, s[40:43], 0 offen offset:32 sc1 nt\n"
"v_pk_add_f32 v[84:85], v[84:85], v[24:25]\n"
"v_pk_add_f32 v[86:87], v[86:87], v[26:27]\n"
"v_pk_add_f32 v[44:45], v[44:45], v[64:65]\n"
"v_pk_add_f32 v[46:47], v[46:47], v[66:67]\n"
"v_pk_add_f32 v[84:85], v[84:85], v[44:45]\n"
"v_pk_add_f32 v[86:87], v[86:87], v[46:47]\n"
"v_add_f32 v84, v84, v254\n"
"v_add_f32 v85, v85, v254\n"
"v_add_f32 v86, v86, v254\n"
"v_add_f32 v87, v87, v254\n"
"v_mul_f32 v196, s92, v84\n"
"v_mul_f32 v197, s92, v85\n"
"v_mul_f32 v198, s92, v86\n"
"v_mul_f32 v199, s92, v87\n"
"v_exp_f32 v196, v196\n"
"v_exp_f32 v197, v197\n"
"v_exp_f32 v198, v198\n"
"v_exp_f32 v199, v199\n"
"v_pk_mul_f32 v[84:85], v[84:85], v[184:185]\n"
"v_pk_mul_f32 v[86:87], v[86:87], v[186:187]\n"
"v_lshlrev_b32 v184, 16, v248\n"
"v_and_b32 v185, 0xffff0000, v248\n"
"v_lshlrev_b32 v186, 16, v249\n"
"v_and_b32 v187, 0xffff0000, v249\n"
"v_add_f32 v196, 1.0, v196\n"
"v_add_f32 v197, 1.0, v197\n"
"v_add_f32 v198, 1.0, v198\n"
"v_add_f32 v199, 1.0, v199\n"
"v_rcp_f32 v196, v196\n"
"v_rcp_f32 v197, v197\n"
"v_rcp_f32 v198, v198\n"
"v_rcp_f32 v199, v199\n"
"s_nop 0\n"
"v_pk_fma_f32 v[84:85], v[84:85], v[196:197], v[184:185]\n"
"v_pk_fma_f32 v[86:87], v[86:87], v[198:199], v[186:187]\n"
"v_cvt_pk_bf16_f32 v196, v84, v85\n"
"v_cvt_pk_bf16_f32 v197, v86, v87\n"
"buffer_store_dwordx2 v[196:197], v11, s[40:43], 0 offen offset:64 sc1 nt\n"
"v_pk_add_f32 v[88:89], v[88:89], v[28:29]\n"
"v_pk_add_f32 v[90:91], v[90:91], v[30:31]\n"
"v_pk_add_f32 v[48:49], v[48:49], v[68:69]\n"
"v_pk_add_f32 v[50:51], v[50:51], v[70:71]\n"
"v_pk_add_f32 v[88:89], v[88:89], v[48:49]\n"
"v_pk_add_f32 v[90:91], v[90:91], v[50:51]\n"
"v_add_f32 v88, v88, v254\n"
"v_add_f32 v89, v89, v254\n"
"v_add_f32 v90, v90, v254\n"
"v_add_f32 v91, v91, v254\n"
"v_mul_f32 v200, s92, v88\n"
"v_mul_f32 v201, s92, v89\n"
"v_mul_f32 v202, s92, v90\n"
"v_mul_f32 v203, s92, v91\n"
"v_exp_f32 v200, v200\n"
"v_exp_f32 v201, v201\n"
"v_exp_f32 v202, v202\n"
"v_exp_f32 v203, v203\n"
"v_pk_mul_f32 v[88:89], v[88:89], v[188:189]\n"
"v_pk_mul_f32 v[90:91], v[90:91], v[190:191]\n"
"v_lshlrev_b32 v188, 16, v250\n"
"v_and_b32 v189, 0xffff0000, v250\n"
"v_lshlrev_b32 v190, 16, v251\n"
"v_and_b32 v191, 0xffff0000, v251\n"
"v_add_f32 v200, 1.0, v200\n"
"v_add_f32 v201, 1.0, v201\n"
"v_add_f32 v202, 1.0, v202\n"
"v_add_f32 v203, 1.0, v203\n"
"v_rcp_f32 v200, v200\n"
"v_rcp_f32 v201, v201\n"
"v_rcp_f32 v202, v202\n"
"v_rcp_f32 v203, v203\n"
"s_nop 0\n"
"v_pk_fma_f32 v[88:89], v[88:89], v[200:201], v[188:189]\n"
"v_pk_fma_f32 v[90:91], v[90:91], v[202:203], v[190:191]\n"
"v_cvt_pk_bf16_f32 v200, v88, v89\n"
"v_cvt_pk_bf16_f32 v201, v90, v91\n"
"buffer_store_dwordx2 v[200:201], v11, s[40:43], 0 offen offset:96 sc1 nt\n"
"v_pk_add_f32 v[92:93], v[92:93], v[32:33]\n"
"v_pk_add_f32 v[94:95], v[94:95], v[34:35]\n"
"v_pk_add_f32 v[52:53], v[52:53], v[72:73]\n"
"v_pk_add_f32 v[54:55], v[54:55], v[74:75]\n"
"v_pk_add_f32 v[92:93], v[92:93], v[52:53]\n"
"v_pk_add_f32 v[94:95], v[94:95], v[54:55]\n"
"v_add_f32 v92, v92, v254\n"
"v_add_f32 v93, v93, v254\n"
"v_add_f32 v94, v94, v254\n"
"v_add_f32 v95, v95, v254\n"
"v_mul_f32 v196, s92, v92\n"
"v_mul_f32 v197, s92, v93\n"
"v_mul_f32 v198, s92, v94\n"
"v_mul_f32 v199, s92, v95\n"
"v_exp_f32 v196, v196\n"
"v_exp_f32 v197, v197\n"
"v_exp_f32 v198, v198\n"
"v_exp_f32 v199, v199\n"
"v_pk_mul_f32 v[92:93], v[92:93], v[192:193]\n"
"v_pk_mul_f32 v[94:95], v[94:95], v[194:195]\n"
"v_lshlrev_b32 v192, 16, v252\n"
"v_and_b32 v193, 0xffff0000, v252\n"
"v_lshlrev_b32 v194, 16, v253\n"
"v_and_b32 v195, 0xffff0000, v253\n"
"v_add_f32 v196, 1.0, v196\n"
"v_add_f32 v197, 1.0, v197\n"
"v_add_f32 v198, 1.0, v198\n"
"v_add_f32 v199, 1.0, v199\n"
"v_rcp_f32 v196, v196\n"
"v_rcp_f32 v197, v197\n"
"v_rcp_f32 v198, v198\n"
"v_rcp_f32 v199, v199\n"
"s_nop 0\n"
"v_pk_fma_f32 v[92:93], v[92:93], v[196:197], v[192:193]\n"
"v_pk_fma_f32 v[94:95], v[94:95], v[198:199], v[194:195]\n"
"v_cvt_pk_bf16_f32 v196, v92, v93\n"
"v_cvt_pk_bf16_f32 v197, v94, v95\n"
"buffer_store_dwordx2 v[196:197], v11, s[40:43], 0 offen offset:128 sc1 nt\n"
"s_endpgm\n"
"L_fallback_%=:\n"
"s_waitcnt vmcnt(0)\n"
"s_barrier\n"
"s_mov_b32 s66, 1\n"
"s_mov_b32 s56, 0\n"
"s_mov_b32 s63, 32\n"
"v_mov_b32 v8, 0xff800000\n"
"v_mov_b32 v9, 0xff800000\n"
"v_mov_b32 v10, 0xff800000\n"
"v_mov_b32 v11, 0xff800000\n"
"s_add_u32 m0, s64, 0\n"
"s_nop 0\n"
"L_fb_loop_%=:\n"
"buffer_load_dwordx4 v161, s[36:39], s56 offen offset:0 lds\n"
"buffer_load_dwordx4 v162, s[36:39], s56 offen offset:1024 lds\n"
"buffer_load_dwordx4 v163, s[36:39], s56 offen offset:2048 lds\n"
"buffer_load_dwordx4 v164, s[36:39], s56 offen offset:3072 lds\n"
"s_waitcnt vmcnt(0)\n"
"s_barrier\n"
"ds_read_b128 a[192:195], v149 offset:0\n"
"ds_read_b128 a[196:199], v150 offset:0\n"
"ds_read_b128 a[200:203], v151 offset:0\n"
"ds_read_b128 a[204:207], v152 offset:0\n"
"s_waitcnt lgkmcnt(0)\n"
"v_mfma_f32_16x16x32_bf16 v[16:19], a[192:195], v[176:179], 0\n"
"v_mfma_f32_16x16x32_bf16 v[16:19], a[196:199], v[180:183], v[16:19]\n"
"v_mfma_f32_16x16x32_bf16 v[16:19], a[200:203], v[184:187], v[16:19]\n"
"v_mfma_f32_16x16x32_bf16 v[16:19], a[204:207], v[188:191], v[16:19]\n"
"v_mfma_f32_16x16x32_bf16 v[20:23], a[192:195], v[192:195], 0\n"
"v_mfma_f32_16x16x32_bf16 v[20:23], a[196:199], v[196:199], v[20:23]\n"
"v_mfma_f32_16x16x32_bf16 v[20:23], a[200:203], v[200:203], v[20:23]\n"
"v_mfma_f32_16x16x32_bf16 v[20:23], a[204:207], v[204:207], v[20:23]\n"
"v_mfma_f32_16x16x32_bf16 v[24:27], a[192:195], v[208:211], 0\n"
"v_mfma_f32_16x16x32_bf16 v[24:27], a[196:199], v[212:215], v[24:27]\n"
"v_mfma_f32_16x16x32_bf16 v[24:27], a[200:203], v[216:219], v[24:27]\n"
"v_mfma_f32_16x16x32_bf16 v[24:27], a[204:207], v[220:223], v[24:27]\n"
"v_mfma_f32_16x16x32_bf16 v[28:31], a[192:195], v[224:227], 0\n"
"v_mfma_f32_16x16x32_bf16 v[28:31], a[196:199], v[228:231], v[28:31]\n"
"v_mfma_f32_16x16x32_bf16 v[28:31], a[200:203], v[232:235], v[28:31]\n"
"v_mfma_f32_16x16x32_bf16 v[28:31], a[204:207], v[236:239], v[28:31]\n"
"s_nop 7\n"
"v_max3_f32 v8, v8, v16, v17\n"
"v_max3_f32 v8, v8, v18, v19\n"
"v_max3_f32 v9, v9, v20, v21\n"
"v_max3_f32 v9, v9, v22, v23\n"
"v_max3_f32 v10, v10, v24, v25\n"
"v_max3_f32 v10, v10, v26, v27\n"
"v_max3_f32 v11, v11, v28, v29\n"
"v_max3_f32 v11, v11, v30, v31\n"
"ds_read_b128 a[192:195], v149 offset:4096\n"
"ds_read_b128 a[196:199], v150 offset:4096\n"
"ds_read_b128 a[200:203], v151 offset:4096\n"
"ds_read_b128 a[204:207], v152 offset:4096\n"
"s_waitcnt lgkmcnt(0)\n"
"v_mfma_f32_16x16x32_bf16 v[16:19], a[192:195], v[176:179], 0\n"
"v_mfma_f32_16x16x32_bf16 v[16:19], a[196:199], v[180:183], v[16:19]\n"
"v_mfma_f32_16x16x32_bf16 v[16:19], a[200:203], v[184:187], v[16:19]\n"
"v_mfma_f32_16x16x32_bf16 v[16:19], a[204:207], v[188:191], v[16:19]\n"
"v_mfma_f32_16x16x32_bf16 v[20:23], a[192:195], v[192:195], 0\n"
"v_mfma_f32_16x16x32_bf16 v[20:23], a[196:199], v[196:199], v[20:23]\n"
"v_mfma_f32_16x16x32_bf16 v[20:23], a[200:203], v[200:203], v[20:23]\n"
"v_mfma_f32_16x16x32_bf16 v[20:23], a[204:207], v[204:207], v[20:23]\n"
"v_mfma_f32_16x16x32_bf16 v[24:27], a[192:195], v[208:211], 0\n"
"v_mfma_f32_16x16x32_bf16 v[24:27], a[196:199], v[212:215], v[24:27]\n"
"v_mfma_f32_16x16x32_bf16 v[24:27], a[200:203], v[216:219], v[24:27]\n"
"v_mfma_f32_16x16x32_bf16 v[24:27], a[204:207], v[220:223], v[24:27]\n"
"v_mfma_f32_16x16x32_bf16 v[28:31], a[192:195], v[224:227], 0\n"
"v_mfma_f32_16x16x32_bf16 v[28:31], a[196:199], v[228:231], v[28:31]\n"
"v_mfma_f32_16x16x32_bf16 v[28:31], a[200:203], v[232:235], v[28:31]\n"
"v_mfma_f32_16x16x32_bf16 v[28:31], a[204:207], v[236:239], v[28:31]\n"
"s_nop 7\n"
"v_max3_f32 v8, v8, v16, v17\n"
"v_max3_f32 v8, v8, v18, v19\n"
"v_max3_f32 v9, v9, v20, v21\n"
"v_max3_f32 v9, v9, v22, v23\n"
"v_max3_f32 v10, v10, v24, v25\n"
"v_max3_f32 v10, v10, v26, v27\n"
"v_max3_f32 v11, v11, v28, v29\n"
"v_max3_f32 v11, v11, v30, v31\n"
"ds_read_b128 a[192:195], v149 offset:8192\n"
"ds_read_b128 a[196:199], v150 offset:8192\n"
"ds_read_b128 a[200:203], v151 offset:8192\n"
"ds_read_b128 a[204:207], v152 offset:8192\n"
"s_waitcnt lgkmcnt(0)\n"
"v_mfma_f32_16x16x32_bf16 v[16:19], a[192:195], v[176:179], 0\n"
"v_mfma_f32_16x16x32_bf16 v[16:19], a[196:199], v[180:183], v[16:19]\n"
"v_mfma_f32_16x16x32_bf16 v[16:19], a[200:203], v[184:187], v[16:19]\n"
"v_mfma_f32_16x16x32_bf16 v[16:19], a[204:207], v[188:191], v[16:19]\n"
"v_mfma_f32_16x16x32_bf16 v[20:23], a[192:195], v[192:195], 0\n"
"v_mfma_f32_16x16x32_bf16 v[20:23], a[196:199], v[196:199], v[20:23]\n"
"v_mfma_f32_16x16x32_bf16 v[20:23], a[200:203], v[200:203], v[20:23]\n"
"v_mfma_f32_16x16x32_bf16 v[20:23], a[204:207], v[204:207], v[20:23]\n"
"v_mfma_f32_16x16x32_bf16 v[24:27], a[192:195], v[208:211], 0\n"
"v_mfma_f32_16x16x32_bf16 v[24:27], a[196:199], v[212:215], v[24:27]\n"
"v_mfma_f32_16x16x32_bf16 v[24:27], a[200:203], v[216:219], v[24:27]\n"
"v_mfma_f32_16x16x32_bf16 v[24:27], a[204:207], v[220:223], v[24:27]\n"
"v_mfma_f32_16x16x32_bf16 v[28:31], a[192:195], v[224:227], 0\n"
"v_mfma_f32_16x16x32_bf16 v[28:31], a[196:199], v[228:231], v[28:31]\n"
"v_mfma_f32_16x16x32_bf16 v[28:31], a[200:203], v[232:235], v[28:31]\n"
"v_mfma_f32_16x16x32_bf16 v[28:31], a[204:207], v[236:239], v[28:31]\n"
"s_nop 7\n"
"v_max3_f32 v8, v8, v16, v17\n"
"v_max3_f32 v8, v8, v18, v19\n"
"v_max3_f32 v9, v9, v20, v21\n"
"v_max3_f32 v9, v9, v22, v23\n"
"v_max3_f32 v10, v10, v24, v25\n"
"v_max3_f32 v10, v10, v26, v27\n"
"v_max3_f32 v11, v11, v28, v29\n"
"v_max3_f32 v11, v11, v30, v31\n"
"ds_read_b128 a[192:195], v149 offset:12288\n"
"ds_read_b128 a[196:199], v150 offset:12288\n"
"ds_read_b128 a[200:203], v151 offset:12288\n"
"ds_read_b128 a[204:207], v152 offset:12288\n"
"s_waitcnt lgkmcnt(0)\n"
"v_mfma_f32_16x16x32_bf16 v[16:19], a[192:195], v[176:179], 0\n"
"v_mfma_f32_16x16x32_bf16 v[16:19], a[196:199], v[180:183], v[16:19]\n"
"v_mfma_f32_16x16x32_bf16 v[16:19], a[200:203], v[184:187], v[16:19]\n"
"v_mfma_f32_16x16x32_bf16 v[16:19], a[204:207], v[188:191], v[16:19]\n"
"v_mfma_f32_16x16x32_bf16 v[20:23], a[192:195], v[192:195], 0\n"
"v_mfma_f32_16x16x32_bf16 v[20:23], a[196:199], v[196:199], v[20:23]\n"
"v_mfma_f32_16x16x32_bf16 v[20:23], a[200:203], v[200:203], v[20:23]\n"
"v_mfma_f32_16x16x32_bf16 v[20:23], a[204:207], v[204:207], v[20:23]\n"
"v_mfma_f32_16x16x32_bf16 v[24:27], a[192:195], v[208:211], 0\n"
"v_mfma_f32_16x16x32_bf16 v[24:27], a[196:199], v[212:215], v[24:27]\n"
"v_mfma_f32_16x16x32_bf16 v[24:27], a[200:203], v[216:219], v[24:27]\n"
"v_mfma_f32_16x16x32_bf16 v[24:27], a[204:207], v[220:223], v[24:27]\n"
"v_mfma_f32_16x16x32_bf16 v[28:31], a[192:195], v[224:227], 0\n"
"v_mfma_f32_16x16x32_bf16 v[28:31], a[196:199], v[228:231], v[28:31]\n"
"v_mfma_f32_16x16x32_bf16 v[28:31], a[200:203], v[232:235], v[28:31]\n"
"v_mfma_f32_16x16x32_bf16 v[28:31], a[204:207], v[236:239], v[28:31]\n"
"s_nop 7\n"
"v_max3_f32 v8, v8, v16, v17\n"
"v_max3_f32 v8, v8, v18, v19\n"
"v_max3_f32 v9, v9, v20, v21\n"
"v_max3_f32 v9, v9, v22, v23\n"
"v_max3_f32 v10, v10, v24, v25\n"
"v_max3_f32 v10, v10, v26, v27\n"
"v_max3_f32 v11, v11, v28, v29\n"
"v_max3_f32 v11, v11, v30, v31\n"
"s_barrier\n"
"s_add_u32 s56, s56, 16384\n"
"s_sub_u32 s63, s63, 1\n"
"s_cmp_lg_u32 s63, 0\n"
"s_cbranch_scc1 L_fb_loop_%=\n"
"v_mov_b32 v4, v8\n"
"v_mov_b32 v5, v9\n"
"v_mov_b32 v6, v10\n"
"v_mov_b32 v7, v11\n"
"s_nop 0\n"
"v_permlane32_swap_b32 v8, v4\n"
"v_permlane32_swap_b32 v9, v5\n"
"v_permlane32_swap_b32 v10, v6\n"
"v_permlane32_swap_b32 v11, v7\n"
"v_max_f32 v8, v8, v4\n"
"v_max_f32 v9, v9, v5\n"
"v_max_f32 v10, v10, v6\n"
"v_max_f32 v11, v11, v7\n"
"v_mov_b32 v4, v8\n"
"v_mov_b32 v5, v9\n"
"v_mov_b32 v6, v10\n"
"v_mov_b32 v7, v11\n"
"s_nop 0\n"
"v_permlane16_swap_b32 v8, v4\n"
"v_permlane16_swap_b32 v9, v5\n"
"v_permlane16_swap_b32 v10, v6\n"
"v_permlane16_swap_b32 v11, v7\n"
"v_max_f32 v8, v8, v4\n"
"v_max_f32 v9, v9, v5\n"
"v_max_f32 v10, v10, v6\n"
"v_max_f32 v11, v11, v7\n"
"v_sub_f32 v112, 0, v8\n"
"v_sub_f32 v113, 0, v8\n"
"v_sub_f32 v114, 0, v8\n"
"v_sub_f32 v115, 0, v8\n"
"v_sub_f32 v116, 0, v9\n"
"v_sub_f32 v117, 0, v9\n"
"v_sub_f32 v118, 0, v9\n"
"v_sub_f32 v119, 0, v9\n"
"v_sub_f32 v120, 0, v10\n"
"v_sub_f32 v121, 0, v10\n"
"v_sub_f32 v122, 0, v10\n"
"v_sub_f32 v123, 0, v10\n"
"v_sub_f32 v124, 0, v11\n"
"v_sub_f32 v125, 0, v11\n"
"v_sub_f32 v126, 0, v11\n"
"v_sub_f32 v127, 0, v11\n"
"s_mov_b32 s56, 0\n"
"s_mov_b32 s57, 0\n"
"s_add_u32 m0, s64, 0\n"
"s_nop 0\n"
"buffer_load_dwordx4 v161, s[36:39], s56 offen offset:0 lds\n"
"buffer_load_dwordx4 v162, s[36:39], s56 offen offset:1024 lds\n"
"buffer_load_dwordx4 v163, s[36:39], s56 offen offset:2048 lds\n"
"buffer_load_dwordx4 v164, s[36:39], s56 offen offset:3072 lds\n"
"s_add_u32 s56, s56, 16384\n"
"s_add_u32 m0, s64, 16384\n"
"s_nop 0\n"
"buffer_load_dwordx4 v161, s[36:39], s56 offen offset:0 lds\n"
"buffer_load_dwordx4 v162, s[36:39], s56 offen offset:1024 lds\n"
"buffer_load_dwordx4 v163, s[36:39], s56 offen offset:2048 lds\n"
"buffer_load_dwordx4 v164, s[36:39], s56 offen offset:3072 lds\n"
"s_add_u32 s56, s56, 16384\n"
"s_add_u32 m0, s64, 32768\n"
"s_nop 0\n"
"buffer_load_dwordx4 v161, s[36:39], s56 offen offset:0 lds\n"
"buffer_load_dwordx4 v162, s[36:39], s56 offen offset:1024 lds\n"
"buffer_load_dwordx4 v163, s[36:39], s56 offen offset:2048 lds\n"
"buffer_load_dwordx4 v164, s[36:39], s56 offen offset:3072 lds\n"
"s_add_u32 s56, s56, 16384\n"
"s_add_u32 m0, s65, 0\n"
"s_nop 0\n"
"buffer_load_dwordx4 v165, s[40:43], s57 offen offset:0 lds\n"
"buffer_load_dwordx4 v166, s[40:43], s57 offen offset:1024 lds\n"
"buffer_load_dwordx4 v165, s[40:43], s57 offen offset:2048 lds\n"
"buffer_load_dwordx4 v166, s[40:43], s57 offen offset:3072 lds\n"
"s_add_u32 s57, s57, 16384\n"
"s_add_u32 m0, s65, 16384\n"
"s_nop 0\n"
"buffer_load_dwordx4 v165, s[40:43], s57 offen offset:0 lds\n"
"buffer_load_dwordx4 v166, s[40:43], s57 offen offset:1024 lds\n"
"buffer_load_dwordx4 v165, s[40:43], s57 offen offset:2048 lds\n"
"buffer_load_dwordx4 v166, s[40:43], s57 offen offset:3072 lds\n"
"s_add_u32 s57, s57, 16384\n"
"s_and_b32 s69, s5, 3\n"
"s_lshl_b32 s69, s69, 1\n"
"s_lshr_b32 s70, s5, 2\n"
"s_add_u32 s69, s69, s70\n"
"s_sub_u32 s70, s69, 8\n"
"s_cselect_b32 s69, s69, s70\n"
"s_mul_i32 s58, s69, 10240\n"
"s_mul_i32 s60, s69, 221184\n"
"s_waitcnt vmcnt(0)\n"
"s_barrier\n"
"ds_read_b128 a[192:195], v149 offset:0\n"
"ds_read_b128 a[196:199], v150 offset:0\n"
"ds_read_b128 a[200:203], v151 offset:0\n"
"ds_read_b128 a[204:207], v152 offset:0\n"
"s_waitcnt lgkmcnt(0)\n"
"v_mfma_f32_16x16x32_bf16 v[64:67], a[192:195], v[176:179], v[112:115]\n"
"ds_read_b128 a[128:131], v149 offset:4096\n"
"v_mov_b32 v128, 0\n"
"v_mfma_f32_16x16x32_bf16 v[64:67], a[196:199], v[180:183], v[64:67]\n"
"ds_read_b128 a[132:135], v150 offset:4096\n"
"v_mov_b32 v129, 0\n"
"v_mfma_f32_16x16x32_bf16 v[64:67], a[200:203], v[184:187], v[64:67]\n"
"ds_read_b128 a[136:139], v151 offset:4096\n"
"v_mov_b32 v130, 0\n"
"v_mfma_f32_16x16x32_bf16 v[64:67], a[204:207], v[188:191], v[64:67]\n"
"ds_read_b128 a[140:143], v152 offset:4096\n"
"v_mov_b32 v131, 0\n"
"v_mfma_f32_16x16x32_bf16 v[68:71], a[192:195], v[192:195], v[116:119]\n"
"v_mov_b32 v132, 0\n"
"v_mfma_f32_16x16x32_bf16 v[68:71], a[196:199], v[196:199], v[68:71]\n"
"v_mov_b32 v133, 0\n"
"v_mfma_f32_16x16x32_bf16 v[68:71], a[200:203], v[200:203], v[68:71]\n"
"v_mov_b32 v134, 0\n"
"v_mfma_f32_16x16x32_bf16 v[68:71], a[204:207], v[204:207], v[68:71]\n"
"v_mov_b32 v135, 0\n"
"v_mfma_f32_16x16x32_bf16 v[72:75], a[192:195], v[208:211], v[120:123]\n"
"v_mov_b32 v136, 0\n"
"v_mfma_f32_16x16x32_bf16 v[72:75], a[196:199], v[212:215], v[72:75]\n"
"v_mov_b32 v137, 0\n"
"v_mfma_f32_16x16x32_bf16 v[72:75], a[200:203], v[216:219], v[72:75]\n"
"v_mov_b32 v138, 0\n"
"v_mfma_f32_16x16x32_bf16 v[72:75], a[204:207], v[220:223], v[72:75]\n"
"v_mov_b32 v139, 0\n"
"v_mfma_f32_16x16x32_bf16 v[76:79], a[192:195], v[224:227], v[124:127]\n"
"v_mov_b32 v140, 0\n"
"v_mfma_f32_16x16x32_bf16 v[76:79], a[196:199], v[228:231], v[76:79]\n"
"v_mov_b32 v141, 0\n"
"v_mfma_f32_16x16x32_bf16 v[76:79], a[200:203], v[232:235], v[76:79]\n"
"v_mov_b32 v142, 0\n"
"v_mfma_f32_16x16x32_bf16 v[76:79], a[204:207], v[236:239], v[76:79]\n"
"v_mov_b32 v143, 0\n"
"s_waitcnt lgkmcnt(0)\n"
"s_nop 7\n"
"s_branch L_att_%=\n"
"L_slow_pollH_%=:\n"
"s_mov_b32 vcc_hi, 0x20000\n"
"s_mov_b32 s95, vcc_hi\n"
"L_spin_pollH_%=:\n"
"s_sleep 1\n"
"global_load_dwordx2 v[14:15], v254, s[6:7] sc1\n"
"s_waitcnt vmcnt(0)\n"
"v_cmp_ne_u64 vcc, s[34:35], v[14:15]\n"
"s_sub_u32 s95, s95, 1\n"
"s_cbranch_scc1 L_back_pollH_%=\n"
"s_cmp_eq_u64 vcc, 0\n"
"s_cbranch_scc0 L_spin_pollH_%=\n"
"s_branch L_back_pollH_%=\n"
"L_slow_pollO_%=:\n"
"s_mov_b32 vcc_hi, 0x20000\n"
"s_mov_b32 s95, vcc_hi\n"
"L_spin_pollO_%=:\n"
"s_sleep 1\n"
"global_load_dwordx2 v[2:3], v4, s[6:7] sc1\n"
"s_waitcnt vmcnt(0)\n"
"v_cmp_ne_u64 vcc, s[34:35], v[2:3]\n"
"s_sub_u32 s95, s95, 1\n"
"s_cbranch_scc1 L_back_pollO_%=\n"
"s_cmp_eq_u64 vcc, 0\n"
"s_cbranch_scc0 L_spin_pollO_%=\n"
"s_branch L_back_pollO_%=\n"
"L_slow_pollU_%=:\n"
"s_mov_b32 vcc_hi, 0x20000\n"
"s_mov_b32 s95, vcc_hi\n"
"L_spin_pollU_%=:\n"
"s_sleep 1\n"
"global_load_dwordx2 v[6:7], v4, s[6:7] sc1\n"
"s_waitcnt vmcnt(0)\n"
"v_cmp_ne_u64 vcc, s[34:35], v[6:7]\n"
"s_sub_u32 s95, s95, 1\n"
"s_cbranch_scc1 L_back_pollU_%=\n"
"s_cmp_eq_u64 vcc, 0\n"
"s_cbranch_scc0 L_spin_pollU_%=\n"
"s_branch L_back_pollU_%=\n"
"L_slow_pollS_%=:\n"
"s_mov_b32 vcc_hi, 0x20000\n"
"s_mov_b32 s95, vcc_hi\n"
"L_spin_pollS_%=:\n"
"s_sleep 1\n"
"global_load_dwordx2 v[6:7], v9, s[6:7] sc1\n"
"s_waitcnt vmcnt(0)\n"
"v_cmp_ne_u64 vcc, s[34:35], v[6:7]\n"
"s_sub_u32 s95, s95, 1\n"
"s_cbranch_scc1 L_back_pollS_%=\n"
"s_cmp_eq_u64 vcc, 0\n"
"s_cbranch_scc0 L_spin_pollS_%=\n"
"s_branch L_back_pollS_%=\n"
"L_slow_gr_%=:\n"
"s_mov_b32 s95, 0x20000\n"
"L_spin_gr_%=:\n"
"s_sleep 1\n"
"global_load_dwordx4 v[224:227], v214, s[28:29] offset:0 sc1\n"
"global_load_dwordx4 v[228:231], v214, s[28:29] offset:256 sc1\n"
"global_load_dwordx4 v[232:235], v214, s[28:29] offset:512 sc1\n"
"global_load_dwordx4 v[236:239], v214, s[28:29] offset:768 sc1\n"
"global_load_dwordx4 v[176:179], v221, s[28:29] offset:-2048 sc1\n"
"global_load_dwordx4 v[180:183], v221, s[28:29] offset:-1024 sc1\n"
"global_load_dwordx4 v[184:187], v221, s[28:29] offset:0 sc1\n"
"global_load_dwordx4 v[188:191], v221, s[28:29] offset:1024 sc1\n"
"global_load_dwordx4 v[192:195], v221, s[28:29] offset:2048 sc1\n"
"s_waitcnt vmcnt(0)\n"
"v_xor_b32 v14, s34, v225\n"
"v_bitop3_b32 v14, v14, v227, s34 bitop3:0xf6\n"
"v_bitop3_b32 v14, v14, v229, s34 bitop3:0xf6\n"
"v_bitop3_b32 v14, v14, v231, s34 bitop3:0xf6\n"
"v_bitop3_b32 v14, v14, v233, s34 bitop3:0xf6\n"
"v_bitop3_b32 v14, v14, v235, s34 bitop3:0xf6\n"
"v_bitop3_b32 v14, v14, v237, s34 bitop3:0xf6\n"
"v_bitop3_b32 v14, v14, v239, s34 bitop3:0xf6\n"
"v_bitop3_b32 v14, v14, v177, s34 bitop3:0xf6\n"
"v_bitop3_b32 v14, v14, v179, s34 bitop3:0xf6\n"
"v_bitop3_b32 v14, v14, v181, s34 bitop3:0xf6\n"
"v_bitop3_b32 v14, v14, v183, s34 bitop3:0xf6\n"
"v_bitop3_b32 v14, v14, v185, s34 bitop3:0xf6\n"
"v_bitop3_b32 v14, v14, v187, s34 bitop3:0xf6\n"
"v_bitop3_b32 v14, v14, v189, s34 bitop3:0xf6\n"
"v_bitop3_b32 v14, v14, v191, s34 bitop3:0xf6\n"
"v_bitop3_b32 v14, v14, v193, s34 bitop3:0xf6\n"
"v_bitop3_b32 v14, v14, v195, s34 bitop3:0xf6\n"
"v_cmp_ne_u32 vcc, 0, v14\n"
"s_sub_u32 s95, s95, 1\n"
"s_cbranch_scc1 L_back_gr_%=\n"
"s_cmp_eq_u64 vcc, 0\n"
"s_cbranch_scc0 L_spin_gr_%=\n"
"s_branch L_back_gr_%=\n"
"L_last_%=:\n"
"L_end_%=:\n"
      :
      : "s"(__builtin_amdgcn_kernarg_segment_ptr()), "s"(blockIdx.x), "s"(w), "s"((unsigned)(size_t)lds)
      : "v0","v1","v2","v3","v4","v5","v6","v7","v8","v9","v10","v11","v12","v13","v14","v15","v16","v17","v18","v19","v20","v21","v22","v23","v24","v25","v26","v27","v28","v29","v30","v31","v32","v33","v34","v35","v36","v37","v38","v39","v40","v41","v42","v43","v44","v45","v46","v47","v48","v49","v50","v51","v52","v53","v54","v55","v56","v57","v58","v59","v60","v61","v62","v63","v64","v65","v66","v67","v68","v69","v70","v71","v72","v73","v74","v75","v76","v77","v78","v79","v80","v81","v82","v83","v84","v85","v86","v87","v88","v89","v90","v91","v92","v93","v94","v95","v96","v97","v98","v99","v100","v101","v102","v103","v104","v105","v106","v107","v108","v109","v110","v111","v112","v113","v114","v115","v116","v117","v118","v119","v120","v121","v122","v123","v124","v125","v126","v127","v128","v129","v130","v131","v132","v133","v134","v135","v136","v137","v138","v139","v140","v141","v142","v143","v144","v145","v146","v147","v148","v149","v150","v151","v152","v153","v154","v155","v156","v157","v158","v159","v160","v161","v162","v163","v164","v165","v166","v167","v168","v169","v170","v171","v172","v173","v174","v175","v176","v177","v178","v179","v180","v181","v182","v183","v184","v185","v186","v187","v188","v189","v190","v191","v192","v193","v194","v195","v196","v197","v198","v199","v200","v201","v202","v203","v204","v205","v206","v207","v208","v209","v210","v211","v212","v213","v214","v215","v216","v217","v218","v219","v220","v221","v222","v223","v224","v225","v226","v227","v228","v229","v230","v231","v232","v233","v234","v235","v236","v237","v238","v239","v240","v241","v242","v243","v244","v245","v246","v247","v248","v249","v250","v251","v252","v253","v254","v255","a0","a1","a2","a3","a4","a5","a6","a7","a8","a9","a10","a11","a12","a13","a14","a15","a16","a17","a18","a19","a20","a21","a22","a23","a24","a25","a26","a27","a28","a29","a30","a31","a32","a33","a34","a35","a36","a37","a38","a39","a40","a41","a42","a43","a44","a45","a46","a47","a48","a49","a50","a51","a52","a53","a54","a55","a56","a57","a58","a59","a60","a61","a62","a63","a64","a65","a66","a67","a68","a69","a70","a71","a72","a73","a74","a75","a76","a77","a78","a79","a80","a81","a82","a83","a84","a85","a86","a87","a88","a89","a90","a91","a92","a93","a94","a95","a96","a97","a98","a99","a100","a101","a102","a103","a104","a105","a106","a107","a108","a109","a110","a111","a112","a113","a114","a115","a116","a117","a118","a119","a120","a121","a122","a123","a124","a125","a126","a127","a128","a129","a130","a131","a132","a133","a134","a135","a136","a137","a138","a139","a140","a141","a142","a143","a144","a145","a146","a147","a148","a149","a150","a151","a152","a153","a154","a155","a156","a157","a158","a159","a160","a161","a162","a163","a164","a165","a166","a167","a168","a169","a170","a171","a172","a173","a174","a175","a176","a177","a178","a179","a180","a181","a182","a183","a184","a185","a186","a187","a188","a189","a190","a191","a192","a193","a194","a195","a196","a197","a198","a199","a200","a201","a202","a203","a204","a205","a206","a207","a208","a209","a210","a211","a212","a213","a214","a215","a216","a217","a218","a219","a220","a221","a222","a223","a224","a225","a226","a227","a228","a229","a230","a231","a232","a233","a234","a235","a236","a237","a238","a239","a240","a241","a242","a243","a244","a245","a246","a247","a248","a249","a250","a251","a252","a253","a254","a255","s5","s6","s7","s8","s9","s10","s11","s12","s13","s14","s15","s16","s17","s18","s19","s20","s21","s22","s23","s24","s25","s26","s27","s28","s29","s30","s31","s33","s34","s35","s36","s37","s38","s39","s40","s41","s42","s43","s44","s45","s46","s47","s48","s49","s50","s51","s52","s53","s54","s55","s56","s57","s58","s59","s60","s61","s62","s63","s64","s65","s66","s67","s68","s69","s70","s71","s72","s73","s74","s75","s76","s77","s78","s79","s80","s81","s82","s83","s84","s85","s86","s87","s88","s89","s90","s91","s92","s93","s94","s95","s96","s97","s98","s99","vcc","scc","memory");
}

extern "C" void kernel_launch(void* const* d_in, const int* in_sizes, int n_in, void* d_out, int out_size, void* d_ws, size_t ws_size,
                              hipStream_t stream) {
  Args a;
  a.x = d_in[0]; a.k = d_in[1]; a.v = d_in[2]; a.wq = d_in[3]; a.wo = d_in[4];
  a.w1 = d_in[6]; a.b1 = d_in[7]; a.w2 = d_in[8]; a.b2 = d_in[9];
  a.out = d_out; a.ws = d_ws;
  fused_block_kernel<<<256, 256, 0, stream>>>(a);
}
```
